```python
import torch

def _softmax(x, axis=-1):
    return torch.softmax(x, dim=axis)
def _gelu(x):
    return torch.nn.functional.gelu(x, approximate="tanh")
def _silu(x):
    return torch.nn.functional.silu(x)
def _sigmoid(x):
    return torch.sigmoid(x)
def _layernorm(x, axis=-1, eps=1e-5):
    m = x.mean(dim=axis, keepdim=True)
    v = x.var(dim=axis, keepdim=True, unbiased=False)
    return (x - m) / torch.sqrt(v + eps)
def _scatter_add(tgt, idx, src, axis=0):
    return tgt.clone().index_add_(axis, idx, src)
def _segment_sum(src, seg, n):
    out = torch.zeros((n,) + src.shape[1:], dtype=src.dtype)
    return out.index_add_(0, seg, src)
def _pad(x, pw):
    flat = tuple(v for pair in reversed(pw) for v in pair)
    return torch.nn.functional.pad(x, flat)

kernel_name = 'concat_heads__gqa_block_cached__mha_block_cached__near_ridge'

def setup_inputs():

    _w = (lambda a: (lambda t: t.to(torch.bfloat16) if t.is_floating_point() else t)(torch.as_tensor(a)))
    def _A_setup():
        B, Sq, D = 8, 192, 640
        Hq, Hkv, Sk, Dh = 32, 32, 512, 128
        assert Hq % Hkv == 0, (Hq, Hkv)
        return {
            "x": _w((lambda *s: torch.randn(*s, dtype=torch.bfloat16) * 0.8)(B, Sq, D)),
            "k_cache": _w((lambda *s: torch.randn(*s, dtype=torch.bfloat16) * 0.8)(B, Hkv, Sk, Dh)),
            "v_cache": _w((lambda *s: torch.randn(*s, dtype=torch.bfloat16) * 0.05)(B, Hkv, Sk, Dh)),
            "wq": _w((lambda *s: torch.randn(*s, dtype=torch.bfloat16) * 0.8)(D, Hq * Dh) * (D ** -0.5)),
            "wo": _w((lambda *s: torch.randn(*s, dtype=torch.bfloat16) * 0.05)(Hq * Dh, D) * ((Hq * Dh) ** -0.5)),
        }
    _A_in = _A_setup()

    def _B_setup():
        B, Sq, D = 8, 192, 640
        H, Sk, Dh = 32, 512, 128
        return {
            "x": _w((lambda *s: torch.randn(*s, dtype=torch.bfloat16) * 0.8)(B, Sq, D)),
            "k_cache": _w((lambda *s: torch.randn(*s, dtype=torch.bfloat16) * 0.8)(B, H, Sk, Dh)),
            "v_cache": _w((lambda *s: torch.randn(*s, dtype=torch.bfloat16) * 0.05)(B, H, Sk, Dh)),
            "wq": _w((lambda *s: torch.randn(*s, dtype=torch.bfloat16) * 0.8)(D, H * Dh) * (D ** -0.5)),
            "wo": _w((lambda *s: torch.randn(*s, dtype=torch.bfloat16) * 0.05)(H * Dh, D) * ((H * Dh) ** -0.5)),
        }
    _B_in = _B_setup()

    return {
        "A_x": _A_in["x"],
        "A_k_cache": _A_in["k_cache"],
        "A_v_cache": _A_in["v_cache"],
        "A_wq": _A_in["wq"],
        "A_wo": _A_in["wo"],
        "B_x": _B_in["x"],
        "B_k_cache": _B_in["k_cache"],
        "B_v_cache": _B_in["v_cache"],
        "B_wq": _B_in["wq"],
        "B_wo": _B_in["wo"],
    }

def reference(A_x, A_k_cache, A_v_cache, A_wq, A_wo, B_x, B_k_cache, B_v_cache, B_wq, B_wo):
    def _A_ref(x, k_cache, v_cache, wq, wo):
        B, Sq, D = x.shape
        Hq, Dh = 32, 128
        Hkv = k_cache.shape[1]
        rep = Hq // Hkv
        q = (lambda x, *d: x.permute(*d))((x @ wq).reshape(B, Sq, Hq, Dh), 0, 2, 1, 3)
        k = (lambda x, n, axis: torch.repeat_interleave(x, n, dim=axis))(k_cache, rep, axis=1)
        v = (lambda x, n, axis: torch.repeat_interleave(x, n, dim=axis))(v_cache, rep, axis=1)
        scores = torch.einsum("bhqd,bhkd->bhqk", q, k) * (Dh ** -0.5)
        p = _softmax(scores, axis=-1)
        o = torch.einsum("bhqk,bhkd->bhqd", p, v)
        return (lambda x, *d: x.permute(*d))(o, 0, 2, 1, 3).reshape(B, Sq, Hq * Dh) @ wo

    def _B_ref(x, k_cache, v_cache, wq, wo):
        B, Sq, D = x.shape
        H, Dh = 32, 128
        q = (lambda x, *d: x.permute(*d))((x @ wq).reshape(B, Sq, H, Dh), 0, 2, 1, 3)
        scores = torch.einsum("bhqd,bhkd->bhqk", q, k_cache) * (Dh ** -0.5)
        p = _softmax(scores, axis=-1)
        o = torch.einsum("bhqk,bhkd->bhqd", p, v_cache)
        return (lambda x, *d: x.permute(*d))(o, 0, 2, 1, 3).reshape(B, Sq, H * Dh) @ wo

    a = _A_ref(A_x, A_k_cache, A_v_cache, A_wq, A_wo)
    b = _B_ref(B_x, B_k_cache, B_v_cache, B_wq, B_wo)
    return torch.concatenate([a, b], axis=-1)
```

```cpp
#include <hip/hip_runtime.h>
#include <hip/hip_bf16.h>

typedef short    s16x4  __attribute__((ext_vector_type(4)));
typedef short    s16x8  __attribute__((ext_vector_type(8)));
typedef __bf16   bf16x8 __attribute__((ext_vector_type(8)));
typedef __bf16   bf16x2 __attribute__((ext_vector_type(2)));
typedef float    f32x2  __attribute__((ext_vector_type(2)));
typedef float    f32x4  __attribute__((ext_vector_type(4)));
typedef unsigned u32x4  __attribute__((ext_vector_type(4)));
typedef unsigned u32x2  __attribute__((ext_vector_type(2)));
#define DI __device__ __forceinline__
#define LDS3 __attribute__((address_space(3)))

constexpr int NB = 8, SQ = 192, DM = 640, NH = 32, SK = 512, DH = 128;
constexpr unsigned OF_BYTES  = 2u * 8 * 2 * 32 * 4 * 6 * 1024;
constexpr unsigned WOF_BYTES = 2u * 8 * 32 * 4 * 5 * 1024;
constexpr float QSCALE = 0.08838834764831845f * 1.4426950408889634f;
constexpr float THR = 8.0f;
constexpr int LDS1 = 81920;

DI unsigned rfl(unsigned x) { return __builtin_amdgcn_readfirstlane(x); }
DI unsigned cvtpk(float lo, float hi) { f32x2 v = {lo, hi}; bf16x2 b = __builtin_convertvector(v, bf16x2); return __builtin_bit_cast(unsigned, b); }
DI f32x4 mfma(u32x4 a, u32x4 b, f32x4 c) {
  return __builtin_amdgcn_mfma_f32_16x16x32_bf16(__builtin_bit_cast(bf16x8, a), __builtin_bit_cast(bf16x8, b), c, 0, 0, 0);
}
DI float max3f(float a, float b, float c) { float r; asm("v_max3_f32 %0, %1, %2, %3" : "=v"(r) : "v"(a), "v"(b), "v"(c)); return r; }
DI float fadd_s(float a, float b) { float r; asm("v_add_f32_e32 %0, %1, %2" : "=v"(r) : "v"(a), "v"(b)); return r; }
DI u32x4 lds_b128(unsigned addr) { return *(const LDS3 u32x4*)addr; }
DI u32x2 lds_tr(unsigned addr) { return __builtin_bit_cast(u32x2, __builtin_amdgcn_ds_read_tr16_b64_v4i16((LDS3 s16x4*)addr)); }
typedef __amdgpu_buffer_rsrc_t rsrc_t;
DI rsrc_t mkrsrc(const void* p, unsigned bytes) { return __builtin_amdgcn_make_buffer_rsrc((void*)p, (short)0, (int)bytes, 0x00020000); }
DI void dma(rsrc_t r, unsigned voff, unsigned soff, unsigned m0v) {
  asm volatile("s_mov_b32 m0, %0\n\ts_nop 0\n\tbuffer_load_dwordx4 %1, %2, %3 offen lds" :: "s"(m0v), "v"(voff), "s"(r), "s"(soff) : "memory");
}
DI void dma_nt(rsrc_t r, unsigned voff, unsigned soff, unsigned m0v) {
  asm volatile("s_mov_b32 m0, %0\n\ts_nop 0\n\tbuffer_load_dwordx4 %1, %2, %3 offen nt lds" :: "s"(m0v), "v"(voff), "s"(r), "s"(soff) : "memory");
}
template <int IMM> DI void bload(u32x4& d, rsrc_t r, unsigned voff, unsigned soff) {
  asm volatile("buffer_load_dwordx4 %0, %1, %2, %3 offen offset:%4" : "=v"(d) : "v"(voff), "s"(r), "s"(soff), "i"(IMM) : "memory");
}
#define WAITV(n) asm volatile("s_waitcnt vmcnt(" #n ")" ::: "memory")
#define BARRIER() asm volatile("s_waitcnt lgkmcnt(0)\n\ts_barrier" ::: "memory")

struct Ptrs { const void* x[2]; const void* k[2]; const void* v[2]; const void* wq[2]; const void* wo[2]; void* ws; void* out; };

__global__ __launch_bounds__(256, 2) void k1_attn(Ptrs P) {
  extern __shared__ __attribute__((aligned(16))) char smem[];
  const unsigned tid = threadIdx.x, l = tid & 63u, w = rfl(tid >> 6);
  const unsigned i16 = l & 15u, g = l >> 4;
  const unsigned bid = blockIdx.x, xcd = bid & 7u, j = bid >> 3;
  const unsigned br = xcd >> 2, b = j & 7u, h = 8u * (xcd & 3u) + (j >> 3);
  const unsigned lb = (unsigned)(size_t)smem;

  const char* xp  = (const char*)(br ? P.x[1] : P.x[0]) + (size_t)b * SQ * DM * 2;
  const char* kp  = (const char*)(br ? P.k[1] : P.k[0]) + (size_t)(b * NH + h) * SK * DH * 2;
  const char* vp  = (const char*)(br ? P.v[1] : P.v[0]) + (size_t)(b * NH + h) * SK * DH * 2;
  const char* wqp = (const char*)(br ? P.wq[1] : P.wq[0]);
  const char* wop = (const char*)(br ? P.wo[1] : P.wo[0]);
  const rsrc_t rX = mkrsrc(xp, SQ * DM * 2), rK = mkrsrc(kp, SK * DH * 2), rV = mkrsrc(vp, SK * DH * 2);
  const rsrc_t rW = mkrsrc(wqp, DM * NH * DH * 2), rWo = mkrsrc(wop, NH * DH * DM * 2);

  const unsigned vK0 = (l >> 3) * 256u + (((l & 7u) ^ (l >> 4)) * 16u), vK1 = vK0 ^ 64u;
  const unsigned vV  = ((l >> 2) & 7u) * 256u + (l >> 5) * 64u + ((((l & 3u) >> 1) ^ ((l >> 4) & 1u)) * 32u) + (l & 1u) * 16u;
  const unsigned vW0 = ((l >> 2) & 7u) * 8192u + (l >> 5) * 64u + (l & 3u) * 16u, vW1 = vW0 ^ 32u;
  const unsigned vX  = (48u * w + i16) * 1280u + g * 16u;
  const unsigned m0w = lb + w * 4096u;

  auto issueW = [&](int t) {
    const unsigned slot = 2u + (unsigned)((t + 2) % 3), so = (64u * t + 16u * w) * 8192u + h * 256u;
    dma(rW, vW0, so, m0w + slot * 16384u);
    dma(rW, vW0, so + 128u, m0w + slot * 16384u + 1024u);
    dma(rW, vW1, so + 65536u, m0w + slot * 16384u + 2048u);
    dma(rW, vW1, so + 65536u + 128u, m0w + slot * 16384u + 3072u);
  };
  auto issueK = [&](unsigned it, unsigned slot) {
    const unsigned so = it * 16384u + w * 4096u, m = m0w + slot * 16384u;
    dma_nt(rK, vK0, so, m); dma_nt(rK, vK0, so + 128u, m + 1024u); dma_nt(rK, vK1, so + 2048u, m + 2048u); dma_nt(rK, vK1, so + 2048u + 128u, m + 3072u);
  };
  auto issueV = [&](unsigned it, unsigned slot) {
    const unsigned so = it * 16384u + w * 4096u, m = m0w + slot * 16384u;
    dma_nt(rV, vV, so, m); dma_nt(rV, vV, so + 128u, m + 1024u); dma_nt(rV, vV, so + 2048u, m + 2048u); dma_nt(rV, vV, so + 2048u + 128u, m + 3072u);
  };
  u32x4 xb[3][3][2];
  auto issueX = [&](int t) {
#pragma unroll
    for (int qt = 0; qt < 3; ++qt) { bload<0>(xb[t % 3][qt][0], rX, vX, 128u * t + 20480u * qt); bload<64>(xb[t % 3][qt][1], rX, vX, 128u * t + 20480u * qt); }
  };

  issueX(0); issueW(0); issueX(1); issueW(1); issueW(2);

  f32x4 qa[8][3];
#pragma unroll
  for (int tt = 0; tt < 8; ++tt)
#pragma unroll
    for (int qt = 0; qt < 3; ++qt) qa[tt][qt] = f32x4{0.f, 0.f, 0.f, 0.f};
  {
    const unsigned q4 = i16 >> 2, p = l & 3u;
    const unsigned wb0 = lb + g * 2048u + q4 * 64u + ((p ^ (2u * (g & 1u))) * 16u) + (p >> 1) * 8u, wb1 = wb0 ^ 8u;
#pragma unroll
    for (int t = 0; t < 10; ++t) {
      if (t == 0 || t == 1 || t == 3) WAITV(14); else if (t == 2) WAITV(18); else if (t == 9) WAITV(4); else WAITV(10);
      asm volatile("" : "+v"(xb[t % 3][0][0]), "+v"(xb[t % 3][0][1]), "+v"(xb[t % 3][1][0]), "+v"(xb[t % 3][1][1]), "+v"(xb[t % 3][2][0]), "+v"(xb[t % 3][2][1]));
      BARRIER();
      if (t + 2 <= 9) issueX(t + 2);
      if (t >= 1 && t <= 7) issueW(t + 2);
      if (t == 0) issueK(0, 0);
      if (t == 1) issueV(0, 1);
      if (t == 8) issueK(1, 2);
      if (t == 9) issueV(1, 3);
      const unsigned sl = (2u + (unsigned)((t + 2) % 3)) * 16384u;
#pragma unroll
      for (int ks = 0; ks < 2; ++ks)
#pragma unroll
        for (int tt = 0; tt < 8; ++tt) {
          const unsigned base = ((tt & 1) ? wb1 : wb0) + sl + ks * 8192u + (tt >> 1) * 512u;
          const u32x2 lo = lds_tr(base), hi = lds_tr(base + 256u);
          const u32x4 a = {lo[0], lo[1], hi[0], hi[1]};
#pragma unroll
          for (int qt = 0; qt < 3; ++qt) qa[tt][qt] = mfma(a, xb[t % 3][qt][ks], qa[tt][qt]);
        }
    }
  }
  u32x4 qf[3][4];
  {
    const bool sw = g >= 2;
#pragma unroll
    for (int qt = 0; qt < 3; ++qt)
#pragma unroll
      for (int u = 0; u < 4; ++u) {
        const f32x4 e0 = qa[2 * u][qt] * QSCALE, e1 = qa[2 * u + 1][qt] * QSCALE;
        const unsigned a0 = cvtpk(e0[0], e0[1]), a1 = cvtpk(e0[2], e0[3]), b0 = cvtpk(e1[0], e1[1]), b1 = cvtpk(e1[2], e1[3]);
        qf[qt][u] = u32x4{sw ? b0 : a0, sw ? b1 : a1, sw ? a0 : b0, sw ? a1 : b1};
      }
  }

  f32x4 o[8][3];
#pragma unroll
  for (int dt = 0; dt < 8; ++dt)
#pragma unroll
    for (int qt = 0; qt < 3; ++qt) o[dt][qt] = f32x4{0.f, 0.f, 0.f, 0.f};
  f32x4 negm[3] = {f32x4{0.f, 0.f, 0.f, 0.f}, f32x4{0.f, 0.f, 0.f, 0.f}, f32x4{0.f, 0.f, 0.f, 0.f}};
  float lsum[3] = {0.f, 0.f, 0.f};
  const unsigned kb0 = lb + (i16 >> 3) * 2048u + (i16 & 7u) * 128u + ((g ^ (i16 >> 1)) * 16u);
  const unsigned vb0 = lb + (g >> 1) * 2048u + (4u * (g & 1u) + (i16 >> 2)) * 64u + (l & 3u) * 8u + (g & 1u) * 32u;
  const unsigned vWo = l;
  (void)vWo;
  unsigned slotK = 0;
#pragma unroll 1
  for (unsigned it = 0; it < 8; ++it) {
    const unsigned slotV = slotK == 4u ? 0u : slotK + 1u;
    const unsigned freeK = slotK == 0u ? 4u : slotK - 1u;
    if (it < 7) WAITV(12); else WAITV(9);
    BARRIER();
    if (it < 6) issueK(it + 2, freeK);
    f32x4 s[4][3];
    {
      const unsigned k0 = kb0 + slotK * 16384u, k1 = k0 ^ 64u;
#pragma unroll
      for (int kt = 0; kt < 4; ++kt)
#pragma unroll
        for (int ks = 0; ks < 4; ++ks) {
          const u32x4 a = lds_b128(((ks & 1) ? k1 : k0) + kt * 4096u + (ks >> 1) * 1024u);
#pragma unroll
          for (int qt = 0; qt < 3; ++qt) s[kt][qt] = mfma(a, qf[qt][ks], ks == 0 ? negm[qt] : s[kt][qt]);
        }
    }
    float rm[3];
#pragma unroll
    for (int qt = 0; qt < 3; ++qt) {
      float m = max3f(s[0][qt][0], s[0][qt][1], s[0][qt][2]);
      m = max3f(m, s[0][qt][3], s[1][qt][0]); m = max3f(m, s[1][qt][1], s[1][qt][2]); m = max3f(m, s[1][qt][3], s[2][qt][0]);
      m = max3f(m, s[2][qt][1], s[2][qt][2]); m = max3f(m, s[2][qt][3], s[3][qt][0]); m = max3f(m, s[3][qt][1], s[3][qt][2]);
      rm[qt] = max3f(m, s[3][qt][3], s[3][qt][3]);
    }
    const float mx = max3f(rm[0], rm[1], rm[2]);
    if (it == 0 || __builtin_amdgcn_ballot_w64(mx > THR) != 0ull) {
#pragma unroll
      for (int qt = 0; qt < 3; ++qt) {
        float r = rm[qt];
        r = fmaxf(r, __shfl_xor(r, 16)); r = fmaxf(r, __shfl_xor(r, 32));
        const float dl = it == 0 ? r : fmaxf(r, 0.f);
        const float f = it == 0 ? 1.f : __builtin_amdgcn_exp2f(-dl);
#pragma unroll
        for (int kt = 0; kt < 4; ++kt) s[kt][qt] -= dl;
        negm[qt] -= dl;
        lsum[qt] *= f;
#pragma unroll
        for (int dt = 0; dt < 8; ++dt) o[dt][qt] *= f;
      }
    }
    u32x4 pf[2][3];
#pragma unroll
    for (int qt = 0; qt < 3; ++qt) {
      float a0 = lsum[qt], a1 = 0.f;
#pragma unroll
      for (int kt = 0; kt < 4; ++kt) {
#pragma unroll
        for (int r = 0; r < 4; ++r) s[kt][qt][r] = __builtin_amdgcn_exp2f(s[kt][qt][r]);
        a0 = fadd_s(a0, s[kt][qt][0]); a1 = fadd_s(a1, s[kt][qt][1]); a0 = fadd_s(a0, s[kt][qt][2]); a1 = fadd_s(a1, s[kt][qt][3]);
      }
      lsum[qt] = fadd_s(a0, a1);
#pragma unroll
      for (int kb = 0; kb < 2; ++kb)
        pf[kb][qt] = u32x4{cvtpk(s[2 * kb][qt][0], s[2 * kb][qt][1]), cvtpk(s[2 * kb][qt][2], s[2 * kb][qt][3]),
                           cvtpk(s[2 * kb + 1][qt][0], s[2 * kb + 1][qt][1]), cvtpk(s[2 * kb + 1][qt][2], s[2 * kb + 1][qt][3])};
    }
    if (it < 6) WAITV(12); else if (it == 6) WAITV(8); else WAITV(5);
    BARRIER();
    if (it < 6) issueV(it + 2, slotK);
    if (it == 6) {
#pragma unroll
      for (int n = 0; n < 5; ++n) {
        const unsigned ci = (w * 5u + n) * 64u + l, row = ci / 10u, cc = ci - row * 10u;
        dma(rWo, row * 1280u + cc * 16u, h * 128u * 1280u + b * 160u, lb + 16384u + (w * 5u + n) * 1024u);
      }
    }
    {
      const unsigned v0 = vb0 + slotV * 16384u, v1 = v0 ^ 32u;
#pragma unroll
      for (int kb = 0; kb < 2; ++kb)
#pragma unroll
        for (int dt = 0; dt < 8; ++dt) {
          const unsigned base = ((dt & 1) ? v1 : v0) + kb * 8192u + (dt >> 1) * 512u;
          const u32x2 lo = lds_tr(base), hi = lds_tr(base + 4096u);
          const u32x4 a = {lo[0], lo[1], hi[0], hi[1]};
#pragma unroll
          for (int qt = 0; qt < 3; ++qt) o[dt][qt] = mfma(a, pf[kb][qt], o[dt][qt]);
        }
    }
    slotK = slotV == 4u ? 0u : slotV + 1u;
  }
  {
    char* of = (char*)P.ws + ((((size_t)(br * 8u + b) * 2u + (w >> 1)) * 32u + h) * 24u + 3u * (w & 1u)) * 1024u + l * 16u;
#pragma unroll
    for (int qt = 0; qt < 3; ++qt) {
      float ls = lsum[qt];
      ls += __shfl_xor(ls, 16); ls += __shfl_xor(ls, 32);
      const float inv = 1.0f / ls;
#pragma unroll
      for (int u = 0; u < 4; ++u) {
        const f32x4 e0 = o[2 * u][qt] * inv, e1 = o[2 * u + 1][qt] * inv;
        const u32x4 d = {cvtpk(e0[0], e0[1]), cvtpk(e0[2], e0[3]), cvtpk(e1[0], e1[1]), cvtpk(e1[2], e1[3])};
        *(u32x4*)(of + (u * 6 + qt) * 1024) = d;
      }
    }
  }
  WAITV(12);
  BARRIER();
  {
    char* wf = (char*)P.ws + OF_BYTES + ((((size_t)(br * 8u + b) * 32u + h) * 4u + w) * 5u) * 1024u + l * 16u;
    const unsigned base = lb + 16384u + (32u * w + 4u * g + (i16 >> 2)) * 160u + (l & 3u) * 8u;
#pragma unroll
    for (int nt = 0; nt < 5; ++nt) {
      const u32x2 lo = lds_tr(base + nt * 32u), hi = lds_tr(base + nt * 32u + 16u * 160u);
      *(u32x4*)(wf + nt * 1024) = u32x4{lo[0], lo[1], hi[0], hi[1]};
    }
  }
}

__global__ __launch_bounds__(256, 1) void k2_oproj(Ptrs P) {
  extern __shared__ __attribute__((aligned(16))) char smem[];
  const unsigned tid = threadIdx.x, l = tid & 63u, w = rfl(tid >> 6);
  const unsigned bid = blockIdx.x, xcd = bid & 7u, j = bid >> 3;
  const unsigned br = xcd >> 2, b = 2u * (xcd & 3u) + (j >> 4), rh = (j >> 3) & 1u, c = j & 7u;
  const u32x4* of = (const u32x4*)((const char*)P.ws + ((((size_t)(br * 8u + b) * 2u + rh) * 32u + 8u * w) * 24u) * 1024u) + l;
  const u32x4* wf = (const u32x4*)((const char*)P.ws + OF_BYTES + ((((size_t)(br * 8u + c) * 32u + 8u * w) * 4u) * 5u) * 1024u) + l;
  f32x4 acc[5][6];
#pragma unroll
  for (int nt = 0; nt < 5; ++nt)
#pragma unroll
    for (int qt = 0; qt < 6; ++qt) acc[nt][qt] = f32x4{0.f, 0.f, 0.f, 0.f};
  u32x4 A[3][5], B[3][6];
#pragma unroll
  for (int s = 0; s < 2; ++s) {
#pragma unroll
    for (int qt = 0; qt < 6; ++qt) B[s][qt] = of[(s * 6 + qt) * 64];
#pragma unroll
    for (int nt = 0; nt < 5; ++nt) A[s][nt] = wf[(s * 5 + nt) * 64];
  }
#pragma unroll 1
  for (int k0 = 0; k0 < 30; k0 += 3) {
#pragma unroll
    for (int s = 0; s < 3; ++s) {
      const int kn = k0 + s + 2;
#pragma unroll
      for (int qt = 0; qt < 6; ++qt) B[(s + 2) % 3][qt] = of[(kn * 6 + qt) * 64];
#pragma unroll
      for (int nt = 0; nt < 5; ++nt) A[(s + 2) % 3][nt] = wf[(kn * 5 + nt) * 64];
#pragma unroll
      for (int nt = 0; nt < 5; ++nt)
#pragma unroll
        for (int qt = 0; qt < 6; ++qt) acc[nt][qt] = mfma(A[s][nt], B[s][qt], acc[nt][qt]);
    }
  }
#pragma unroll
  for (int s = 0; s < 2; ++s)
#pragma unroll
    for (int nt = 0; nt < 5; ++nt)
#pragma unroll
      for (int qt = 0; qt < 6; ++qt) acc[nt][qt] = mfma(A[s][nt], B[s][qt], acc[nt][qt]);
  f32x4* red = (f32x4*)smem;
#pragma unroll
  for (int nt = 0; nt < 5; ++nt)
#pragma unroll
    for (int qt = 0; qt < 6; ++qt) red[(w * 30 + nt * 6 + qt) * 64 + l] = acc[nt][qt];
  __syncthreads();
  const unsigned i16 = l & 15u, g = l >> 4;
  for (unsigned t = w; t < 30; t += 4) {
    const f32x4 v = red[t * 64 + l] + red[(30 + t) * 64 + l] + red[(60 + t) * 64 + l] + red[(90 + t) * 64 + l];
    const unsigned nt = t / 6u, qt = t - nt * 6u;
    const unsigned row = b * 192u + rh * 96u + qt * 16u + i16, col = br * 640u + c * 80u + nt * 16u + 4u * g;
    *(u32x2*)((char*)P.out + ((size_t)row * 1280u + col) * 2u) = u32x2{cvtpk(v[0], v[1]), cvtpk(v[2], v[3])};
  }
}

extern "C" void kernel_launch(void* const* d_in, const int* in_sizes, int n_in, void* d_out, int out_size, void* d_ws, size_t ws_size, hipStream_t stream) {
  Ptrs P;
  for (int br = 0; br < 2; ++br) { P.x[br] = d_in[5 * br + 0]; P.k[br] = d_in[5 * br + 1]; P.v[br] = d_in[5 * br + 2]; P.wq[br] = d_in[5 * br + 3]; P.wo[br] = d_in[5 * br + 4]; }
  P.ws = d_ws; P.out = d_out;
  (void)hipFuncSetAttribute((const void*)k1_attn, hipFuncAttributeMaxDynamicSharedMemorySize, LDS1);
  (void)hipFuncSetAttribute((const void*)k2_oproj, hipFuncAttributeMaxDynamicSharedMemorySize, 122880);
  hipLaunchKernelGGL(k1_attn, dim3(512), dim3(256), LDS1, stream, P);
  hipLaunchKernelGGL(k2_oproj, dim3(256), dim3(256), 122880, stream, P);
}
```

```cpp
#include <hip/hip_runtime.h>

constexpr int LDS1 = 163840;
struct Ptrs { const void* x[2]; const void* k[2]; const void* v[2]; const void* wq[2]; const void* wo[2]; void* ws; void* out; };

__global__ __launch_bounds__(512, 2) void kf_attn(Ptrs P) {
  extern __shared__ __attribute__((aligned(16))) char smem[];
  const unsigned w = __builtin_amdgcn_readfirstlane(threadIdx.x >> 6), bid = blockIdx.x, lb = (unsigned)(size_t)smem;
  const void* ka = (const void*)__builtin_amdgcn_kernarg_segment_ptr();
  asm volatile(
    "s_mov_b32 s28, %1\n\t"
    "s_mov_b32 s99, %2\n\t"
    "s_mov_b32 s27, %3\n\t"
    "s_bfe_u32 s29, s28, 0x10002\n\t"
    "s_lshl_b32 s84, s29, 3\n\t"
    "s_add_u32 s85, s84, 0\n\t"
    "s_load_dwordx2 s[12:13], %0, s85\n\t"
    "s_add_u32 s85, s84, 48\n\t"
    "s_load_dwordx2 s[18:19], %0, s85\n\t"
    "s_add_u32 s85, s84, 16\n\t"
    "s_load_dwordx2 s[14:15], %0, s85\n\t"
    "s_add_u32 s85, s84, 32\n\t"
    "s_load_dwordx2 s[16:17], %0, s85\n\t"
    "s_add_u32 s85, s84, 64\n\t"
    "s_load_dwordx2 s[20:21], %0, s85\n\t"
    "s_load_dwordx2 s[22:23], %0, 80\n\t"
    "s_load_dwordx2 s[24:25], %0, 88\n\t"
    "s_and_b32 s26, s99, 3\n\t"
    "s_lshr_b32 s75, s99, 2\n\t"
    "s_and_b32 s34, s28, 3\n\t"
    "s_lshr_b32 s35, s28, 3\n\t"
    "s_and_b32 s30, s35, 7\n\t"
    "s_lshr_b32 s84, s35, 3\n\t"
    "s_mul_i32 s31, s34, 8\n\t"
    "s_mul_i32 s87, s84, 1\n\t"
    "s_add_u32 s31, s31, s87\n\t"
    "s_mul_i32 s96, s34, 4\n\t"
    "s_mul_i32 s87, s84, 1\n\t"
    "s_add_u32 s96, s96, s87\n\t"
    "s_mul_i32 s91, s29, 1024\n\t"
    "s_mul_i32 s87, s30, 128\n\t"
    "s_add_u32 s91, s91, s87\n\t"
    "s_mul_i32 s87, s31, 4\n\t"
    "s_add_u32 s91, s91, s87\n\t"
    "v_mbcnt_lo_u32_b32 v253, -1, 0\n\t"
    "v_mbcnt_hi_u32_b32 v253, -1, v253\n\t"
    "v_cmp_lt_u32 s[82:83], 31, v253\n\t"
    "s_cmp_lg_u32 s75, 0\n\t"
    "s_cbranch_scc1 L_help_%=\n\t"
    "s_getpc_b64 s[60:61]\n\t"
    "s_and_b32 s61, s61, 0xffff\n\t"
    "s_mov_b32 s62, L_hot_end_%= - L_hot_beg_%=\n\t"
    "L_hot_beg_%=:\n\t"
    "s_mov_b32 s63, 0x00020000\n\t"
    "s_mul_i32 s85, s35, 2048\n\t"
    "s_mul_i32 s87, s26, 512\n\t"
    "s_add_u32 s85, s85, s87\n\t"
    "v_lshlrev_b32 v254, 7, v253\n\t"
    "v_cmp_gt_u32 vcc, 4, v253\n\t"
    "s_nop 1\n\t"
    "v_cndmask_b32 v254, -1, v254, vcc\n\t"
    "buffer_load_dword v244, v254, s[60:63], s85 offen\n\t"
    "s_branch L_comp_%=\n\t"
    "L_help_%=:\n\t"
    "v_and_b32 v254, 7, v253\n\t"
    "v_lshrrev_b32 v255, 4, v253\n\t"
    "v_xor_b32 v254, v254, v255\n\t"
    "v_lshlrev_b32 v254, 4, v254\n\t"
    "v_lshrrev_b32 v255, 3, v253\n\t"
    "v_mul_u32_u24 v200, 1280, v255\n\t"
    "v_add_u32 v200, v200, v254\n\t"
    "v_xor_b32 v201, 64, v200\n\t"
    "v_lshl_add_u32 v204, v255, 8, v254\n\t"
    "v_bfe_u32 v255, v253, 2, 3\n\t"
    "v_lshrrev_b32 v254, 5, v253\n\t"
    "v_lshlrev_b32 v254, 6, v254\n\t"
    "v_and_b32 v203, 3, v253\n\t"
    "v_lshl_add_u32 v254, v203, 4, v254\n\t"
    "v_lshl_add_u32 v202, v255, 13, v254\n\t"
    "v_xor_b32 v203, 32, v202\n\t"
    "s_mul_i32 s72, s26, 6144\n\t"
    "s_add_u32 s72, s72, 114688\n\t"
    "s_add_u32 s72, s72, s27\n\t"
    "s_mul_i32 s73, s26, 4096\n\t"
    "s_add_u32 s73, s73, 65536\n\t"
    "s_add_u32 s73, s73, s27\n\t"
    "s_mul_i32 s85, s30, 245760\n\t"
    "s_mul_i32 s87, s26, 61440\n\t"
    "s_add_u32 s85, s85, s87\n\t"
    "s_mul_i32 s86, s31, 256\n\t"
    "s_mul_i32 s87, s26, 131072\n\t"
    "s_add_u32 s86, s86, s87\n\t"
    "s_sub_u32 s97, 5242880, s86\n\t"
    "s_lshr_b32 s84, s35, 3\n\t"
    "s_mul_i32 s96, s30, 1\n\t"
    "s_mul_i32 s87, s84, 3\n\t"
    "s_add_u32 s96, s96, s87\n\t"
    "s_mul_i32 s84, s84, 5\n\t"
    "s_lshr_b32 s84, s84, 1\n\t"
    "s_mul_i32 s96, s30, 5\n\t"
    "s_lshr_b32 s96, s96, 2\n\t"
    "s_add_u32 s96, s96, s84\n\t"
    "s_mul_i32 s84, s96, 26\n\t"
    "s_lshr_b32 s84, s84, 8\n\t"
    "s_mul_i32 s84, s84, 10\n\t"
    "s_sub_u32 s96, s96, s84\n\t"
    "s_waitcnt lgkmcnt(0)\n\t"
    "s_add_u32 s36, s12, s85\n\t"
    "s_addc_u32 s37, s13, 0\n\t"
    "s_and_b32 s37, s37, 0xffff\n\t"
    "s_mov_b32 s38, 61440\n\t"
    "s_mov_b32 s39, 0x00020000\n\t"
    "s_add_u32 s40, s18, s86\n\t"
    "s_addc_u32 s41, s19, 0\n\t"
    "s_and_b32 s41, s41, 0xffff\n\t"
    "s_mov_b32 s42, s97\n\t"
    "s_mov_b32 s43, 0x00020000\n\t"
    "s_add_u32 s98, s96, 0\n\t"
    "s_sub_u32 s87, s98, 10\n\t"
    "s_cselect_b32 s98, s98, s87\n\t"
    "s_mul_i32 s98, s98, 128\n\t"
    "s_add_u32 m0, s72, 0\n\t"
    "s_add_u32 s84, s98, 0\n\t"
    "buffer_load_dwordx4 v200, s[36:39], s84 offen offset:0 lds\n\t"
    "s_add_u32 m0, s72, 1024\n\t"
    "s_add_u32 s84, s98, 10240\n\t"
    "buffer_load_dwordx4 v201, s[36:39], s84 offen offset:0 lds\n\t"
    "s_add_u32 m0, s72, 2048\n\t"
    "s_add_u32 s84, s98, 20480\n\t"
    "buffer_load_dwordx4 v200, s[36:39], s84 offen offset:0 lds\n\t"
    "s_add_u32 m0, s72, 3072\n\t"
    "s_add_u32 s84, s98, 30720\n\t"
    "buffer_load_dwordx4 v201, s[36:39], s84 offen offset:0 lds\n\t"
    "s_add_u32 m0, s72, 4096\n\t"
    "s_add_u32 s84, s98, 40960\n\t"
    "buffer_load_dwordx4 v200, s[36:39], s84 offen offset:0 lds\n\t"
    "s_add_u32 m0, s72, 5120\n\t"
    "s_add_u32 s84, s98, 51200\n\t"
    "buffer_load_dwordx4 v201, s[36:39], s84 offen offset:0 lds\n\t"
    "s_add_u32 s98, s96, 0\n\t"
    "s_sub_u32 s87, s98, 10\n\t"
    "s_cselect_b32 s98, s98, s87\n\t"
    "s_mul_i32 s98, s98, 524288\n\t"
    "s_add_u32 m0, s73, 0\n\t"
    "s_add_u32 s85, s98, 1024\n\t"
    "buffer_load_dwordx4 v202, s[40:43], s85 offen offset:0 lds\n\t"
    "s_add_u32 m0, s73, 896\n\t"
    "s_nop 0\n\t"
    "buffer_load_dwordx4 v202, s[40:43], s85 offen offset:128 lds\n\t"
    "s_add_u32 m0, s73, 2048\n\t"
    "s_add_u32 s85, s98, 66560\n\t"
    "buffer_load_dwordx4 v203, s[40:43], s85 offen offset:0 lds\n\t"
    "s_add_u32 m0, s73, 2944\n\t"
    "s_nop 0\n\t"
    "buffer_load_dwordx4 v203, s[40:43], s85 offen offset:128 lds\n\t"
    "s_add_u32 s98, s96, 0\n\t"
    "s_sub_u32 s87, s98, 10\n\t"
    "s_cselect_b32 s98, s98, s87\n\t"
    "s_mul_i32 s98, s98, 524288\n\t"
    "s_add_u32 m0, s73, 16384\n\t"
    "s_add_u32 s85, s98, 0\n\t"
    "buffer_load_dwordx4 v202, s[40:43], s85 offen offset:0 lds\n\t"
    "s_add_u32 m0, s73, 17280\n\t"
    "s_nop 0\n\t"
    "buffer_load_dwordx4 v202, s[40:43], s85 offen offset:128 lds\n\t"
    "s_add_u32 m0, s73, 18432\n\t"
    "s_add_u32 s85, s98, 65536\n\t"
    "buffer_load_dwordx4 v203, s[40:43], s85 offen offset:0 lds\n\t"
    "s_add_u32 m0, s73, 19328\n\t"
    "s_nop 0\n\t"
    "buffer_load_dwordx4 v203, s[40:43], s85 offen offset:128 lds\n\t"
    "s_and_b32 s84, s26, 1\n\t"
    "s_lshl_b32 s84, s84, 6\n\t"
    "v_xor_b32 v204, s84, v204\n\t"
    "v_lshl_add_u32 v205, v255, 8, v254\n\t"
    "v_bfe_u32 v255, v253, 4, 1\n\t"
    "v_lshlrev_b32 v255, 5, v255\n\t"
    "v_xor_b32 v205, v205, v255\n\t"
    "v_lshlrev_b32 v253, 4, v253\n\t"
    "s_lshl_b32 s74, s26, 11\n\t"
    "s_add_u32 s74, s74, s27\n\t"
    "s_mul_i32 s78, s28, 37\n\t"
    "s_lshr_b32 s78, s78, 2\n\t"
    "s_and_b32 s78, s78, 15\n\t"
    "s_lshl_b32 s78, s78, 13\n\t"
    "v_add_u32 v11, s74, v253\n\t"
    "v_add_u32 v12, 49152, v11\n\t"
    "s_add_u32 s64, s22, 50331648\n\t"
    "s_addc_u32 s65, s23, 0\n\t"
    "s_and_b32 s65, s65, 0xffff\n\t"
    "s_mov_b32 s66, 8196\n\t"
    "s_mov_b32 s67, 0x00020000\n\t"
    "s_mov_b32 s84, 8192\n\t"
    "buffer_load_dword v0, off, s[64:67], s84 sc1\n\t"
    "s_mul_i32 s84, s30, 4194304\n\t"
    "s_mul_i32 s87, s31, 131072\n\t"
    "s_add_u32 s84, s84, s87\n\t"
    "s_mul_i32 s87, s26, 2048\n\t"
    "s_add_u32 s84, s84, s87\n\t"
    "s_lshl_b32 s85, s26, 11\n\t"
    "s_sub_u32 s85, 655360, s85\n\t"
    "s_add_u32 s44, s14, s84\n\t"
    "s_addc_u32 s45, s15, 0\n\t"
    "s_and_b32 s45, s45, 0xffff\n\t"
    "s_mov_b32 s46, s85\n\t"
    "s_mov_b32 s47, 0x00020000\n\t"
    "s_add_u32 s48, s16, s84\n\t"
    "s_addc_u32 s49, s17, 0\n\t"
    "s_and_b32 s49, s49, 0xffff\n\t"
    "s_mov_b32 s50, s85\n\t"
    "s_mov_b32 s51, 0x00020000\n\t"
    "s_waitcnt vmcnt(9)\n\t"
    "s_waitcnt vmcnt(5)\n\t"
    "s_barrier\n\t"
    "s_add_u32 s98, s96, 1\n\t"
    "s_sub_u32 s87, s98, 10\n\t"
    "s_cselect_b32 s98, s98, s87\n\t"
    "s_mul_i32 s98, s98, 128\n\t"
    "s_add_u32 m0, s72, 24576\n\t"
    "s_add_u32 s84, s98, 0\n\t"
    "buffer_load_dwordx4 v200, s[36:39], s84 offen offset:0 lds\n\t"
    "s_add_u32 m0, s72, 25600\n\t"
    "s_add_u32 s84, s98, 10240\n\t"
    "buffer_load_dwordx4 v201, s[36:39], s84 offen offset:0 lds\n\t"
    "s_add_u32 m0, s72, 26624\n\t"
    "s_add_u32 s84, s98, 20480\n\t"
    "buffer_load_dwordx4 v200, s[36:39], s84 offen offset:0 lds\n\t"
    "s_add_u32 m0, s72, 27648\n\t"
    "s_add_u32 s84, s98, 30720\n\t"
    "buffer_load_dwordx4 v201, s[36:39], s84 offen offset:0 lds\n\t"
    "s_add_u32 m0, s72, 28672\n\t"
    "s_add_u32 s84, s98, 40960\n\t"
    "buffer_load_dwordx4 v200, s[36:39], s84 offen offset:0 lds\n\t"
    "s_add_u32 m0, s72, 29696\n\t"
    "s_add_u32 s84, s98, 51200\n\t"
    "buffer_load_dwordx4 v201, s[36:39], s84 offen offset:0 lds\n\t"
    "s_add_u32 s98, s96, 1\n\t"
    "s_sub_u32 s87, s98, 10\n\t"
    "s_cselect_b32 s98, s98, s87\n\t"
    "s_mul_i32 s98, s98, 524288\n\t"
    "s_add_u32 m0, s73, 32768\n\t"
    "s_add_u32 s85, s98, 1024\n\t"
    "buffer_load_dwordx4 v202, s[40:43], s85 offen offset:0 lds\n\t"
    "s_add_u32 m0, s73, 33664\n\t"
    "s_nop 0\n\t"
    "buffer_load_dwordx4 v202, s[40:43], s85 offen offset:128 lds\n\t"
    "s_add_u32 m0, s73, 34816\n\t"
    "s_add_u32 s85, s98, 66560\n\t"
    "buffer_load_dwordx4 v203, s[40:43], s85 offen offset:0 lds\n\t"
    "s_add_u32 m0, s73, 35712\n\t"
    "s_nop 0\n\t"
    "buffer_load_dwordx4 v203, s[40:43], s85 offen offset:128 lds\n\t"
    "s_waitcnt vmcnt(10)\n\t"
    "v_readfirstlane_b32 s89, v0\n\t"
    "s_nop 3\n\t"
    "s_add_u32 s89, s89, 0x9e3779b9\n\t"
    "s_waitcnt vmcnt(4)\n\t"
    "s_barrier\n\t"
    "s_add_u32 s98, s96, 1\n\t"
    "s_sub_u32 s87, s98, 10\n\t"
    "s_cselect_b32 s98, s98, s87\n\t"
    "s_mul_i32 s98, s98, 524288\n\t"
    "s_add_u32 m0, s73, 0\n\t"
    "s_add_u32 s85, s98, 0\n\t"
    "buffer_load_dwordx4 v202, s[40:43], s85 offen offset:0 lds\n\t"
    "s_add_u32 m0, s73, 896\n\t"
    "s_nop 0\n\t"
    "buffer_load_dwordx4 v202, s[40:43], s85 offen offset:128 lds\n\t"
    "s_add_u32 m0, s73, 2048\n\t"
    "s_add_u32 s85, s98, 65536\n\t"
    "buffer_load_dwordx4 v203, s[40:43], s85 offen offset:0 lds\n\t"
    "s_add_u32 m0, s73, 2944\n\t"
    "s_nop 0\n\t"
    "buffer_load_dwordx4 v203, s[40:43], s85 offen offset:128 lds\n\t"
    "s_add_u32 s98, s96, 2\n\t"
    "s_sub_u32 s87, s98, 10\n\t"
    "s_cselect_b32 s98, s98, s87\n\t"
    "s_mul_i32 s98, s98, 128\n\t"
    "s_add_u32 m0, s72, 0\n\t"
    "s_add_u32 s84, s98, 0\n\t"
    "buffer_load_dwordx4 v200, s[36:39], s84 offen offset:0 lds\n\t"
    "s_add_u32 m0, s72, 1024\n\t"
    "s_add_u32 s84, s98, 10240\n\t"
    "buffer_load_dwordx4 v201, s[36:39], s84 offen offset:0 lds\n\t"
    "s_add_u32 m0, s72, 2048\n\t"
    "s_add_u32 s84, s98, 20480\n\t"
    "buffer_load_dwordx4 v200, s[36:39], s84 offen offset:0 lds\n\t"
    "s_add_u32 m0, s72, 3072\n\t"
    "s_add_u32 s84, s98, 30720\n\t"
    "buffer_load_dwordx4 v201, s[36:39], s84 offen offset:0 lds\n\t"
    "s_add_u32 m0, s72, 4096\n\t"
    "s_add_u32 s84, s98, 40960\n\t"
    "buffer_load_dwordx4 v200, s[36:39], s84 offen offset:0 lds\n\t"
    "s_add_u32 m0, s72, 5120\n\t"
    "s_add_u32 s84, s98, 51200\n\t"
    "buffer_load_dwordx4 v201, s[36:39], s84 offen offset:0 lds\n\t"
    "s_waitcnt vmcnt(10)\n\t"
    "s_barrier\n\t"
    "s_add_u32 s98, s96, 2\n\t"
    "s_sub_u32 s87, s98, 10\n\t"
    "s_cselect_b32 s98, s98, s87\n\t"
    "s_mul_i32 s98, s98, 524288\n\t"
    "s_add_u32 m0, s73, 16384\n\t"
    "s_add_u32 s85, s98, 1024\n\t"
    "buffer_load_dwordx4 v202, s[40:43], s85 offen offset:0 lds\n\t"
    "s_add_u32 m0, s73, 17280\n\t"
    "s_nop 0\n\t"
    "buffer_load_dwordx4 v202, s[40:43], s85 offen offset:128 lds\n\t"
    "s_add_u32 m0, s73, 18432\n\t"
    "s_add_u32 s85, s98, 66560\n\t"
    "buffer_load_dwordx4 v203, s[40:43], s85 offen offset:0 lds\n\t"
    "s_add_u32 m0, s73, 19328\n\t"
    "s_nop 0\n\t"
    "buffer_load_dwordx4 v203, s[40:43], s85 offen offset:128 lds\n\t"
    "s_waitcnt vmcnt(10)\n\t"
    "s_waitcnt vmcnt(4)\n\t"
    "s_barrier\n\t"
    "s_add_u32 s98, s96, 2\n\t"
    "s_sub_u32 s87, s98, 10\n\t"
    "s_cselect_b32 s98, s98, s87\n\t"
    "s_mul_i32 s98, s98, 524288\n\t"
    "s_add_u32 m0, s73, 32768\n\t"
    "s_add_u32 s85, s98, 0\n\t"
    "buffer_load_dwordx4 v202, s[40:43], s85 offen offset:0 lds\n\t"
    "s_add_u32 m0, s73, 33664\n\t"
    "s_nop 0\n\t"
    "buffer_load_dwordx4 v202, s[40:43], s85 offen offset:128 lds\n\t"
    "s_add_u32 m0, s73, 34816\n\t"
    "s_add_u32 s85, s98, 65536\n\t"
    "buffer_load_dwordx4 v203, s[40:43], s85 offen offset:0 lds\n\t"
    "s_add_u32 m0, s73, 35712\n\t"
    "s_nop 0\n\t"
    "buffer_load_dwordx4 v203, s[40:43], s85 offen offset:128 lds\n\t"
    "s_add_u32 s98, s96, 3\n\t"
    "s_sub_u32 s87, s98, 10\n\t"
    "s_cselect_b32 s98, s98, s87\n\t"
    "s_mul_i32 s98, s98, 128\n\t"
    "s_add_u32 m0, s72, 24576\n\t"
    "s_add_u32 s84, s98, 0\n\t"
    "buffer_load_dwordx4 v200, s[36:39], s84 offen offset:0 lds\n\t"
    "s_add_u32 m0, s72, 25600\n\t"
    "s_add_u32 s84, s98, 10240\n\t"
    "buffer_load_dwordx4 v201, s[36:39], s84 offen offset:0 lds\n\t"
    "s_add_u32 m0, s72, 26624\n\t"
    "s_add_u32 s84, s98, 20480\n\t"
    "buffer_load_dwordx4 v200, s[36:39], s84 offen offset:0 lds\n\t"
    "s_add_u32 m0, s72, 27648\n\t"
    "s_add_u32 s84, s98, 30720\n\t"
    "buffer_load_dwordx4 v201, s[36:39], s84 offen offset:0 lds\n\t"
    "s_add_u32 m0, s72, 28672\n\t"
    "s_add_u32 s84, s98, 40960\n\t"
    "buffer_load_dwordx4 v200, s[36:39], s84 offen offset:0 lds\n\t"
    "s_add_u32 m0, s72, 29696\n\t"
    "s_add_u32 s84, s98, 51200\n\t"
    "buffer_load_dwordx4 v201, s[36:39], s84 offen offset:0 lds\n\t"
    "s_waitcnt vmcnt(10)\n\t"
    "s_barrier\n\t"
    "s_add_u32 s98, s96, 3\n\t"
    "s_sub_u32 s87, s98, 10\n\t"
    "s_cselect_b32 s98, s98, s87\n\t"
    "s_mul_i32 s98, s98, 524288\n\t"
    "s_add_u32 m0, s73, 0\n\t"
    "s_add_u32 s85, s98, 1024\n\t"
    "buffer_load_dwordx4 v202, s[40:43], s85 offen offset:0 lds\n\t"
    "s_add_u32 m0, s73, 896\n\t"
    "s_nop 0\n\t"
    "buffer_load_dwordx4 v202, s[40:43], s85 offen offset:128 lds\n\t"
    "s_add_u32 m0, s73, 2048\n\t"
    "s_add_u32 s85, s98, 66560\n\t"
    "buffer_load_dwordx4 v203, s[40:43], s85 offen offset:0 lds\n\t"
    "s_add_u32 m0, s73, 2944\n\t"
    "s_nop 0\n\t"
    "buffer_load_dwordx4 v203, s[40:43], s85 offen offset:128 lds\n\t"
    "s_add_u32 m0, s74, 0\n\t"
    "s_add_u32 s86, s78, 0\n\t"
    "s_and_b32 s86, s86, 0x1ffff\n\t"
    "buffer_load_dwordx4 v204, s[44:47], s86 offen offset:0 nt lds\n\t"
    "s_add_u32 m0, s74, 896\n\t"
    "s_nop 0\n\t"
    "buffer_load_dwordx4 v204, s[44:47], s86 offen offset:128 nt lds\n\t"
    "s_add_u32 m0, s74, 8192\n\t"
    "s_add_u32 s86, s78, 8192\n\t"
    "s_and_b32 s86, s86, 0x1ffff\n\t"
    "buffer_load_dwordx4 v204, s[44:47], s86 offen offset:0 nt lds\n\t"
    "s_add_u32 m0, s74, 9088\n\t"
    "s_nop 0\n\t"
    "buffer_load_dwordx4 v204, s[44:47], s86 offen offset:128 nt lds\n\t"
    "s_add_u32 m0, s74, 16384\n\t"
    "s_add_u32 s86, s78, 0\n\t"
    "s_and_b32 s86, s86, 0x1ffff\n\t"
    "buffer_load_dwordx4 v205, s[48:51], s86 offen offset:0 nt lds\n\t"
    "s_add_u32 m0, s74, 17280\n\t"
    "s_nop 0\n\t"
    "buffer_load_dwordx4 v205, s[48:51], s86 offen offset:128 nt lds\n\t"
    "s_add_u32 m0, s74, 24576\n\t"
    "s_add_u32 s86, s78, 16384\n\t"
    "s_and_b32 s86, s86, 0x1ffff\n\t"
    "buffer_load_dwordx4 v204, s[44:47], s86 offen offset:0 nt lds\n\t"
    "s_add_u32 m0, s74, 25472\n\t"
    "s_nop 0\n\t"
    "buffer_load_dwordx4 v204, s[44:47], s86 offen offset:128 nt lds\n\t"
    "s_waitcnt vmcnt(18)\n\t"
    "s_waitcnt vmcnt(12)\n\t"
    "s_barrier\n\t"
    "s_add_u32 s98, s96, 3\n\t"
    "s_sub_u32 s87, s98, 10\n\t"
    "s_cselect_b32 s98, s98, s87\n\t"
    "s_mul_i32 s98, s98, 524288\n\t"
    "s_add_u32 m0, s73, 16384\n\t"
    "s_add_u32 s85, s98, 0\n\t"
    "buffer_load_dwordx4 v202, s[40:43], s85 offen offset:0 lds\n\t"
    "s_add_u32 m0, s73, 17280\n\t"
    "s_nop 0\n\t"
    "buffer_load_dwordx4 v202, s[40:43], s85 offen offset:128 lds\n\t"
    "s_add_u32 m0, s73, 18432\n\t"
    "s_add_u32 s85, s98, 65536\n\t"
    "buffer_load_dwordx4 v203, s[40:43], s85 offen offset:0 lds\n\t"
    "s_add_u32 m0, s73, 19328\n\t"
    "s_nop 0\n\t"
    "buffer_load_dwordx4 v203, s[40:43], s85 offen offset:128 lds\n\t"
    "s_add_u32 s98, s96, 4\n\t"
    "s_sub_u32 s87, s98, 10\n\t"
    "s_cselect_b32 s98, s98, s87\n\t"
    "s_mul_i32 s98, s98, 128\n\t"
    "s_add_u32 m0, s72, 0\n\t"
    "s_add_u32 s84, s98, 0\n\t"
    "buffer_load_dwordx4 v200, s[36:39], s84 offen offset:0 lds\n\t"
    "s_add_u32 m0, s72, 1024\n\t"
    "s_add_u32 s84, s98, 10240\n\t"
    "buffer_load_dwordx4 v201, s[36:39], s84 offen offset:0 lds\n\t"
    "s_add_u32 m0, s72, 2048\n\t"
    "s_add_u32 s84, s98, 20480\n\t"
    "buffer_load_dwordx4 v200, s[36:39], s84 offen offset:0 lds\n\t"
    "s_add_u32 m0, s72, 3072\n\t"
    "s_add_u32 s84, s98, 30720\n\t"
    "buffer_load_dwordx4 v201, s[36:39], s84 offen offset:0 lds\n\t"
    "s_add_u32 m0, s72, 4096\n\t"
    "s_add_u32 s84, s98, 40960\n\t"
    "buffer_load_dwordx4 v200, s[36:39], s84 offen offset:0 lds\n\t"
    "s_add_u32 m0, s72, 5120\n\t"
    "s_add_u32 s84, s98, 51200\n\t"
    "buffer_load_dwordx4 v201, s[36:39], s84 offen offset:0 lds\n\t"
    "s_waitcnt vmcnt(18)\n\t"
    "s_barrier\n\t"
    "s_add_u32 s98, s96, 4\n\t"
    "s_sub_u32 s87, s98, 10\n\t"
    "s_cselect_b32 s98, s98, s87\n\t"
    "s_mul_i32 s98, s98, 524288\n\t"
    "s_add_u32 m0, s73, 32768\n\t"
    "s_add_u32 s85, s98, 1024\n\t"
    "buffer_load_dwordx4 v202, s[40:43], s85 offen offset:0 lds\n\t"
    "s_add_u32 m0, s73, 33664\n\t"
    "s_nop 0\n\t"
    "buffer_load_dwordx4 v202, s[40:43], s85 offen offset:128 lds\n\t"
    "s_add_u32 m0, s73, 34816\n\t"
    "s_add_u32 s85, s98, 66560\n\t"
    "buffer_load_dwordx4 v203, s[40:43], s85 offen offset:0 lds\n\t"
    "s_add_u32 m0, s73, 35712\n\t"
    "s_nop 0\n\t"
    "buffer_load_dwordx4 v203, s[40:43], s85 offen offset:128 lds\n\t"
    "s_waitcnt vmcnt(10)\n\t"
    "s_waitcnt vmcnt(4)\n\t"
    "s_barrier\n\t"
    "s_add_u32 s98, s96, 4\n\t"
    "s_sub_u32 s87, s98, 10\n\t"
    "s_cselect_b32 s98, s98, s87\n\t"
    "s_mul_i32 s98, s98, 524288\n\t"
    "s_add_u32 m0, s73, 0\n\t"
    "s_add_u32 s85, s98, 0\n\t"
    "buffer_load_dwordx4 v202, s[40:43], s85 offen offset:0 lds\n\t"
    "s_add_u32 m0, s73, 896\n\t"
    "s_nop 0\n\t"
    "buffer_load_dwordx4 v202, s[40:43], s85 offen offset:128 lds\n\t"
    "s_add_u32 m0, s73, 2048\n\t"
    "s_add_u32 s85, s98, 65536\n\t"
    "buffer_load_dwordx4 v203, s[40:43], s85 offen offset:0 lds\n\t"
    "s_add_u32 m0, s73, 2944\n\t"
    "s_nop 0\n\t"
    "buffer_load_dwordx4 v203, s[40:43], s85 offen offset:128 lds\n\t"
    "s_add_u32 s98, s96, 5\n\t"
    "s_sub_u32 s87, s98, 10\n\t"
    "s_cselect_b32 s98, s98, s87\n\t"
    "s_mul_i32 s98, s98, 128\n\t"
    "s_add_u32 m0, s72, 24576\n\t"
    "s_add_u32 s84, s98, 0\n\t"
    "buffer_load_dwordx4 v200, s[36:39], s84 offen offset:0 lds\n\t"
    "s_add_u32 m0, s72, 25600\n\t"
    "s_add_u32 s84, s98, 10240\n\t"
    "buffer_load_dwordx4 v201, s[36:39], s84 offen offset:0 lds\n\t"
    "s_add_u32 m0, s72, 26624\n\t"
    "s_add_u32 s84, s98, 20480\n\t"
    "buffer_load_dwordx4 v200, s[36:39], s84 offen offset:0 lds\n\t"
    "s_add_u32 m0, s72, 27648\n\t"
    "s_add_u32 s84, s98, 30720\n\t"
    "buffer_load_dwordx4 v201, s[36:39], s84 offen offset:0 lds\n\t"
    "s_add_u32 m0, s72, 28672\n\t"
    "s_add_u32 s84, s98, 40960\n\t"
    "buffer_load_dwordx4 v200, s[36:39], s84 offen offset:0 lds\n\t"
    "s_add_u32 m0, s72, 29696\n\t"
    "s_add_u32 s84, s98, 51200\n\t"
    "buffer_load_dwordx4 v201, s[36:39], s84 offen offset:0 lds\n\t"
    "s_add_u32 m0, s74, 32768\n\t"
    "s_add_u32 s86, s78, 8192\n\t"
    "s_and_b32 s86, s86, 0x1ffff\n\t"
    "buffer_load_dwordx4 v205, s[48:51], s86 offen offset:0 nt lds\n\t"
    "s_add_u32 m0, s74, 33664\n\t"
    "s_nop 0\n\t"
    "buffer_load_dwordx4 v205, s[48:51], s86 offen offset:128 nt lds\n\t"
    "s_add_u32 m0, s74, 40960\n\t"
    "s_add_u32 s86, s78, 24576\n\t"
    "s_and_b32 s86, s86, 0x1ffff\n\t"
    "buffer_load_dwordx4 v204, s[44:47], s86 offen offset:0 nt lds\n\t"
    "s_add_u32 m0, s74, 41856\n\t"
    "s_nop 0\n\t"
    "buffer_load_dwordx4 v204, s[44:47], s86 offen offset:128 nt lds\n\t"
    "s_add_u32 m0, s74, 49152\n\t"
    "s_add_u32 s86, s78, 16384\n\t"
    "s_and_b32 s86, s86, 0x1ffff\n\t"
    "buffer_load_dwordx4 v205, s[48:51], s86 offen offset:0 nt lds\n\t"
    "s_add_u32 m0, s74, 50048\n\t"
    "s_nop 0\n\t"
    "buffer_load_dwordx4 v205, s[48:51], s86 offen offset:128 nt lds\n\t"
    "s_add_u32 m0, s74, 57344\n\t"
    "s_add_u32 s86, s78, 32768\n\t"
    "s_and_b32 s86, s86, 0x1ffff\n\t"
    "buffer_load_dwordx4 v204, s[44:47], s86 offen offset:0 nt lds\n\t"
    "s_add_u32 m0, s74, 58240\n\t"
    "s_nop 0\n\t"
    "buffer_load_dwordx4 v204, s[44:47], s86 offen offset:128 nt lds\n\t"
    "s_waitcnt vmcnt(18)\n\t"
    "s_barrier\n\t"
    "s_add_u32 s98, s96, 5\n\t"
    "s_sub_u32 s87, s98, 10\n\t"
    "s_cselect_b32 s98, s98, s87\n\t"
    "s_mul_i32 s98, s98, 524288\n\t"
    "s_add_u32 m0, s73, 16384\n\t"
    "s_add_u32 s85, s98, 1024\n\t"
    "buffer_load_dwordx4 v202, s[40:43], s85 offen offset:0 lds\n\t"
    "s_add_u32 m0, s73, 17280\n\t"
    "s_nop 0\n\t"
    "buffer_load_dwordx4 v202, s[40:43], s85 offen offset:128 lds\n\t"
    "s_add_u32 m0, s73, 18432\n\t"
    "s_add_u32 s85, s98, 66560\n\t"
    "buffer_load_dwordx4 v203, s[40:43], s85 offen offset:0 lds\n\t"
    "s_add_u32 m0, s73, 19328\n\t"
    "s_nop 0\n\t"
    "buffer_load_dwordx4 v203, s[40:43], s85 offen offset:128 lds\n\t"
    "s_waitcnt vmcnt(18)\n\t"
    "s_waitcnt vmcnt(12)\n\t"
    "s_barrier\n\t"
    "s_add_u32 s98, s96, 5\n\t"
    "s_sub_u32 s87, s98, 10\n\t"
    "s_cselect_b32 s98, s98, s87\n\t"
    "s_mul_i32 s98, s98, 524288\n\t"
    "s_add_u32 m0, s73, 32768\n\t"
    "s_add_u32 s85, s98, 0\n\t"
    "buffer_load_dwordx4 v202, s[40:43], s85 offen offset:0 lds\n\t"
    "s_add_u32 m0, s73, 33664\n\t"
    "s_nop 0\n\t"
    "buffer_load_dwordx4 v202, s[40:43], s85 offen offset:128 lds\n\t"
    "s_add_u32 m0, s73, 34816\n\t"
    "s_add_u32 s85, s98, 65536\n\t"
    "buffer_load_dwordx4 v203, s[40:43], s85 offen offset:0 lds\n\t"
    "s_add_u32 m0, s73, 35712\n\t"
    "s_nop 0\n\t"
    "buffer_load_dwordx4 v203, s[40:43], s85 offen offset:128 lds\n\t"
    "s_add_u32 s98, s96, 6\n\t"
    "s_sub_u32 s87, s98, 10\n\t"
    "s_cselect_b32 s98, s98, s87\n\t"
    "s_mul_i32 s98, s98, 128\n\t"
    "s_add_u32 m0, s72, 0\n\t"
    "s_add_u32 s84, s98, 0\n\t"
    "buffer_load_dwordx4 v200, s[36:39], s84 offen offset:0 lds\n\t"
    "s_add_u32 m0, s72, 1024\n\t"
    "s_add_u32 s84, s98, 10240\n\t"
    "buffer_load_dwordx4 v201, s[36:39], s84 offen offset:0 lds\n\t"
    "s_add_u32 m0, s72, 2048\n\t"
    "s_add_u32 s84, s98, 20480\n\t"
    "buffer_load_dwordx4 v200, s[36:39], s84 offen offset:0 lds\n\t"
    "s_add_u32 m0, s72, 3072\n\t"
    "s_add_u32 s84, s98, 30720\n\t"
    "buffer_load_dwordx4 v201, s[36:39], s84 offen offset:0 lds\n\t"
    "s_add_u32 m0, s72, 4096\n\t"
    "s_add_u32 s84, s98, 40960\n\t"
    "buffer_load_dwordx4 v200, s[36:39], s84 offen offset:0 lds\n\t"
    "s_add_u32 m0, s72, 5120\n\t"
    "s_add_u32 s84, s98, 51200\n\t"
    "buffer_load_dwordx4 v201, s[36:39], s84 offen offset:0 lds\n\t"
    "s_waitcnt vmcnt(10)\n\t"
    "s_barrier\n\t"
    "s_add_u32 s98, s96, 6\n\t"
    "s_sub_u32 s87, s98, 10\n\t"
    "s_cselect_b32 s98, s98, s87\n\t"
    "s_mul_i32 s98, s98, 524288\n\t"
    "s_add_u32 m0, s73, 0\n\t"
    "s_add_u32 s85, s98, 1024\n\t"
    "buffer_load_dwordx4 v202, s[40:43], s85 offen offset:0 lds\n\t"
    "s_add_u32 m0, s73, 896\n\t"
    "s_nop 0\n\t"
    "buffer_load_dwordx4 v202, s[40:43], s85 offen offset:128 lds\n\t"
    "s_add_u32 m0, s73, 2048\n\t"
    "s_add_u32 s85, s98, 66560\n\t"
    "buffer_load_dwordx4 v203, s[40:43], s85 offen offset:0 lds\n\t"
    "s_add_u32 m0, s73, 2944\n\t"
    "s_nop 0\n\t"
    "buffer_load_dwordx4 v203, s[40:43], s85 offen offset:128 lds\n\t"
    "s_add_u32 s86, s78, 49152\n\t"
    "s_and_b32 s86, s86, 0x1ffff\n\t"
    "buffer_load_dwordx4 v[16:19], v205, s[48:51], s86 offen offset:0 nt\n\t"
    "buffer_load_dwordx4 v[20:23], v205, s[48:51], s86 offen offset:128 nt\n\t"
    "s_add_u32 s86, s78, 65536\n\t"
    "s_and_b32 s86, s86, 0x1ffff\n\t"
    "buffer_load_dwordx4 v[24:27], v204, s[44:47], s86 offen offset:0 nt\n\t"
    "buffer_load_dwordx4 v[28:31], v204, s[44:47], s86 offen offset:128 nt\n\t"
    "s_add_u32 s86, s78, 81920\n\t"
    "s_and_b32 s86, s86, 0x1ffff\n\t"
    "buffer_load_dwordx4 v[32:35], v205, s[48:51], s86 offen offset:0 nt\n\t"
    "buffer_load_dwordx4 v[36:39], v205, s[48:51], s86 offen offset:128 nt\n\t"
    "s_add_u32 s86, s78, 98304\n\t"
    "s_and_b32 s86, s86, 0x1ffff\n\t"
    "buffer_load_dwordx4 v[40:43], v204, s[44:47], s86 offen offset:0 nt\n\t"
    "buffer_load_dwordx4 v[44:47], v204, s[44:47], s86 offen offset:128 nt\n\t"
    "s_waitcnt vmcnt(18)\n\t"
    "s_waitcnt vmcnt(12)\n\t"
    "s_barrier\n\t"
    "s_add_u32 s98, s96, 6\n\t"
    "s_sub_u32 s87, s98, 10\n\t"
    "s_cselect_b32 s98, s98, s87\n\t"
    "s_mul_i32 s98, s98, 524288\n\t"
    "s_add_u32 m0, s73, 16384\n\t"
    "s_add_u32 s85, s98, 0\n\t"
    "buffer_load_dwordx4 v202, s[40:43], s85 offen offset:0 lds\n\t"
    "s_add_u32 m0, s73, 17280\n\t"
    "s_nop 0\n\t"
    "buffer_load_dwordx4 v202, s[40:43], s85 offen offset:128 lds\n\t"
    "s_add_u32 m0, s73, 18432\n\t"
    "s_add_u32 s85, s98, 65536\n\t"
    "buffer_load_dwordx4 v203, s[40:43], s85 offen offset:0 lds\n\t"
    "s_add_u32 m0, s73, 19328\n\t"
    "s_nop 0\n\t"
    "buffer_load_dwordx4 v203, s[40:43], s85 offen offset:128 lds\n\t"
    "s_add_u32 s98, s96, 7\n\t"
    "s_sub_u32 s87, s98, 10\n\t"
    "s_cselect_b32 s98, s98, s87\n\t"
    "s_mul_i32 s98, s98, 128\n\t"
    "s_add_u32 m0, s72, 24576\n\t"
    "s_add_u32 s84, s98, 0\n\t"
    "buffer_load_dwordx4 v200, s[36:39], s84 offen offset:0 lds\n\t"
    "s_add_u32 m0, s72, 25600\n\t"
    "s_add_u32 s84, s98, 10240\n\t"
    "buffer_load_dwordx4 v201, s[36:39], s84 offen offset:0 lds\n\t"
    "s_add_u32 m0, s72, 26624\n\t"
    "s_add_u32 s84, s98, 20480\n\t"
    "buffer_load_dwordx4 v200, s[36:39], s84 offen offset:0 lds\n\t"
    "s_add_u32 m0, s72, 27648\n\t"
    "s_add_u32 s84, s98, 30720\n\t"
    "buffer_load_dwordx4 v201, s[36:39], s84 offen offset:0 lds\n\t"
    "s_add_u32 m0, s72, 28672\n\t"
    "s_add_u32 s84, s98, 40960\n\t"
    "buffer_load_dwordx4 v200, s[36:39], s84 offen offset:0 lds\n\t"
    "s_add_u32 m0, s72, 29696\n\t"
    "s_add_u32 s84, s98, 51200\n\t"
    "buffer_load_dwordx4 v201, s[36:39], s84 offen offset:0 lds\n\t"
    "s_waitcnt vmcnt(18)\n\t"
    "s_barrier\n\t"
    "s_add_u32 s98, s96, 7\n\t"
    "s_sub_u32 s87, s98, 10\n\t"
    "s_cselect_b32 s98, s98, s87\n\t"
    "s_mul_i32 s98, s98, 524288\n\t"
    "s_add_u32 m0, s73, 32768\n\t"
    "s_add_u32 s85, s98, 1024\n\t"
    "buffer_load_dwordx4 v202, s[40:43], s85 offen offset:0 lds\n\t"
    "s_add_u32 m0, s73, 33664\n\t"
    "s_nop 0\n\t"
    "buffer_load_dwordx4 v202, s[40:43], s85 offen offset:128 lds\n\t"
    "s_add_u32 m0, s73, 34816\n\t"
    "s_add_u32 s85, s98, 66560\n\t"
    "buffer_load_dwordx4 v203, s[40:43], s85 offen offset:0 lds\n\t"
    "s_add_u32 m0, s73, 35712\n\t"
    "s_nop 0\n\t"
    "buffer_load_dwordx4 v203, s[40:43], s85 offen offset:128 lds\n\t"
    "s_waitcnt vmcnt(10)\n\t"
    "s_waitcnt vmcnt(4)\n\t"
    "s_barrier\n\t"
    "s_add_u32 s98, s96, 7\n\t"
    "s_sub_u32 s87, s98, 10\n\t"
    "s_cselect_b32 s98, s98, s87\n\t"
    "s_mul_i32 s98, s98, 524288\n\t"
    "s_add_u32 m0, s73, 0\n\t"
    "s_add_u32 s85, s98, 0\n\t"
    "buffer_load_dwordx4 v202, s[40:43], s85 offen offset:0 lds\n\t"
    "s_add_u32 m0, s73, 896\n\t"
    "s_nop 0\n\t"
    "buffer_load_dwordx4 v202, s[40:43], s85 offen offset:128 lds\n\t"
    "s_add_u32 m0, s73, 2048\n\t"
    "s_add_u32 s85, s98, 65536\n\t"
    "buffer_load_dwordx4 v203, s[40:43], s85 offen offset:0 lds\n\t"
    "s_add_u32 m0, s73, 2944\n\t"
    "s_nop 0\n\t"
    "buffer_load_dwordx4 v203, s[40:43], s85 offen offset:128 lds\n\t"
    "s_add_u32 s98, s96, 8\n\t"
    "s_sub_u32 s87, s98, 10\n\t"
    "s_cselect_b32 s98, s98, s87\n\t"
    "s_mul_i32 s98, s98, 128\n\t"
    "s_add_u32 m0, s72, 0\n\t"
    "s_add_u32 s84, s98, 0\n\t"
    "buffer_load_dwordx4 v200, s[36:39], s84 offen offset:0 lds\n\t"
    "s_add_u32 m0, s72, 1024\n\t"
    "s_add_u32 s84, s98, 10240\n\t"
    "buffer_load_dwordx4 v201, s[36:39], s84 offen offset:0 lds\n\t"
    "s_add_u32 m0, s72, 2048\n\t"
    "s_add_u32 s84, s98, 20480\n\t"
    "buffer_load_dwordx4 v200, s[36:39], s84 offen offset:0 lds\n\t"
    "s_add_u32 m0, s72, 3072\n\t"
    "s_add_u32 s84, s98, 30720\n\t"
    "buffer_load_dwordx4 v201, s[36:39], s84 offen offset:0 lds\n\t"
    "s_add_u32 m0, s72, 4096\n\t"
    "s_add_u32 s84, s98, 40960\n\t"
    "buffer_load_dwordx4 v200, s[36:39], s84 offen offset:0 lds\n\t"
    "s_add_u32 m0, s72, 5120\n\t"
    "s_add_u32 s84, s98, 51200\n\t"
    "buffer_load_dwordx4 v201, s[36:39], s84 offen offset:0 lds\n\t"
    "s_add_u32 s86, s78, 114688\n\t"
    "s_and_b32 s86, s86, 0x1ffff\n\t"
    "buffer_load_dwordx4 v[48:51], v205, s[48:51], s86 offen offset:0 nt\n\t"
    "buffer_load_dwordx4 v[52:55], v205, s[48:51], s86 offen offset:128 nt\n\t"
    "s_add_u32 s86, s78, 8192\n\t"
    "s_and_b32 s86, s86, 0x1ffff\n\t"
    "s_add_u32 s86, s86, 524288\n\t"
    "buffer_load_dwordx4 v[64:67], v205, s[48:51], s86 offen offset:0 nt\n\t"
    "buffer_load_dwordx4 v[68:71], v205, s[48:51], s86 offen offset:128 nt\n\t"
    "s_add_u32 s86, s78, 24576\n\t"
    "s_and_b32 s86, s86, 0x1ffff\n\t"
    "s_add_u32 s86, s86, 524288\n\t"
    "buffer_load_dwordx4 v[72:75], v204, s[44:47], s86 offen offset:0 nt\n\t"
    "buffer_load_dwordx4 v[76:79], v204, s[44:47], s86 offen offset:128 nt\n\t"
    "s_waitcnt vmcnt(16)\n\t"
    "s_barrier\n\t"
    "s_add_u32 s98, s96, 8\n\t"
    "s_sub_u32 s87, s98, 10\n\t"
    "s_cselect_b32 s98, s98, s87\n\t"
    "s_mul_i32 s98, s98, 524288\n\t"
    "s_add_u32 m0, s73, 16384\n\t"
    "s_add_u32 s85, s98, 1024\n\t"
    "buffer_load_dwordx4 v202, s[40:43], s85 offen offset:0 lds\n\t"
    "s_add_u32 m0, s73, 17280\n\t"
    "s_nop 0\n\t"
    "buffer_load_dwordx4 v202, s[40:43], s85 offen offset:128 lds\n\t"
    "s_add_u32 m0, s73, 18432\n\t"
    "s_add_u32 s85, s98, 66560\n\t"
    "buffer_load_dwordx4 v203, s[40:43], s85 offen offset:0 lds\n\t"
    "s_add_u32 m0, s73, 19328\n\t"
    "s_nop 0\n\t"
    "buffer_load_dwordx4 v203, s[40:43], s85 offen offset:128 lds\n\t"
    "s_waitcnt vmcnt(16)\n\t"
    "s_waitcnt vmcnt(10)\n\t"
    "s_barrier\n\t"
    "s_add_u32 s98, s96, 8\n\t"
    "s_sub_u32 s87, s98, 10\n\t"
    "s_cselect_b32 s98, s98, s87\n\t"
    "s_mul_i32 s98, s98, 524288\n\t"
    "s_add_u32 m0, s73, 32768\n\t"
    "s_add_u32 s85, s98, 0\n\t"
    "buffer_load_dwordx4 v202, s[40:43], s85 offen offset:0 lds\n\t"
    "s_add_u32 m0, s73, 33664\n\t"
    "s_nop 0\n\t"
    "buffer_load_dwordx4 v202, s[40:43], s85 offen offset:128 lds\n\t"
    "s_add_u32 m0, s73, 34816\n\t"
    "s_add_u32 s85, s98, 65536\n\t"
    "buffer_load_dwordx4 v203, s[40:43], s85 offen offset:0 lds\n\t"
    "s_add_u32 m0, s73, 35712\n\t"
    "s_nop 0\n\t"
    "buffer_load_dwordx4 v203, s[40:43], s85 offen offset:128 lds\n\t"
    "s_add_u32 s98, s96, 9\n\t"
    "s_sub_u32 s87, s98, 10\n\t"
    "s_cselect_b32 s98, s98, s87\n\t"
    "s_mul_i32 s98, s98, 128\n\t"
    "s_add_u32 m0, s72, 24576\n\t"
    "s_add_u32 s84, s98, 0\n\t"
    "buffer_load_dwordx4 v200, s[36:39], s84 offen offset:0 lds\n\t"
    "s_add_u32 m0, s72, 25600\n\t"
    "s_add_u32 s84, s98, 10240\n\t"
    "buffer_load_dwordx4 v201, s[36:39], s84 offen offset:0 lds\n\t"
    "s_add_u32 m0, s72, 26624\n\t"
    "s_add_u32 s84, s98, 20480\n\t"
    "buffer_load_dwordx4 v200, s[36:39], s84 offen offset:0 lds\n\t"
    "s_add_u32 m0, s72, 27648\n\t"
    "s_add_u32 s84, s98, 30720\n\t"
    "buffer_load_dwordx4 v201, s[36:39], s84 offen offset:0 lds\n\t"
    "s_add_u32 m0, s72, 28672\n\t"
    "s_add_u32 s84, s98, 40960\n\t"
    "buffer_load_dwordx4 v200, s[36:39], s84 offen offset:0 lds\n\t"
    "s_add_u32 m0, s72, 29696\n\t"
    "s_add_u32 s84, s98, 51200\n\t"
    "buffer_load_dwordx4 v201, s[36:39], s84 offen offset:0 lds\n\t"
    "s_waitcnt vmcnt(10)\n\t"
    "s_barrier\n\t"
    "s_add_u32 s98, s96, 9\n\t"
    "s_sub_u32 s87, s98, 10\n\t"
    "s_cselect_b32 s98, s98, s87\n\t"
    "s_mul_i32 s98, s98, 524288\n\t"
    "s_add_u32 m0, s73, 0\n\t"
    "s_add_u32 s85, s98, 1024\n\t"
    "buffer_load_dwordx4 v202, s[40:43], s85 offen offset:0 lds\n\t"
    "s_add_u32 m0, s73, 896\n\t"
    "s_nop 0\n\t"
    "buffer_load_dwordx4 v202, s[40:43], s85 offen offset:128 lds\n\t"
    "s_add_u32 m0, s73, 2048\n\t"
    "s_add_u32 s85, s98, 66560\n\t"
    "buffer_load_dwordx4 v203, s[40:43], s85 offen offset:0 lds\n\t"
    "s_add_u32 m0, s73, 2944\n\t"
    "s_nop 0\n\t"
    "buffer_load_dwordx4 v203, s[40:43], s85 offen offset:128 lds\n\t"
    "s_add_u32 s86, s78, 40960\n\t"
    "s_and_b32 s86, s86, 0x1ffff\n\t"
    "s_add_u32 s86, s86, 524288\n\t"
    "buffer_load_dwordx4 v[80:83], v205, s[48:51], s86 offen offset:0 nt\n\t"
    "buffer_load_dwordx4 v[84:87], v205, s[48:51], s86 offen offset:128 nt\n\t"
    "s_add_u32 s86, s78, 57344\n\t"
    "s_and_b32 s86, s86, 0x1ffff\n\t"
    "s_add_u32 s86, s86, 524288\n\t"
    "buffer_load_dwordx4 v[88:91], v204, s[44:47], s86 offen offset:0 nt\n\t"
    "buffer_load_dwordx4 v[92:95], v204, s[44:47], s86 offen offset:128 nt\n\t"
    "s_add_u32 s86, s78, 73728\n\t"
    "s_and_b32 s86, s86, 0x1ffff\n\t"
    "s_add_u32 s86, s86, 524288\n\t"
    "buffer_load_dwordx4 v[96:99], v205, s[48:51], s86 offen offset:0 nt\n\t"
    "buffer_load_dwordx4 v[100:103], v205, s[48:51], s86 offen offset:128 nt\n\t"
    "s_add_u32 s86, s78, 90112\n\t"
    "s_and_b32 s86, s86, 0x1ffff\n\t"
    "s_add_u32 s86, s86, 524288\n\t"
    "buffer_load_dwordx4 v[104:107], v204, s[44:47], s86 offen offset:0 nt\n\t"
    "buffer_load_dwordx4 v[108:111], v204, s[44:47], s86 offen offset:128 nt\n\t"
    "s_waitcnt vmcnt(18)\n\t"
    "s_waitcnt vmcnt(12)\n\t"
    "s_barrier\n\t"
    "s_add_u32 s98, s96, 9\n\t"
    "s_sub_u32 s87, s98, 10\n\t"
    "s_cselect_b32 s98, s98, s87\n\t"
    "s_mul_i32 s98, s98, 524288\n\t"
    "s_add_u32 m0, s73, 16384\n\t"
    "s_add_u32 s85, s98, 0\n\t"
    "buffer_load_dwordx4 v202, s[40:43], s85 offen offset:0 lds\n\t"
    "s_add_u32 m0, s73, 17280\n\t"
    "s_nop 0\n\t"
    "buffer_load_dwordx4 v202, s[40:43], s85 offen offset:128 lds\n\t"
    "s_add_u32 m0, s73, 18432\n\t"
    "s_add_u32 s85, s98, 65536\n\t"
    "buffer_load_dwordx4 v203, s[40:43], s85 offen offset:0 lds\n\t"
    "s_add_u32 m0, s73, 19328\n\t"
    "s_nop 0\n\t"
    "buffer_load_dwordx4 v203, s[40:43], s85 offen offset:128 lds\n\t"
    "s_waitcnt vmcnt(12)\n\t"
    "s_barrier\n\t"
    "s_waitcnt vmcnt(0)\n\t"
    "s_barrier\n\t"
    "s_add_u32 m0, s74, 65536\n\t"
    "s_add_u32 s86, s78, 24576\n\t"
    "s_and_b32 s86, s86, 0x1ffff\n\t"
    "buffer_load_dwordx4 v205, s[48:51], s86 offen offset:0 nt lds\n\t"
    "s_add_u32 m0, s74, 66432\n\t"
    "s_nop 0\n\t"
    "buffer_load_dwordx4 v205, s[48:51], s86 offen offset:128 nt lds\n\t"
    "s_add_u32 m0, s74, 73728\n\t"
    "s_add_u32 s86, s78, 40960\n\t"
    "s_and_b32 s86, s86, 0x1ffff\n\t"
    "buffer_load_dwordx4 v204, s[44:47], s86 offen offset:0 nt lds\n\t"
    "s_add_u32 m0, s74, 74624\n\t"
    "s_nop 0\n\t"
    "buffer_load_dwordx4 v204, s[44:47], s86 offen offset:128 nt lds\n\t"
    "s_barrier\n\t"
    "s_add_u32 m0, s74, 81920\n\t"
    "s_add_u32 s86, s78, 32768\n\t"
    "s_and_b32 s86, s86, 0x1ffff\n\t"
    "buffer_load_dwordx4 v205, s[48:51], s86 offen offset:0 nt lds\n\t"
    "s_add_u32 m0, s74, 82816\n\t"
    "s_nop 0\n\t"
    "buffer_load_dwordx4 v205, s[48:51], s86 offen offset:128 nt lds\n\t"
    "s_add_u32 m0, s74, 90112\n\t"
    "s_add_u32 s86, s78, 49152\n\t"
    "s_and_b32 s86, s86, 0x1ffff\n\t"
    "buffer_load_dwordx4 v204, s[44:47], s86 offen offset:0 nt lds\n\t"
    "s_add_u32 m0, s74, 91008\n\t"
    "s_nop 0\n\t"
    "buffer_load_dwordx4 v204, s[44:47], s86 offen offset:128 nt lds\n\t"
    "s_waitcnt lgkmcnt(0)\n\t"
    "s_barrier\n\t"
    "s_add_u32 m0, s74, 0\n\t"
    "s_add_u32 s86, s78, 40960\n\t"
    "s_and_b32 s86, s86, 0x1ffff\n\t"
    "buffer_load_dwordx4 v205, s[48:51], s86 offen offset:0 nt lds\n\t"
    "s_add_u32 m0, s74, 896\n\t"
    "s_nop 0\n\t"
    "buffer_load_dwordx4 v205, s[48:51], s86 offen offset:128 nt lds\n\t"
    "s_add_u32 m0, s74, 8192\n\t"
    "s_add_u32 s86, s78, 57344\n\t"
    "s_and_b32 s86, s86, 0x1ffff\n\t"
    "buffer_load_dwordx4 v204, s[44:47], s86 offen offset:0 nt lds\n\t"
    "s_add_u32 m0, s74, 9088\n\t"
    "s_nop 0\n\t"
    "buffer_load_dwordx4 v204, s[44:47], s86 offen offset:128 nt lds\n\t"
    "s_waitcnt lgkmcnt(0)\n\t"
    "s_barrier\n\t"
    "ds_write_b128 v11, v[16:19] offset:16384\n\t"
    "ds_write_b128 v11, v[20:23] offset:17408\n\t"
    "ds_write_b128 v11, v[24:27] offset:24576\n\t"
    "ds_write_b128 v11, v[28:31] offset:25600\n\t"
    "s_mul_i32 s84, s34, 1310720\n\t"
    "s_mul_i32 s87, s35, 40960\n\t"
    "s_add_u32 s84, s84, s87\n\t"
    "s_mul_i32 s87, s26, 10240\n\t"
    "s_add_u32 s84, s84, s87\n\t"
    "s_add_u32 s52, s20, s84\n\t"
    "s_addc_u32 s53, s21, 0\n\t"
    "s_and_b32 s53, s53, 0xffff\n\t"
    "s_mov_b32 s54, 10240\n\t"
    "s_mov_b32 s55, 0x00020000\n\t"
    "v_lshlrev_b32 v8, 3, v253\n\t"
    "s_mov_b32 s84, 8192\n\t"
    "s_mov_b32 s54, 5120\n\t"
    "s_mov_b32 s84, 0\n\t"
    "buffer_load_dword v10, v8, s[52:55], s84 offen\n\t"
    "s_waitcnt lgkmcnt(0)\n\t"
    "s_barrier\n\t"
    "s_add_u32 m0, s74, 32768\n\t"
    "s_add_u32 s86, s78, 57344\n\t"
    "s_and_b32 s86, s86, 0x1ffff\n\t"
    "buffer_load_dwordx4 v205, s[48:51], s86 offen offset:0 nt lds\n\t"
    "s_add_u32 m0, s74, 33664\n\t"
    "s_nop 0\n\t"
    "buffer_load_dwordx4 v205, s[48:51], s86 offen offset:128 nt lds\n\t"
    "s_add_u32 m0, s74, 40960\n\t"
    "s_add_u32 s86, s78, 73728\n\t"
    "s_and_b32 s86, s86, 0x1ffff\n\t"
    "buffer_load_dwordx4 v204, s[44:47], s86 offen offset:0 nt lds\n\t"
    "s_add_u32 m0, s74, 41856\n\t"
    "s_nop 0\n\t"
    "buffer_load_dwordx4 v204, s[44:47], s86 offen offset:128 nt lds\n\t"
    "s_waitcnt vmcnt(13)\n\t"
    "s_waitcnt lgkmcnt(0)\n\t"
    "s_barrier\n\t"
    "s_add_u32 m0, s74, 49152\n\t"
    "s_add_u32 s86, s78, 65536\n\t"
    "s_and_b32 s86, s86, 0x1ffff\n\t"
    "buffer_load_dwordx4 v205, s[48:51], s86 offen offset:0 nt lds\n\t"
    "s_add_u32 m0, s74, 50048\n\t"
    "s_nop 0\n\t"
    "buffer_load_dwordx4 v205, s[48:51], s86 offen offset:128 nt lds\n\t"
    "s_add_u32 m0, s74, 57344\n\t"
    "s_add_u32 s86, s78, 81920\n\t"
    "s_and_b32 s86, s86, 0x1ffff\n\t"
    "buffer_load_dwordx4 v204, s[44:47], s86 offen offset:0 nt lds\n\t"
    "s_add_u32 m0, s74, 58240\n\t"
    "s_nop 0\n\t"
    "buffer_load_dwordx4 v204, s[44:47], s86 offen offset:128 nt lds\n\t"
    "s_waitcnt vmcnt(13)\n\t"
    "s_waitcnt lgkmcnt(0)\n\t"
    "s_barrier\n\t"
    "s_add_u32 m0, s74, 65536\n\t"
    "s_add_u32 s86, s78, 73728\n\t"
    "s_and_b32 s86, s86, 0x1ffff\n\t"
    "buffer_load_dwordx4 v205, s[48:51], s86 offen offset:0 nt lds\n\t"
    "s_add_u32 m0, s74, 66432\n\t"
    "s_nop 0\n\t"
    "buffer_load_dwordx4 v205, s[48:51], s86 offen offset:128 nt lds\n\t"
    "s_add_u32 m0, s74, 73728\n\t"
    "s_add_u32 s86, s78, 90112\n\t"
    "s_and_b32 s86, s86, 0x1ffff\n\t"
    "buffer_load_dwordx4 v204, s[44:47], s86 offen offset:0 nt lds\n\t"
    "s_add_u32 m0, s74, 74624\n\t"
    "s_nop 0\n\t"
    "buffer_load_dwordx4 v204, s[44:47], s86 offen offset:128 nt lds\n\t"
    "s_waitcnt vmcnt(13)\n\t"
    "s_waitcnt lgkmcnt(0)\n\t"
    "s_barrier\n\t"
    "ds_write_b128 v12, v[32:35] offset:32768\n\t"
    "ds_write_b128 v12, v[36:39] offset:33792\n\t"
    "ds_write_b128 v12, v[40:43] offset:40960\n\t"
    "ds_write_b128 v12, v[44:47] offset:41984\n\t"
    "s_mul_i32 s84, s34, 1310720\n\t"
    "s_mul_i32 s87, s35, 40960\n\t"
    "s_add_u32 s84, s84, s87\n\t"
    "s_mul_i32 s87, s26, 10240\n\t"
    "s_add_u32 s84, s84, s87\n\t"
    "s_add_u32 s52, s20, s84\n\t"
    "s_addc_u32 s53, s21, 0\n\t"
    "s_and_b32 s53, s53, 0xffff\n\t"
    "s_mov_b32 s54, 10240\n\t"
    "s_mov_b32 s55, 0x00020000\n\t"
    "v_lshlrev_b32 v8, 3, v253\n\t"
    "s_mov_b32 s84, 8192\n\t"
    "s_mov_b32 s54, 10240\n\t"
    "s_mov_b32 s84, 5120\n\t"
    "s_waitcnt vmcnt(12)\n\t"
    "buffer_load_dword v10, v8, s[52:55], s84 offen\n\t"
    "s_waitcnt lgkmcnt(0)\n\t"
    "s_barrier\n\t"
    "s_add_u32 m0, s74, 0\n\t"
    "s_add_u32 s86, s78, 90112\n\t"
    "s_and_b32 s86, s86, 0x1ffff\n\t"
    "buffer_load_dwordx4 v205, s[48:51], s86 offen offset:0 nt lds\n\t"
    "s_add_u32 m0, s74, 896\n\t"
    "s_nop 0\n\t"
    "buffer_load_dwordx4 v205, s[48:51], s86 offen offset:128 nt lds\n\t"
    "s_add_u32 m0, s74, 8192\n\t"
    "s_add_u32 s86, s78, 106496\n\t"
    "s_and_b32 s86, s86, 0x1ffff\n\t"
    "buffer_load_dwordx4 v204, s[44:47], s86 offen offset:0 nt lds\n\t"
    "s_add_u32 m0, s74, 9088\n\t"
    "s_nop 0\n\t"
    "buffer_load_dwordx4 v204, s[44:47], s86 offen offset:128 nt lds\n\t"
    "s_waitcnt vmcnt(13)\n\t"
    "s_waitcnt lgkmcnt(0)\n\t"
    "s_barrier\n\t"
    "s_add_u32 m0, s74, 16384\n\t"
    "s_add_u32 s86, s78, 98304\n\t"
    "s_and_b32 s86, s86, 0x1ffff\n\t"
    "buffer_load_dwordx4 v205, s[48:51], s86 offen offset:0 nt lds\n\t"
    "s_add_u32 m0, s74, 17280\n\t"
    "s_nop 0\n\t"
    "buffer_load_dwordx4 v205, s[48:51], s86 offen offset:128 nt lds\n\t"
    "s_add_u32 m0, s74, 24576\n\t"
    "s_add_u32 s86, s78, 114688\n\t"
    "s_and_b32 s86, s86, 0x1ffff\n\t"
    "buffer_load_dwordx4 v204, s[44:47], s86 offen offset:0 nt lds\n\t"
    "s_add_u32 m0, s74, 25472\n\t"
    "s_nop 0\n\t"
    "buffer_load_dwordx4 v204, s[44:47], s86 offen offset:128 nt lds\n\t"
    "s_waitcnt vmcnt(13)\n\t"
    "s_waitcnt lgkmcnt(0)\n\t"
    "s_barrier\n\t"
    "s_add_u32 m0, s74, 32768\n\t"
    "s_add_u32 s86, s78, 106496\n\t"
    "s_and_b32 s86, s86, 0x1ffff\n\t"
    "buffer_load_dwordx4 v205, s[48:51], s86 offen offset:0 nt lds\n\t"
    "s_add_u32 m0, s74, 33664\n\t"
    "s_nop 0\n\t"
    "buffer_load_dwordx4 v205, s[48:51], s86 offen offset:128 nt lds\n\t"
    "s_add_u32 m0, s74, 40960\n\t"
    "s_add_u32 s86, s78, 122880\n\t"
    "s_and_b32 s86, s86, 0x1ffff\n\t"
    "buffer_load_dwordx4 v204, s[44:47], s86 offen offset:0 nt lds\n\t"
    "s_add_u32 m0, s74, 41856\n\t"
    "s_nop 0\n\t"
    "buffer_load_dwordx4 v204, s[44:47], s86 offen offset:128 nt lds\n\t"
    "s_waitcnt vmcnt(13)\n\t"
    "s_waitcnt lgkmcnt(0)\n\t"
    "s_barrier\n\t"
    "ds_write_b128 v12, v[48:51] offset:0\n\t"
    "ds_write_b128 v12, v[52:55] offset:1024\n\t"
    "s_waitcnt lgkmcnt(0)\n\t"
    "s_barrier\n\t"
    "s_add_u32 m0, s74, 65536\n\t"
    "s_add_u32 s86, s78, 122880\n\t"
    "s_and_b32 s86, s86, 0x1ffff\n\t"
    "buffer_load_dwordx4 v205, s[48:51], s86 offen offset:0 nt lds\n\t"
    "s_add_u32 m0, s74, 66432\n\t"
    "s_nop 0\n\t"
    "buffer_load_dwordx4 v205, s[48:51], s86 offen offset:128 nt lds\n\t"
    "s_waitcnt vmcnt(10)\n\t"
    "s_waitcnt lgkmcnt(0)\n\t"
    "s_barrier\n\t"
    "s_add_u32 m0, s74, 81920\n\t"
    "s_add_u32 s86, s78, 0\n\t"
    "s_and_b32 s86, s86, 0x1ffff\n\t"
    "s_add_u32 s86, s86, 524288\n\t"
    "buffer_load_dwordx4 v204, s[44:47], s86 offen offset:0 nt lds\n\t"
    "s_add_u32 m0, s74, 82816\n\t"
    "s_nop 0\n\t"
    "buffer_load_dwordx4 v204, s[44:47], s86 offen offset:128 nt lds\n\t"
    "s_add_u32 m0, s74, 90112\n\t"
    "s_add_u32 s86, s78, 8192\n\t"
    "s_and_b32 s86, s86, 0x1ffff\n\t"
    "s_add_u32 s86, s86, 524288\n\t"
    "buffer_load_dwordx4 v204, s[44:47], s86 offen offset:0 nt lds\n\t"
    "s_add_u32 m0, s74, 91008\n\t"
    "s_nop 0\n\t"
    "buffer_load_dwordx4 v204, s[44:47], s86 offen offset:128 nt lds\n\t"
    "s_waitcnt vmcnt(10)\n\t"
    "s_waitcnt lgkmcnt(0)\n\t"
    "s_barrier\n\t"
    "s_add_u32 m0, s74, 0\n\t"
    "s_add_u32 s86, s78, 0\n\t"
    "s_and_b32 s86, s86, 0x1ffff\n\t"
    "s_add_u32 s86, s86, 524288\n\t"
    "buffer_load_dwordx4 v205, s[48:51], s86 offen offset:0 nt lds\n\t"
    "s_add_u32 m0, s74, 896\n\t"
    "s_nop 0\n\t"
    "buffer_load_dwordx4 v205, s[48:51], s86 offen offset:128 nt lds\n\t"
    "s_add_u32 m0, s74, 8192\n\t"
    "s_add_u32 s86, s78, 16384\n\t"
    "s_and_b32 s86, s86, 0x1ffff\n\t"
    "s_add_u32 s86, s86, 524288\n\t"
    "buffer_load_dwordx4 v204, s[44:47], s86 offen offset:0 nt lds\n\t"
    "s_add_u32 m0, s74, 9088\n\t"
    "s_nop 0\n\t"
    "buffer_load_dwordx4 v204, s[44:47], s86 offen offset:128 nt lds\n\t"
    "s_waitcnt vmcnt(10)\n\t"
    "s_waitcnt lgkmcnt(0)\n\t"
    "s_barrier\n\t"
    "ds_write_b128 v11, v[64:67] offset:16384\n\t"
    "ds_write_b128 v11, v[68:71] offset:17408\n\t"
    "ds_write_b128 v11, v[72:75] offset:24576\n\t"
    "ds_write_b128 v11, v[76:79] offset:25600\n\t"
    "s_waitcnt lgkmcnt(0)\n\t"
    "s_barrier\n\t"
    "s_add_u32 m0, s74, 32768\n\t"
    "s_add_u32 s86, s78, 16384\n\t"
    "s_and_b32 s86, s86, 0x1ffff\n\t"
    "s_add_u32 s86, s86, 524288\n\t"
    "buffer_load_dwordx4 v205, s[48:51], s86 offen offset:0 nt lds\n\t"
    "s_add_u32 m0, s74, 33664\n\t"
    "s_nop 0\n\t"
    "buffer_load_dwordx4 v205, s[48:51], s86 offen offset:128 nt lds\n\t"
    "s_add_u32 m0, s74, 40960\n\t"
    "s_add_u32 s86, s78, 32768\n\t"
    "s_and_b32 s86, s86, 0x1ffff\n\t"
    "s_add_u32 s86, s86, 524288\n\t"
    "buffer_load_dwordx4 v204, s[44:47], s86 offen offset:0 nt lds\n\t"
    "s_add_u32 m0, s74, 41856\n\t"
    "s_nop 0\n\t"
    "buffer_load_dwordx4 v204, s[44:47], s86 offen offset:128 nt lds\n\t"
    "s_waitcnt vmcnt(12)\n\t"
    "s_waitcnt lgkmcnt(0)\n\t"
    "s_barrier\n\t"
    "s_add_u32 m0, s74, 49152\n\t"
    "s_add_u32 s86, s78, 24576\n\t"
    "s_and_b32 s86, s86, 0x1ffff\n\t"
    "s_add_u32 s86, s86, 524288\n\t"
    "buffer_load_dwordx4 v205, s[48:51], s86 offen offset:0 nt lds\n\t"
    "s_add_u32 m0, s74, 50048\n\t"
    "s_nop 0\n\t"
    "buffer_load_dwordx4 v205, s[48:51], s86 offen offset:128 nt lds\n\t"
    "s_add_u32 m0, s74, 57344\n\t"
    "s_add_u32 s86, s78, 40960\n\t"
    "s_and_b32 s86, s86, 0x1ffff\n\t"
    "s_add_u32 s86, s86, 524288\n\t"
    "buffer_load_dwordx4 v204, s[44:47], s86 offen offset:0 nt lds\n\t"
    "s_add_u32 m0, s74, 58240\n\t"
    "s_nop 0\n\t"
    "buffer_load_dwordx4 v204, s[44:47], s86 offen offset:128 nt lds\n\t"
    "s_waitcnt vmcnt(12)\n\t"
    "s_waitcnt lgkmcnt(0)\n\t"
    "s_barrier\n\t"
    "s_add_u32 m0, s74, 65536\n\t"
    "s_add_u32 s86, s78, 32768\n\t"
    "s_and_b32 s86, s86, 0x1ffff\n\t"
    "s_add_u32 s86, s86, 524288\n\t"
    "buffer_load_dwordx4 v205, s[48:51], s86 offen offset:0 nt lds\n\t"
    "s_add_u32 m0, s74, 66432\n\t"
    "s_nop 0\n\t"
    "buffer_load_dwordx4 v205, s[48:51], s86 offen offset:128 nt lds\n\t"
    "s_add_u32 m0, s74, 73728\n\t"
    "s_add_u32 s86, s78, 49152\n\t"
    "s_and_b32 s86, s86, 0x1ffff\n\t"
    "s_add_u32 s86, s86, 524288\n\t"
    "buffer_load_dwordx4 v204, s[44:47], s86 offen offset:0 nt lds\n\t"
    "s_add_u32 m0, s74, 74624\n\t"
    "s_nop 0\n\t"
    "buffer_load_dwordx4 v204, s[44:47], s86 offen offset:128 nt lds\n\t"
    "s_waitcnt vmcnt(12)\n\t"
    "s_waitcnt lgkmcnt(0)\n\t"
    "s_barrier\n\t"
    "ds_write_b128 v12, v[80:83] offset:32768\n\t"
    "ds_write_b128 v12, v[84:87] offset:33792\n\t"
    "ds_write_b128 v12, v[88:91] offset:40960\n\t"
    "ds_write_b128 v12, v[92:95] offset:41984\n\t"
    "s_waitcnt lgkmcnt(0)\n\t"
    "s_barrier\n\t"
    "s_add_u32 m0, s74, 0\n\t"
    "s_add_u32 s86, s78, 49152\n\t"
    "s_and_b32 s86, s86, 0x1ffff\n\t"
    "s_add_u32 s86, s86, 524288\n\t"
    "buffer_load_dwordx4 v205, s[48:51], s86 offen offset:0 nt lds\n\t"
    "s_add_u32 m0, s74, 896\n\t"
    "s_nop 0\n\t"
    "buffer_load_dwordx4 v205, s[48:51], s86 offen offset:128 nt lds\n\t"
    "s_add_u32 m0, s74, 8192\n\t"
    "s_add_u32 s86, s78, 65536\n\t"
    "s_and_b32 s86, s86, 0x1ffff\n\t"
    "s_add_u32 s86, s86, 524288\n\t"
    "buffer_load_dwordx4 v204, s[44:47], s86 offen offset:0 nt lds\n\t"
    "s_add_u32 m0, s74, 9088\n\t"
    "s_nop 0\n\t"
    "buffer_load_dwordx4 v204, s[44:47], s86 offen offset:128 nt lds\n\t"
    "s_waitcnt vmcnt(12)\n\t"
    "s_waitcnt lgkmcnt(0)\n\t"
    "s_barrier\n\t"
    "s_add_u32 m0, s74, 16384\n\t"
    "s_add_u32 s86, s78, 57344\n\t"
    "s_and_b32 s86, s86, 0x1ffff\n\t"
    "s_add_u32 s86, s86, 524288\n\t"
    "buffer_load_dwordx4 v205, s[48:51], s86 offen offset:0 nt lds\n\t"
    "s_add_u32 m0, s74, 17280\n\t"
    "s_nop 0\n\t"
    "buffer_load_dwordx4 v205, s[48:51], s86 offen offset:128 nt lds\n\t"
    "s_add_u32 m0, s74, 24576\n\t"
    "s_add_u32 s86, s78, 73728\n\t"
    "s_and_b32 s86, s86, 0x1ffff\n\t"
    "s_add_u32 s86, s86, 524288\n\t"
    "buffer_load_dwordx4 v204, s[44:47], s86 offen offset:0 nt lds\n\t"
    "s_add_u32 m0, s74, 25472\n\t"
    "s_nop 0\n\t"
    "buffer_load_dwordx4 v204, s[44:47], s86 offen offset:128 nt lds\n\t"
    "s_waitcnt vmcnt(12)\n\t"
    "s_waitcnt lgkmcnt(0)\n\t"
    "s_barrier\n\t"
    "s_add_u32 m0, s74, 32768\n\t"
    "s_add_u32 s86, s78, 65536\n\t"
    "s_and_b32 s86, s86, 0x1ffff\n\t"
    "s_add_u32 s86, s86, 524288\n\t"
    "buffer_load_dwordx4 v205, s[48:51], s86 offen offset:0 nt lds\n\t"
    "s_add_u32 m0, s74, 33664\n\t"
    "s_nop 0\n\t"
    "buffer_load_dwordx4 v205, s[48:51], s86 offen offset:128 nt lds\n\t"
    "s_add_u32 m0, s74, 40960\n\t"
    "s_add_u32 s86, s78, 81920\n\t"
    "s_and_b32 s86, s86, 0x1ffff\n\t"
    "s_add_u32 s86, s86, 524288\n\t"
    "buffer_load_dwordx4 v204, s[44:47], s86 offen offset:0 nt lds\n\t"
    "s_add_u32 m0, s74, 41856\n\t"
    "s_nop 0\n\t"
    "buffer_load_dwordx4 v204, s[44:47], s86 offen offset:128 nt lds\n\t"
    "s_cmp_lg_u32 s26, 0\n\t"
    "s_cbranch_scc1 L_nfa_%=\n\t"
    "v_mov_b32 v4, s89\n\t"
    "v_lshlrev_b32 v5, 12, v253\n\t"
    "buffer_store_dword v4, v5, s[64:67], s91 offen offset:0 sc1\n\t"
    "s_nop 1\n\t"
    "L_nfa_%=:\n\t"
    "s_waitcnt vmcnt(12)\n\t"
    "s_waitcnt lgkmcnt(0)\n\t"
    "s_barrier\n\t"
    "ds_write_b128 v12, v[96:99] offset:0\n\t"
    "ds_write_b128 v12, v[100:103] offset:1024\n\t"
    "ds_write_b128 v12, v[104:107] offset:8192\n\t"
    "ds_write_b128 v12, v[108:111] offset:9216\n\t"
    "s_waitcnt lgkmcnt(0)\n\t"
    "s_barrier\n\t"
    "s_add_u32 m0, s74, 65536\n\t"
    "s_add_u32 s86, s78, 81920\n\t"
    "s_and_b32 s86, s86, 0x1ffff\n\t"
    "s_add_u32 s86, s86, 524288\n\t"
    "buffer_load_dwordx4 v205, s[48:51], s86 offen offset:0 nt lds\n\t"
    "s_add_u32 m0, s74, 66432\n\t"
    "s_nop 0\n\t"
    "buffer_load_dwordx4 v205, s[48:51], s86 offen offset:128 nt lds\n\t"
    "s_add_u32 m0, s74, 73728\n\t"
    "s_add_u32 s86, s78, 98304\n\t"
    "s_and_b32 s86, s86, 0x1ffff\n\t"
    "s_add_u32 s86, s86, 524288\n\t"
    "buffer_load_dwordx4 v204, s[44:47], s86 offen offset:0 nt lds\n\t"
    "s_add_u32 m0, s74, 74624\n\t"
    "s_nop 0\n\t"
    "buffer_load_dwordx4 v204, s[44:47], s86 offen offset:128 nt lds\n\t"
    "s_waitcnt vmcnt(12)\n\t"
    "s_waitcnt lgkmcnt(0)\n\t"
    "s_barrier\n\t"
    "s_add_u32 m0, s74, 81920\n\t"
    "s_add_u32 s86, s78, 90112\n\t"
    "s_and_b32 s86, s86, 0x1ffff\n\t"
    "s_add_u32 s86, s86, 524288\n\t"
    "buffer_load_dwordx4 v205, s[48:51], s86 offen offset:0 nt lds\n\t"
    "s_add_u32 m0, s74, 82816\n\t"
    "s_nop 0\n\t"
    "buffer_load_dwordx4 v205, s[48:51], s86 offen offset:128 nt lds\n\t"
    "s_add_u32 m0, s74, 90112\n\t"
    "s_add_u32 s86, s78, 106496\n\t"
    "s_and_b32 s86, s86, 0x1ffff\n\t"
    "s_add_u32 s86, s86, 524288\n\t"
    "buffer_load_dwordx4 v204, s[44:47], s86 offen offset:0 nt lds\n\t"
    "s_add_u32 m0, s74, 91008\n\t"
    "s_nop 0\n\t"
    "buffer_load_dwordx4 v204, s[44:47], s86 offen offset:128 nt lds\n\t"
    "s_waitcnt vmcnt(12)\n\t"
    "s_waitcnt lgkmcnt(0)\n\t"
    "s_barrier\n\t"
    "s_add_u32 m0, s74, 0\n\t"
    "s_add_u32 s86, s78, 98304\n\t"
    "s_and_b32 s86, s86, 0x1ffff\n\t"
    "s_add_u32 s86, s86, 524288\n\t"
    "buffer_load_dwordx4 v205, s[48:51], s86 offen offset:0 nt lds\n\t"
    "s_add_u32 m0, s74, 896\n\t"
    "s_nop 0\n\t"
    "buffer_load_dwordx4 v205, s[48:51], s86 offen offset:128 nt lds\n\t"
    "s_add_u32 m0, s74, 8192\n\t"
    "s_add_u32 s86, s78, 114688\n\t"
    "s_and_b32 s86, s86, 0x1ffff\n\t"
    "s_add_u32 s86, s86, 524288\n\t"
    "buffer_load_dwordx4 v204, s[44:47], s86 offen offset:0 nt lds\n\t"
    "s_add_u32 m0, s74, 9088\n\t"
    "s_nop 0\n\t"
    "buffer_load_dwordx4 v204, s[44:47], s86 offen offset:128 nt lds\n\t"
    "s_waitcnt vmcnt(12)\n\t"
    "s_waitcnt lgkmcnt(0)\n\t"
    "s_barrier\n\t"
    "s_add_u32 m0, s74, 16384\n\t"
    "s_add_u32 s86, s78, 106496\n\t"
    "s_and_b32 s86, s86, 0x1ffff\n\t"
    "s_add_u32 s86, s86, 524288\n\t"
    "buffer_load_dwordx4 v205, s[48:51], s86 offen offset:0 nt lds\n\t"
    "s_add_u32 m0, s74, 17280\n\t"
    "s_nop 0\n\t"
    "buffer_load_dwordx4 v205, s[48:51], s86 offen offset:128 nt lds\n\t"
    "s_add_u32 m0, s74, 24576\n\t"
    "s_add_u32 s86, s78, 122880\n\t"
    "s_and_b32 s86, s86, 0x1ffff\n\t"
    "s_add_u32 s86, s86, 524288\n\t"
    "buffer_load_dwordx4 v204, s[44:47], s86 offen offset:0 nt lds\n\t"
    "s_add_u32 m0, s74, 25472\n\t"
    "s_nop 0\n\t"
    "buffer_load_dwordx4 v204, s[44:47], s86 offen offset:128 nt lds\n\t"
    "s_waitcnt lgkmcnt(0)\n\t"
    "s_barrier\n\t"
    "s_add_u32 m0, s74, 32768\n\t"
    "s_add_u32 s86, s78, 114688\n\t"
    "s_and_b32 s86, s86, 0x1ffff\n\t"
    "s_add_u32 s86, s86, 524288\n\t"
    "buffer_load_dwordx4 v205, s[48:51], s86 offen offset:0 nt lds\n\t"
    "s_add_u32 m0, s74, 33664\n\t"
    "s_nop 0\n\t"
    "buffer_load_dwordx4 v205, s[48:51], s86 offen offset:128 nt lds\n\t"
    "s_waitcnt vmcnt(14)\n\t"
    "s_waitcnt lgkmcnt(0)\n\t"
    "s_barrier\n\t"
    "s_add_u32 m0, s74, 49152\n\t"
    "s_add_u32 s86, s78, 122880\n\t"
    "s_and_b32 s86, s86, 0x1ffff\n\t"
    "s_add_u32 s86, s86, 524288\n\t"
    "buffer_load_dwordx4 v205, s[48:51], s86 offen offset:0 nt lds\n\t"
    "s_add_u32 m0, s74, 50048\n\t"
    "s_nop 0\n\t"
    "buffer_load_dwordx4 v205, s[48:51], s86 offen offset:128 nt lds\n\t"
    "s_lshr_b32 s79, s35, 3\n\t"
    "s_lshr_b32 s85, s26, 1\n\t"
    "s_and_b32 s86, s26, 1\n\t"
    "s_mul_i32 s84, s29, 12582912\n\t"
    "s_mul_i32 s87, s30, 1572864\n\t"
    "s_add_u32 s84, s84, s87\n\t"
    "s_mul_i32 s87, s34, 393216\n\t"
    "s_add_u32 s84, s84, s87\n\t"
    "s_mul_i32 s87, s26, 3072\n\t"
    "s_add_u32 s84, s84, s87\n\t"
    "s_add_u32 s36, s22, s84\n\t"
    "s_addc_u32 s37, s23, 0\n\t"
    "s_and_b32 s37, s37, 0xffff\n\t"
    "s_mov_b32 s38, 384000\n\t"
    "s_mov_b32 s39, 0x00020000\n\t"
    "s_mul_i32 s86, s79, 320\n\t"
    "s_andn2_b32 s86, s86, 127\n\t"
    "s_mul_i32 s84, s34, 1310720\n\t"
    "s_mul_i32 s87, s26, 10240\n\t"
    "s_add_u32 s84, s84, s87\n\t"
    "s_mul_i32 s87, s86, 1\n\t"
    "s_add_u32 s84, s84, s87\n\t"
    "s_add_u32 s40, s20, s84\n\t"
    "s_addc_u32 s41, s21, 0\n\t"
    "s_and_b32 s41, s41, 0xffff\n\t"
    "s_mov_b32 s42, 1279104\n\t"
    "s_mov_b32 s43, 0x00020000\n\t"
    "s_mul_i32 s72, s26, 3072\n\t"
    "s_add_u32 s72, s72, 65536\n\t"
    "s_add_u32 s72, s72, s27\n\t"
    "v_bfe_u32 v6, v253, 6, 3\n\t"
    "v_lshl_add_u32 v206, v6, 10, v205\n\t"
    "s_mul_i32 s84, s29, 1024\n\t"
    "s_mul_i32 s87, s30, 128\n\t"
    "s_add_u32 s84, s84, s87\n\t"
    "s_mul_i32 s87, s34, 32\n\t"
    "s_add_u32 s84, s84, s87\n\t"
    "v_bfe_u32 v7, v253, 4, 3\n\t"
    "v_lshlrev_b32 v7, 2, v7\n\t"
    "v_add_u32 v7, s84, v7\n\t"
    "s_mov_b32 s97, 0\n\t"
    "s_waitcnt vmcnt(12)\n\t"
    "s_waitcnt lgkmcnt(0)\n\t"
    "s_barrier\n\t"
    "s_bitcmp1_b32 s97, 0\n\t"
    "s_cbranch_scc0 L_pw0_%=\n\t"
    "L_pr0_%=:\n\t"
    "s_add_u32 m0, s72, 49152\n\t"
    "s_mov_b32 s84, 0\n\t"
    "buffer_load_dwordx4 v253, s[36:39], s84 offen offset:0 sc1 lds\n\t"
    "buffer_load_dwordx4 v253, s[36:39], s84 offen offset:1024 sc1 lds\n\t"
    "buffer_load_dwordx4 v253, s[36:39], s84 offen offset:2048 sc1 lds\n\t"
    "s_mov_b32 s84, 0\n\t"
    "s_add_u32 m0, s72, 61440\n\t"
    "s_nop 0\n\t"
    "buffer_load_dwordx4 v206, s[40:43], s84 offen offset:0 lds\n\t"
    "s_add_u32 m0, s72, 62336\n\t"
    "s_nop 0\n\t"
    "buffer_load_dwordx4 v206, s[40:43], s84 offen offset:128 lds\n\t"
    "s_add_u32 m0, s72, 63232\n\t"
    "s_nop 0\n\t"
    "buffer_load_dwordx4 v206, s[40:43], s84 offen offset:256 lds\n\t"
    "s_waitcnt vmcnt(14)\n\t"
    "s_waitcnt lgkmcnt(0)\n\t"
    "s_barrier\n\t"
    "s_add_u32 m0, s72, 73728\n\t"
    "s_mov_b32 s84, 12288\n\t"
    "buffer_load_dwordx4 v253, s[36:39], s84 offen offset:0 sc1 lds\n\t"
    "buffer_load_dwordx4 v253, s[36:39], s84 offen offset:1024 sc1 lds\n\t"
    "buffer_load_dwordx4 v253, s[36:39], s84 offen offset:2048 sc1 lds\n\t"
    "s_mov_b32 s84, 40960\n\t"
    "s_add_u32 m0, s72, 86016\n\t"
    "s_nop 0\n\t"
    "buffer_load_dwordx4 v206, s[40:43], s84 offen offset:0 lds\n\t"
    "s_add_u32 m0, s72, 86912\n\t"
    "s_nop 0\n\t"
    "buffer_load_dwordx4 v206, s[40:43], s84 offen offset:128 lds\n\t"
    "s_add_u32 m0, s72, 87808\n\t"
    "s_nop 0\n\t"
    "buffer_load_dwordx4 v206, s[40:43], s84 offen offset:256 lds\n\t"
    "s_waitcnt vmcnt(16)\n\t"
    "s_waitcnt lgkmcnt(0)\n\t"
    "s_barrier\n\t"
    "s_add_u32 m0, s72, 0\n\t"
    "s_mov_b32 s84, 24576\n\t"
    "buffer_load_dwordx4 v253, s[36:39], s84 offen offset:0 sc1 lds\n\t"
    "buffer_load_dwordx4 v253, s[36:39], s84 offen offset:1024 sc1 lds\n\t"
    "buffer_load_dwordx4 v253, s[36:39], s84 offen offset:2048 sc1 lds\n\t"
    "s_mov_b32 s84, 81920\n\t"
    "s_add_u32 m0, s72, 12288\n\t"
    "s_nop 0\n\t"
    "buffer_load_dwordx4 v206, s[40:43], s84 offen offset:0 lds\n\t"
    "s_add_u32 m0, s72, 13184\n\t"
    "s_nop 0\n\t"
    "buffer_load_dwordx4 v206, s[40:43], s84 offen offset:128 lds\n\t"
    "s_add_u32 m0, s72, 14080\n\t"
    "s_nop 0\n\t"
    "buffer_load_dwordx4 v206, s[40:43], s84 offen offset:256 lds\n\t"
    "s_waitcnt vmcnt(20)\n\t"
    "s_waitcnt lgkmcnt(0)\n\t"
    "s_barrier\n\t"
    "s_add_u32 m0, s72, 24576\n\t"
    "s_mov_b32 s84, 36864\n\t"
    "buffer_load_dwordx4 v253, s[36:39], s84 offen offset:0 sc1 lds\n\t"
    "buffer_load_dwordx4 v253, s[36:39], s84 offen offset:1024 sc1 lds\n\t"
    "buffer_load_dwordx4 v253, s[36:39], s84 offen offset:2048 sc1 lds\n\t"
    "s_mov_b32 s84, 122880\n\t"
    "s_add_u32 m0, s72, 36864\n\t"
    "s_nop 0\n\t"
    "buffer_load_dwordx4 v206, s[40:43], s84 offen offset:0 lds\n\t"
    "s_add_u32 m0, s72, 37760\n\t"
    "s_nop 0\n\t"
    "buffer_load_dwordx4 v206, s[40:43], s84 offen offset:128 lds\n\t"
    "s_add_u32 m0, s72, 38656\n\t"
    "s_nop 0\n\t"
    "buffer_load_dwordx4 v206, s[40:43], s84 offen offset:256 lds\n\t"
    "s_waitcnt vmcnt(24)\n\t"
    "s_waitcnt lgkmcnt(0)\n\t"
    "s_barrier\n\t"
    "s_waitcnt vmcnt(18)\n\t"
    "s_barrier\n\t"
    "s_waitcnt vmcnt(12)\n\t"
    "s_barrier\n\t"
    "s_bitcmp1_b32 s97, 1\n\t"
    "s_cbranch_scc0 L_pw1_%=\n\t"
    "L_pr1_%=:\n\t"
    "s_add_u32 m0, s72, 49152\n\t"
    "s_mov_b32 s84, 49152\n\t"
    "buffer_load_dwordx4 v253, s[36:39], s84 offen offset:0 sc1 lds\n\t"
    "buffer_load_dwordx4 v253, s[36:39], s84 offen offset:1024 sc1 lds\n\t"
    "buffer_load_dwordx4 v253, s[36:39], s84 offen offset:2048 sc1 lds\n\t"
    "s_mov_b32 s84, 163840\n\t"
    "s_add_u32 m0, s72, 61440\n\t"
    "s_nop 0\n\t"
    "buffer_load_dwordx4 v206, s[40:43], s84 offen offset:0 lds\n\t"
    "s_add_u32 m0, s72, 62336\n\t"
    "s_nop 0\n\t"
    "buffer_load_dwordx4 v206, s[40:43], s84 offen offset:128 lds\n\t"
    "s_add_u32 m0, s72, 63232\n\t"
    "s_nop 0\n\t"
    "buffer_load_dwordx4 v206, s[40:43], s84 offen offset:256 lds\n\t"
    "s_waitcnt vmcnt(12)\n\t"
    "s_barrier\n\t"
    "s_add_u32 m0, s72, 73728\n\t"
    "s_mov_b32 s84, 61440\n\t"
    "buffer_load_dwordx4 v253, s[36:39], s84 offen offset:0 sc1 lds\n\t"
    "buffer_load_dwordx4 v253, s[36:39], s84 offen offset:1024 sc1 lds\n\t"
    "buffer_load_dwordx4 v253, s[36:39], s84 offen offset:2048 sc1 lds\n\t"
    "s_mov_b32 s84, 204800\n\t"
    "s_add_u32 m0, s72, 86016\n\t"
    "s_nop 0\n\t"
    "buffer_load_dwordx4 v206, s[40:43], s84 offen offset:0 lds\n\t"
    "s_add_u32 m0, s72, 86912\n\t"
    "s_nop 0\n\t"
    "buffer_load_dwordx4 v206, s[40:43], s84 offen offset:128 lds\n\t"
    "s_add_u32 m0, s72, 87808\n\t"
    "s_nop 0\n\t"
    "buffer_load_dwordx4 v206, s[40:43], s84 offen offset:256 lds\n\t"
    "s_waitcnt vmcnt(12)\n\t"
    "s_barrier\n\t"
    "s_cmp_lg_u32 s26, 0\n\t"
    "s_cbranch_scc1 L_nfb_%=\n\t"
    "v_mov_b32 v4, s89\n\t"
    "v_lshlrev_b32 v5, 12, v253\n\t"
    "buffer_store_dword v4, v5, s[64:67], s91 offen offset:16 sc1\n\t"
    "s_nop 1\n\t"
    "L_nfb_%=:\n\t"
    "s_add_u32 m0, s72, 0\n\t"
    "s_mov_b32 s84, 73728\n\t"
    "buffer_load_dwordx4 v253, s[36:39], s84 offen offset:0 sc1 lds\n\t"
    "buffer_load_dwordx4 v253, s[36:39], s84 offen offset:1024 sc1 lds\n\t"
    "buffer_load_dwordx4 v253, s[36:39], s84 offen offset:2048 sc1 lds\n\t"
    "s_mov_b32 s84, 245760\n\t"
    "s_add_u32 m0, s72, 12288\n\t"
    "s_nop 0\n\t"
    "buffer_load_dwordx4 v206, s[40:43], s84 offen offset:0 lds\n\t"
    "s_add_u32 m0, s72, 13184\n\t"
    "s_nop 0\n\t"
    "buffer_load_dwordx4 v206, s[40:43], s84 offen offset:128 lds\n\t"
    "s_add_u32 m0, s72, 14080\n\t"
    "s_nop 0\n\t"
    "buffer_load_dwordx4 v206, s[40:43], s84 offen offset:256 lds\n\t"
    "s_waitcnt vmcnt(12)\n\t"
    "s_barrier\n\t"
    "s_add_u32 m0, s72, 24576\n\t"
    "s_mov_b32 s84, 86016\n\t"
    "buffer_load_dwordx4 v253, s[36:39], s84 offen offset:0 sc1 lds\n\t"
    "buffer_load_dwordx4 v253, s[36:39], s84 offen offset:1024 sc1 lds\n\t"
    "buffer_load_dwordx4 v253, s[36:39], s84 offen offset:2048 sc1 lds\n\t"
    "s_mov_b32 s84, 286720\n\t"
    "s_add_u32 m0, s72, 36864\n\t"
    "s_nop 0\n\t"
    "buffer_load_dwordx4 v206, s[40:43], s84 offen offset:0 lds\n\t"
    "s_add_u32 m0, s72, 37760\n\t"
    "s_nop 0\n\t"
    "buffer_load_dwordx4 v206, s[40:43], s84 offen offset:128 lds\n\t"
    "s_add_u32 m0, s72, 38656\n\t"
    "s_nop 0\n\t"
    "buffer_load_dwordx4 v206, s[40:43], s84 offen offset:256 lds\n\t"
    "s_waitcnt vmcnt(12)\n\t"
    "s_barrier\n\t"
    "s_bitcmp1_b32 s97, 2\n\t"
    "s_cbranch_scc0 L_pw2_%=\n\t"
    "L_pr2_%=:\n\t"
    "s_add_u32 m0, s72, 49152\n\t"
    "s_mov_b32 s84, 98304\n\t"
    "buffer_load_dwordx4 v253, s[36:39], s84 offen offset:0 sc1 lds\n\t"
    "buffer_load_dwordx4 v253, s[36:39], s84 offen offset:1024 sc1 lds\n\t"
    "buffer_load_dwordx4 v253, s[36:39], s84 offen offset:2048 sc1 lds\n\t"
    "s_mov_b32 s84, 327680\n\t"
    "s_add_u32 m0, s72, 61440\n\t"
    "s_nop 0\n\t"
    "buffer_load_dwordx4 v206, s[40:43], s84 offen offset:0 lds\n\t"
    "s_add_u32 m0, s72, 62336\n\t"
    "s_nop 0\n\t"
    "buffer_load_dwordx4 v206, s[40:43], s84 offen offset:128 lds\n\t"
    "s_add_u32 m0, s72, 63232\n\t"
    "s_nop 0\n\t"
    "buffer_load_dwordx4 v206, s[40:43], s84 offen offset:256 lds\n\t"
    "s_waitcnt vmcnt(12)\n\t"
    "s_barrier\n\t"
    "s_add_u32 m0, s72, 73728\n\t"
    "s_mov_b32 s84, 110592\n\t"
    "buffer_load_dwordx4 v253, s[36:39], s84 offen offset:0 sc1 lds\n\t"
    "buffer_load_dwordx4 v253, s[36:39], s84 offen offset:1024 sc1 lds\n\t"
    "buffer_load_dwordx4 v253, s[36:39], s84 offen offset:2048 sc1 lds\n\t"
    "s_mov_b32 s84, 368640\n\t"
    "s_add_u32 m0, s72, 86016\n\t"
    "s_nop 0\n\t"
    "buffer_load_dwordx4 v206, s[40:43], s84 offen offset:0 lds\n\t"
    "s_add_u32 m0, s72, 86912\n\t"
    "s_nop 0\n\t"
    "buffer_load_dwordx4 v206, s[40:43], s84 offen offset:128 lds\n\t"
    "s_add_u32 m0, s72, 87808\n\t"
    "s_nop 0\n\t"
    "buffer_load_dwordx4 v206, s[40:43], s84 offen offset:256 lds\n\t"
    "s_waitcnt vmcnt(12)\n\t"
    "s_barrier\n\t"
    "s_add_u32 m0, s72, 0\n\t"
    "s_mov_b32 s84, 122880\n\t"
    "buffer_load_dwordx4 v253, s[36:39], s84 offen offset:0 sc1 lds\n\t"
    "buffer_load_dwordx4 v253, s[36:39], s84 offen offset:1024 sc1 lds\n\t"
    "buffer_load_dwordx4 v253, s[36:39], s84 offen offset:2048 sc1 lds\n\t"
    "s_mov_b32 s84, 409600\n\t"
    "s_add_u32 m0, s72, 12288\n\t"
    "s_nop 0\n\t"
    "buffer_load_dwordx4 v206, s[40:43], s84 offen offset:0 lds\n\t"
    "s_add_u32 m0, s72, 13184\n\t"
    "s_nop 0\n\t"
    "buffer_load_dwordx4 v206, s[40:43], s84 offen offset:128 lds\n\t"
    "s_add_u32 m0, s72, 14080\n\t"
    "s_nop 0\n\t"
    "buffer_load_dwordx4 v206, s[40:43], s84 offen offset:256 lds\n\t"
    "s_waitcnt vmcnt(12)\n\t"
    "s_barrier\n\t"
    "s_add_u32 m0, s72, 24576\n\t"
    "s_mov_b32 s84, 135168\n\t"
    "buffer_load_dwordx4 v253, s[36:39], s84 offen offset:0 sc1 lds\n\t"
    "buffer_load_dwordx4 v253, s[36:39], s84 offen offset:1024 sc1 lds\n\t"
    "buffer_load_dwordx4 v253, s[36:39], s84 offen offset:2048 sc1 lds\n\t"
    "s_mov_b32 s84, 450560\n\t"
    "s_add_u32 m0, s72, 36864\n\t"
    "s_nop 0\n\t"
    "buffer_load_dwordx4 v206, s[40:43], s84 offen offset:0 lds\n\t"
    "s_add_u32 m0, s72, 37760\n\t"
    "s_nop 0\n\t"
    "buffer_load_dwordx4 v206, s[40:43], s84 offen offset:128 lds\n\t"
    "s_add_u32 m0, s72, 38656\n\t"
    "s_nop 0\n\t"
    "buffer_load_dwordx4 v206, s[40:43], s84 offen offset:256 lds\n\t"
    "s_waitcnt vmcnt(12)\n\t"
    "s_barrier\n\t"
    "s_bitcmp1_b32 s97, 3\n\t"
    "s_cbranch_scc0 L_pw3_%=\n\t"
    "L_pr3_%=:\n\t"
    "s_add_u32 m0, s72, 49152\n\t"
    "s_mov_b32 s84, 147456\n\t"
    "buffer_load_dwordx4 v253, s[36:39], s84 offen offset:0 sc1 lds\n\t"
    "buffer_load_dwordx4 v253, s[36:39], s84 offen offset:1024 sc1 lds\n\t"
    "buffer_load_dwordx4 v253, s[36:39], s84 offen offset:2048 sc1 lds\n\t"
    "s_mov_b32 s84, 491520\n\t"
    "s_add_u32 m0, s72, 61440\n\t"
    "s_nop 0\n\t"
    "buffer_load_dwordx4 v206, s[40:43], s84 offen offset:0 lds\n\t"
    "s_add_u32 m0, s72, 62336\n\t"
    "s_nop 0\n\t"
    "buffer_load_dwordx4 v206, s[40:43], s84 offen offset:128 lds\n\t"
    "s_add_u32 m0, s72, 63232\n\t"
    "s_nop 0\n\t"
    "buffer_load_dwordx4 v206, s[40:43], s84 offen offset:256 lds\n\t"
    "s_waitcnt vmcnt(12)\n\t"
    "s_barrier\n\t"
    "s_add_u32 m0, s72, 73728\n\t"
    "s_mov_b32 s84, 159744\n\t"
    "buffer_load_dwordx4 v253, s[36:39], s84 offen offset:0 sc1 lds\n\t"
    "buffer_load_dwordx4 v253, s[36:39], s84 offen offset:1024 sc1 lds\n\t"
    "buffer_load_dwordx4 v253, s[36:39], s84 offen offset:2048 sc1 lds\n\t"
    "s_mov_b32 s84, 532480\n\t"
    "s_add_u32 m0, s72, 86016\n\t"
    "s_nop 0\n\t"
    "buffer_load_dwordx4 v206, s[40:43], s84 offen offset:0 lds\n\t"
    "s_add_u32 m0, s72, 86912\n\t"
    "s_nop 0\n\t"
    "buffer_load_dwordx4 v206, s[40:43], s84 offen offset:128 lds\n\t"
    "s_add_u32 m0, s72, 87808\n\t"
    "s_nop 0\n\t"
    "buffer_load_dwordx4 v206, s[40:43], s84 offen offset:256 lds\n\t"
    "s_waitcnt vmcnt(12)\n\t"
    "s_barrier\n\t"
    "s_add_u32 m0, s72, 0\n\t"
    "s_mov_b32 s84, 172032\n\t"
    "buffer_load_dwordx4 v253, s[36:39], s84 offen offset:0 sc1 lds\n\t"
    "buffer_load_dwordx4 v253, s[36:39], s84 offen offset:1024 sc1 lds\n\t"
    "buffer_load_dwordx4 v253, s[36:39], s84 offen offset:2048 sc1 lds\n\t"
    "s_mov_b32 s84, 573440\n\t"
    "s_add_u32 m0, s72, 12288\n\t"
    "s_nop 0\n\t"
    "buffer_load_dwordx4 v206, s[40:43], s84 offen offset:0 lds\n\t"
    "s_add_u32 m0, s72, 13184\n\t"
    "s_nop 0\n\t"
    "buffer_load_dwordx4 v206, s[40:43], s84 offen offset:128 lds\n\t"
    "s_add_u32 m0, s72, 14080\n\t"
    "s_nop 0\n\t"
    "buffer_load_dwordx4 v206, s[40:43], s84 offen offset:256 lds\n\t"
    "s_waitcnt vmcnt(12)\n\t"
    "s_barrier\n\t"
    "s_add_u32 m0, s72, 24576\n\t"
    "s_mov_b32 s84, 184320\n\t"
    "buffer_load_dwordx4 v253, s[36:39], s84 offen offset:0 sc1 lds\n\t"
    "buffer_load_dwordx4 v253, s[36:39], s84 offen offset:1024 sc1 lds\n\t"
    "buffer_load_dwordx4 v253, s[36:39], s84 offen offset:2048 sc1 lds\n\t"
    "s_mov_b32 s84, 614400\n\t"
    "s_add_u32 m0, s72, 36864\n\t"
    "s_nop 0\n\t"
    "buffer_load_dwordx4 v206, s[40:43], s84 offen offset:0 lds\n\t"
    "s_add_u32 m0, s72, 37760\n\t"
    "s_nop 0\n\t"
    "buffer_load_dwordx4 v206, s[40:43], s84 offen offset:128 lds\n\t"
    "s_add_u32 m0, s72, 38656\n\t"
    "s_nop 0\n\t"
    "buffer_load_dwordx4 v206, s[40:43], s84 offen offset:256 lds\n\t"
    "s_waitcnt vmcnt(12)\n\t"
    "s_barrier\n\t"
    "s_bitcmp1_b32 s97, 4\n\t"
    "s_cbranch_scc0 L_pw4_%=\n\t"
    "L_pr4_%=:\n\t"
    "s_add_u32 m0, s72, 49152\n\t"
    "s_mov_b32 s84, 196608\n\t"
    "buffer_load_dwordx4 v253, s[36:39], s84 offen offset:0 sc1 lds\n\t"
    "buffer_load_dwordx4 v253, s[36:39], s84 offen offset:1024 sc1 lds\n\t"
    "buffer_load_dwordx4 v253, s[36:39], s84 offen offset:2048 sc1 lds\n\t"
    "s_mov_b32 s84, 655360\n\t"
    "s_add_u32 m0, s72, 61440\n\t"
    "s_nop 0\n\t"
    "buffer_load_dwordx4 v206, s[40:43], s84 offen offset:0 lds\n\t"
    "s_add_u32 m0, s72, 62336\n\t"
    "s_nop 0\n\t"
    "buffer_load_dwordx4 v206, s[40:43], s84 offen offset:128 lds\n\t"
    "s_add_u32 m0, s72, 63232\n\t"
    "s_nop 0\n\t"
    "buffer_load_dwordx4 v206, s[40:43], s84 offen offset:256 lds\n\t"
    "s_waitcnt vmcnt(12)\n\t"
    "s_barrier\n\t"
    "s_add_u32 m0, s72, 73728\n\t"
    "s_mov_b32 s84, 208896\n\t"
    "buffer_load_dwordx4 v253, s[36:39], s84 offen offset:0 sc1 lds\n\t"
    "buffer_load_dwordx4 v253, s[36:39], s84 offen offset:1024 sc1 lds\n\t"
    "buffer_load_dwordx4 v253, s[36:39], s84 offen offset:2048 sc1 lds\n\t"
    "s_mov_b32 s84, 696320\n\t"
    "s_add_u32 m0, s72, 86016\n\t"
    "s_nop 0\n\t"
    "buffer_load_dwordx4 v206, s[40:43], s84 offen offset:0 lds\n\t"
    "s_add_u32 m0, s72, 86912\n\t"
    "s_nop 0\n\t"
    "buffer_load_dwordx4 v206, s[40:43], s84 offen offset:128 lds\n\t"
    "s_add_u32 m0, s72, 87808\n\t"
    "s_nop 0\n\t"
    "buffer_load_dwordx4 v206, s[40:43], s84 offen offset:256 lds\n\t"
    "s_waitcnt vmcnt(12)\n\t"
    "s_barrier\n\t"
    "s_add_u32 m0, s72, 0\n\t"
    "s_mov_b32 s84, 221184\n\t"
    "buffer_load_dwordx4 v253, s[36:39], s84 offen offset:0 sc1 lds\n\t"
    "buffer_load_dwordx4 v253, s[36:39], s84 offen offset:1024 sc1 lds\n\t"
    "buffer_load_dwordx4 v253, s[36:39], s84 offen offset:2048 sc1 lds\n\t"
    "s_mov_b32 s84, 737280\n\t"
    "s_add_u32 m0, s72, 12288\n\t"
    "s_nop 0\n\t"
    "buffer_load_dwordx4 v206, s[40:43], s84 offen offset:0 lds\n\t"
    "s_add_u32 m0, s72, 13184\n\t"
    "s_nop 0\n\t"
    "buffer_load_dwordx4 v206, s[40:43], s84 offen offset:128 lds\n\t"
    "s_add_u32 m0, s72, 14080\n\t"
    "s_nop 0\n\t"
    "buffer_load_dwordx4 v206, s[40:43], s84 offen offset:256 lds\n\t"
    "s_waitcnt vmcnt(12)\n\t"
    "s_barrier\n\t"
    "s_add_u32 m0, s72, 24576\n\t"
    "s_mov_b32 s84, 233472\n\t"
    "buffer_load_dwordx4 v253, s[36:39], s84 offen offset:0 sc1 lds\n\t"
    "buffer_load_dwordx4 v253, s[36:39], s84 offen offset:1024 sc1 lds\n\t"
    "buffer_load_dwordx4 v253, s[36:39], s84 offen offset:2048 sc1 lds\n\t"
    "s_mov_b32 s84, 778240\n\t"
    "s_add_u32 m0, s72, 36864\n\t"
    "s_nop 0\n\t"
    "buffer_load_dwordx4 v206, s[40:43], s84 offen offset:0 lds\n\t"
    "s_add_u32 m0, s72, 37760\n\t"
    "s_nop 0\n\t"
    "buffer_load_dwordx4 v206, s[40:43], s84 offen offset:128 lds\n\t"
    "s_add_u32 m0, s72, 38656\n\t"
    "s_nop 0\n\t"
    "buffer_load_dwordx4 v206, s[40:43], s84 offen offset:256 lds\n\t"
    "s_waitcnt vmcnt(12)\n\t"
    "s_barrier\n\t"
    "s_bitcmp1_b32 s97, 5\n\t"
    "s_cbranch_scc0 L_pw5_%=\n\t"
    "L_pr5_%=:\n\t"
    "s_add_u32 m0, s72, 49152\n\t"
    "s_mov_b32 s84, 245760\n\t"
    "buffer_load_dwordx4 v253, s[36:39], s84 offen offset:0 sc1 lds\n\t"
    "buffer_load_dwordx4 v253, s[36:39], s84 offen offset:1024 sc1 lds\n\t"
    "buffer_load_dwordx4 v253, s[36:39], s84 offen offset:2048 sc1 lds\n\t"
    "s_mov_b32 s84, 819200\n\t"
    "s_add_u32 m0, s72, 61440\n\t"
    "s_nop 0\n\t"
    "buffer_load_dwordx4 v206, s[40:43], s84 offen offset:0 lds\n\t"
    "s_add_u32 m0, s72, 62336\n\t"
    "s_nop 0\n\t"
    "buffer_load_dwordx4 v206, s[40:43], s84 offen offset:128 lds\n\t"
    "s_add_u32 m0, s72, 63232\n\t"
    "s_nop 0\n\t"
    "buffer_load_dwordx4 v206, s[40:43], s84 offen offset:256 lds\n\t"
    "s_waitcnt vmcnt(12)\n\t"
    "s_barrier\n\t"
    "s_add_u32 m0, s72, 73728\n\t"
    "s_mov_b32 s84, 258048\n\t"
    "buffer_load_dwordx4 v253, s[36:39], s84 offen offset:0 sc1 lds\n\t"
    "buffer_load_dwordx4 v253, s[36:39], s84 offen offset:1024 sc1 lds\n\t"
    "buffer_load_dwordx4 v253, s[36:39], s84 offen offset:2048 sc1 lds\n\t"
    "s_mov_b32 s84, 860160\n\t"
    "s_add_u32 m0, s72, 86016\n\t"
    "s_nop 0\n\t"
    "buffer_load_dwordx4 v206, s[40:43], s84 offen offset:0 lds\n\t"
    "s_add_u32 m0, s72, 86912\n\t"
    "s_nop 0\n\t"
    "buffer_load_dwordx4 v206, s[40:43], s84 offen offset:128 lds\n\t"
    "s_add_u32 m0, s72, 87808\n\t"
    "s_nop 0\n\t"
    "buffer_load_dwordx4 v206, s[40:43], s84 offen offset:256 lds\n\t"
    "s_waitcnt vmcnt(12)\n\t"
    "s_barrier\n\t"
    "s_add_u32 m0, s72, 0\n\t"
    "s_mov_b32 s84, 270336\n\t"
    "buffer_load_dwordx4 v253, s[36:39], s84 offen offset:0 sc1 lds\n\t"
    "buffer_load_dwordx4 v253, s[36:39], s84 offen offset:1024 sc1 lds\n\t"
    "buffer_load_dwordx4 v253, s[36:39], s84 offen offset:2048 sc1 lds\n\t"
    "s_mov_b32 s84, 901120\n\t"
    "s_add_u32 m0, s72, 12288\n\t"
    "s_nop 0\n\t"
    "buffer_load_dwordx4 v206, s[40:43], s84 offen offset:0 lds\n\t"
    "s_add_u32 m0, s72, 13184\n\t"
    "s_nop 0\n\t"
    "buffer_load_dwordx4 v206, s[40:43], s84 offen offset:128 lds\n\t"
    "s_add_u32 m0, s72, 14080\n\t"
    "s_nop 0\n\t"
    "buffer_load_dwordx4 v206, s[40:43], s84 offen offset:256 lds\n\t"
    "s_waitcnt vmcnt(12)\n\t"
    "s_barrier\n\t"
    "s_add_u32 m0, s72, 24576\n\t"
    "s_mov_b32 s84, 282624\n\t"
    "buffer_load_dwordx4 v253, s[36:39], s84 offen offset:0 sc1 lds\n\t"
    "buffer_load_dwordx4 v253, s[36:39], s84 offen offset:1024 sc1 lds\n\t"
    "buffer_load_dwordx4 v253, s[36:39], s84 offen offset:2048 sc1 lds\n\t"
    "s_mov_b32 s84, 942080\n\t"
    "s_add_u32 m0, s72, 36864\n\t"
    "s_nop 0\n\t"
    "buffer_load_dwordx4 v206, s[40:43], s84 offen offset:0 lds\n\t"
    "s_add_u32 m0, s72, 37760\n\t"
    "s_nop 0\n\t"
    "buffer_load_dwordx4 v206, s[40:43], s84 offen offset:128 lds\n\t"
    "s_add_u32 m0, s72, 38656\n\t"
    "s_nop 0\n\t"
    "buffer_load_dwordx4 v206, s[40:43], s84 offen offset:256 lds\n\t"
    "s_waitcnt vmcnt(12)\n\t"
    "s_barrier\n\t"
    "s_bitcmp1_b32 s97, 6\n\t"
    "s_cbranch_scc0 L_pw6_%=\n\t"
    "L_pr6_%=:\n\t"
    "s_add_u32 m0, s72, 49152\n\t"
    "s_mov_b32 s84, 294912\n\t"
    "buffer_load_dwordx4 v253, s[36:39], s84 offen offset:0 sc1 lds\n\t"
    "buffer_load_dwordx4 v253, s[36:39], s84 offen offset:1024 sc1 lds\n\t"
    "buffer_load_dwordx4 v253, s[36:39], s84 offen offset:2048 sc1 lds\n\t"
    "s_mov_b32 s84, 983040\n\t"
    "s_add_u32 m0, s72, 61440\n\t"
    "s_nop 0\n\t"
    "buffer_load_dwordx4 v206, s[40:43], s84 offen offset:0 lds\n\t"
    "s_add_u32 m0, s72, 62336\n\t"
    "s_nop 0\n\t"
    "buffer_load_dwordx4 v206, s[40:43], s84 offen offset:128 lds\n\t"
    "s_add_u32 m0, s72, 63232\n\t"
    "s_nop 0\n\t"
    "buffer_load_dwordx4 v206, s[40:43], s84 offen offset:256 lds\n\t"
    "s_waitcnt vmcnt(12)\n\t"
    "s_barrier\n\t"
    "s_add_u32 m0, s72, 73728\n\t"
    "s_mov_b32 s84, 307200\n\t"
    "buffer_load_dwordx4 v253, s[36:39], s84 offen offset:0 sc1 lds\n\t"
    "buffer_load_dwordx4 v253, s[36:39], s84 offen offset:1024 sc1 lds\n\t"
    "buffer_load_dwordx4 v253, s[36:39], s84 offen offset:2048 sc1 lds\n\t"
    "s_mov_b32 s84, 1024000\n\t"
    "s_add_u32 m0, s72, 86016\n\t"
    "s_nop 0\n\t"
    "buffer_load_dwordx4 v206, s[40:43], s84 offen offset:0 lds\n\t"
    "s_add_u32 m0, s72, 86912\n\t"
    "s_nop 0\n\t"
    "buffer_load_dwordx4 v206, s[40:43], s84 offen offset:128 lds\n\t"
    "s_add_u32 m0, s72, 87808\n\t"
    "s_nop 0\n\t"
    "buffer_load_dwordx4 v206, s[40:43], s84 offen offset:256 lds\n\t"
    "s_waitcnt vmcnt(12)\n\t"
    "s_barrier\n\t"
    "s_add_u32 m0, s72, 0\n\t"
    "s_mov_b32 s84, 319488\n\t"
    "buffer_load_dwordx4 v253, s[36:39], s84 offen offset:0 sc1 lds\n\t"
    "buffer_load_dwordx4 v253, s[36:39], s84 offen offset:1024 sc1 lds\n\t"
    "buffer_load_dwordx4 v253, s[36:39], s84 offen offset:2048 sc1 lds\n\t"
    "s_mov_b32 s84, 1064960\n\t"
    "s_add_u32 m0, s72, 12288\n\t"
    "s_nop 0\n\t"
    "buffer_load_dwordx4 v206, s[40:43], s84 offen offset:0 lds\n\t"
    "s_add_u32 m0, s72, 13184\n\t"
    "s_nop 0\n\t"
    "buffer_load_dwordx4 v206, s[40:43], s84 offen offset:128 lds\n\t"
    "s_add_u32 m0, s72, 14080\n\t"
    "s_nop 0\n\t"
    "buffer_load_dwordx4 v206, s[40:43], s84 offen offset:256 lds\n\t"
    "s_waitcnt vmcnt(12)\n\t"
    "s_barrier\n\t"
    "s_add_u32 m0, s72, 24576\n\t"
    "s_mov_b32 s84, 331776\n\t"
    "buffer_load_dwordx4 v253, s[36:39], s84 offen offset:0 sc1 lds\n\t"
    "buffer_load_dwordx4 v253, s[36:39], s84 offen offset:1024 sc1 lds\n\t"
    "buffer_load_dwordx4 v253, s[36:39], s84 offen offset:2048 sc1 lds\n\t"
    "s_mov_b32 s84, 1105920\n\t"
    "s_add_u32 m0, s72, 36864\n\t"
    "s_nop 0\n\t"
    "buffer_load_dwordx4 v206, s[40:43], s84 offen offset:0 lds\n\t"
    "s_add_u32 m0, s72, 37760\n\t"
    "s_nop 0\n\t"
    "buffer_load_dwordx4 v206, s[40:43], s84 offen offset:128 lds\n\t"
    "s_add_u32 m0, s72, 38656\n\t"
    "s_nop 0\n\t"
    "buffer_load_dwordx4 v206, s[40:43], s84 offen offset:256 lds\n\t"
    "s_waitcnt vmcnt(12)\n\t"
    "s_barrier\n\t"
    "s_bitcmp1_b32 s97, 7\n\t"
    "s_cbranch_scc0 L_pw7_%=\n\t"
    "L_pr7_%=:\n\t"
    "s_add_u32 m0, s72, 49152\n\t"
    "s_mov_b32 s84, 344064\n\t"
    "buffer_load_dwordx4 v253, s[36:39], s84 offen offset:0 sc1 lds\n\t"
    "buffer_load_dwordx4 v253, s[36:39], s84 offen offset:1024 sc1 lds\n\t"
    "buffer_load_dwordx4 v253, s[36:39], s84 offen offset:2048 sc1 lds\n\t"
    "s_mov_b32 s84, 1146880\n\t"
    "s_add_u32 m0, s72, 61440\n\t"
    "s_nop 0\n\t"
    "buffer_load_dwordx4 v206, s[40:43], s84 offen offset:0 lds\n\t"
    "s_add_u32 m0, s72, 62336\n\t"
    "s_nop 0\n\t"
    "buffer_load_dwordx4 v206, s[40:43], s84 offen offset:128 lds\n\t"
    "s_add_u32 m0, s72, 63232\n\t"
    "s_nop 0\n\t"
    "buffer_load_dwordx4 v206, s[40:43], s84 offen offset:256 lds\n\t"
    "s_waitcnt vmcnt(12)\n\t"
    "s_barrier\n\t"
    "s_add_u32 m0, s72, 73728\n\t"
    "s_mov_b32 s84, 356352\n\t"
    "buffer_load_dwordx4 v253, s[36:39], s84 offen offset:0 sc1 lds\n\t"
    "buffer_load_dwordx4 v253, s[36:39], s84 offen offset:1024 sc1 lds\n\t"
    "buffer_load_dwordx4 v253, s[36:39], s84 offen offset:2048 sc1 lds\n\t"
    "s_mov_b32 s84, 1187840\n\t"
    "s_add_u32 m0, s72, 86016\n\t"
    "s_nop 0\n\t"
    "buffer_load_dwordx4 v206, s[40:43], s84 offen offset:0 lds\n\t"
    "s_add_u32 m0, s72, 86912\n\t"
    "s_nop 0\n\t"
    "buffer_load_dwordx4 v206, s[40:43], s84 offen offset:128 lds\n\t"
    "s_add_u32 m0, s72, 87808\n\t"
    "s_nop 0\n\t"
    "buffer_load_dwordx4 v206, s[40:43], s84 offen offset:256 lds\n\t"
    "s_waitcnt vmcnt(12)\n\t"
    "s_barrier\n\t"
    "s_add_u32 m0, s72, 0\n\t"
    "s_mov_b32 s84, 368640\n\t"
    "buffer_load_dwordx4 v253, s[36:39], s84 offen offset:0 sc1 lds\n\t"
    "buffer_load_dwordx4 v253, s[36:39], s84 offen offset:1024 sc1 lds\n\t"
    "buffer_load_dwordx4 v253, s[36:39], s84 offen offset:2048 sc1 lds\n\t"
    "s_mov_b32 s84, 1228800\n\t"
    "s_add_u32 m0, s72, 12288\n\t"
    "s_nop 0\n\t"
    "buffer_load_dwordx4 v206, s[40:43], s84 offen offset:0 lds\n\t"
    "s_add_u32 m0, s72, 13184\n\t"
    "s_nop 0\n\t"
    "buffer_load_dwordx4 v206, s[40:43], s84 offen offset:128 lds\n\t"
    "s_add_u32 m0, s72, 14080\n\t"
    "s_nop 0\n\t"
    "buffer_load_dwordx4 v206, s[40:43], s84 offen offset:256 lds\n\t"
    "s_waitcnt vmcnt(12)\n\t"
    "s_barrier\n\t"
    "s_add_u32 m0, s72, 24576\n\t"
    "s_mov_b32 s84, 380928\n\t"
    "buffer_load_dwordx4 v253, s[36:39], s84 offen offset:0 sc1 lds\n\t"
    "buffer_load_dwordx4 v253, s[36:39], s84 offen offset:1024 sc1 lds\n\t"
    "buffer_load_dwordx4 v253, s[36:39], s84 offen offset:2048 sc1 lds\n\t"
    "s_mov_b32 s84, 1269760\n\t"
    "s_add_u32 m0, s72, 36864\n\t"
    "s_nop 0\n\t"
    "buffer_load_dwordx4 v206, s[40:43], s84 offen offset:0 lds\n\t"
    "s_add_u32 m0, s72, 37760\n\t"
    "s_nop 0\n\t"
    "buffer_load_dwordx4 v206, s[40:43], s84 offen offset:128 lds\n\t"
    "s_add_u32 m0, s72, 38656\n\t"
    "s_nop 0\n\t"
    "buffer_load_dwordx4 v206, s[40:43], s84 offen offset:256 lds\n\t"
    "s_waitcnt vmcnt(12)\n\t"
    "s_barrier\n\t"
    "s_waitcnt vmcnt(6)\n\t"
    "s_barrier\n\t"
    "s_waitcnt vmcnt(0)\n\t"
    "s_barrier\n\t"
    "v_mov_b32 v252, v253\n\t"
    "s_lshr_b32 s79, s35, 3\n\t"
    "s_mul_i32 s84, s29, 7864320\n\t"
    "s_mul_i32 s87, s30, 983040\n\t"
    "s_add_u32 s84, s84, s87\n\t"
    "s_mul_i32 s87, s79, 245760\n\t"
    "s_add_u32 s84, s84, s87\n\t"
    "s_mul_i32 s87, s26, 61440\n\t"
    "s_add_u32 s84, s84, s87\n\t"
    "s_mul_i32 s87, s34, 15360\n\t"
    "s_add_u32 s84, s84, s87\n\t"
    "s_add_u32 s84, s84, 100663296\n\t"
    "s_add_u32 s56, s22, s84\n\t"
    "s_addc_u32 s57, s23, 0\n\t"
    "s_and_b32 s57, s57, 0xffff\n\t"
    "s_mov_b32 s58, 15360\n\t"
    "s_mov_b32 s59, 0x00020000\n\t"
    "s_mul_i32 s84, s29, 7864320\n\t"
    "s_mul_i32 s87, s30, 983040\n\t"
    "s_add_u32 s84, s84, s87\n\t"
    "s_mul_i32 s87, s79, 245760\n\t"
    "s_add_u32 s84, s84, s87\n\t"
    "s_mul_i32 s87, s34, 61440\n\t"
    "s_add_u32 s84, s84, s87\n\t"
    "s_add_u32 s84, s84, 100663296\n\t"
    "s_add_u32 s60, s22, s84\n\t"
    "s_addc_u32 s61, s23, 0\n\t"
    "s_and_b32 s61, s61, 0xffff\n\t"
    "s_mov_b32 s62, 61440\n\t"
    "s_mov_b32 s63, 0x00020000\n\t"
    "s_lshr_b32 s85, s34, 1\n\t"
    "s_and_b32 s86, s34, 1\n\t"
    "s_mul_i32 s84, s30, 491520\n\t"
    "s_mul_i32 s87, s85, 245760\n\t"
    "s_add_u32 s84, s84, s87\n\t"
    "s_mul_i32 s87, s29, 1280\n\t"
    "s_add_u32 s84, s84, s87\n\t"
    "s_mul_i32 s87, s79, 320\n\t"
    "s_add_u32 s84, s84, s87\n\t"
    "s_mul_i32 s87, s86, 160\n\t"
    "s_add_u32 s84, s84, s87\n\t"
    "s_add_u32 s44, s24, s84\n\t"
    "s_addc_u32 s45, s25, 0\n\t"
    "s_and_b32 s45, s45, 0xffff\n\t"
    "s_mov_b32 s46, 243360\n\t"
    "s_mov_b32 s47, 0x00020000\n\t"
    "s_mul_i32 s78, s29, 512\n\t"
    "s_mul_i32 s87, s30, 64\n\t"
    "s_add_u32 s78, s78, s87\n\t"
    "s_mul_i32 s87, s79, 16\n\t"
    "s_add_u32 s78, s78, s87\n\t"
    "s_add_u32 s78, s78, 4096\n\t"
    "s_barrier\n\t"
    "s_cmp_lg_u32 s26, 0\n\t"
    "s_cbranch_scc1 L_xch_%=\n\t"
    "s_lshl_b32 s84, s34, 2\n\t"
    "s_add_u32 s84, s84, s78\n\t"
    "v_mov_b32 v4, s89\n\t"
    "v_lshlrev_b32 v5, 12, v252\n\t"
    "buffer_store_dword v4, v5, s[64:67], s84 offen sc1\n\t"
    "s_nop 1\n\t"
    "s_branch L_xch_%=\n\t"
    "L_comp_%=:\n\t"
    "s_setprio 2\n\t"
    "v_bfe_u32 v255, v253, 4, 1\n\t"
    "v_lshlrev_b32 v255, 5, v255\n\t"
    "v_lshrrev_b32 v247, 4, v253\n\t"
    "v_and_b32 v248, 15, v253\n\t"
    "v_lshrrev_b32 v254, 1, v248\n\t"
    "v_xor_b32 v254, v254, v247\n\t"
    "v_lshlrev_b32 v254, 4, v254\n\t"
    "v_and_b32 v245, 7, v248\n\t"
    "v_lshl_add_u32 v254, v245, 7, v254\n\t"
    "v_lshrrev_b32 v249, 3, v248\n\t"
    "v_lshl_add_u32 v245, v249, 11, v254\n\t"
    "v_add_u32 v245, s27, v245\n\t"
    "s_mul_i32 s84, s26, 6144\n\t"
    "s_add_u32 s84, s84, 114688\n\t"
    "s_add_u32 s84, s84, s27\n\t"
    "v_lshl_add_u32 v252, v249, 10, v254\n\t"
    "v_add_u32 v252, s84, v252\n\t"
    "v_lshrrev_b32 v254, 2, v248\n\t"
    "v_lshlrev_b32 v254, 6, v254\n\t"
    "v_and_b32 v250, 3, v253\n\t"
    "v_lshl_add_u32 v246, v255, 3, v255\n\t"
    "v_add_u32 v246, v246, v254\n\t"
    "v_lshl_add_u32 v246, v250, 3, v246\n\t"
    "v_lshrrev_b32 v249, 1, v247\n\t"
    "v_lshl_add_u32 v246, v249, 11, v246\n\t"
    "v_add_u32 v246, s27, v246\n\t"
    "v_lshl_add_u32 v251, v247, 11, v254\n\t"
    "v_lshlrev_b32 v254, 4, v250\n\t"
    "v_xor_b32 v254, v254, v255\n\t"
    "v_add_u32 v251, v251, v254\n\t"
    "v_lshrrev_b32 v254, 1, v250\n\t"
    "v_lshl_add_u32 v251, v254, 3, v251\n\t"
    "s_add_u32 s84, s27, 65536\n\t"
    "v_add_u32 v251, s84, v251\n\t"
    "v_lshlrev_b32 v253, 4, v253\n\t"
    "s_mov_b32 s79, 0x41000000\n\t"
    "s_mov_b32 s80, 0x3e0293ee\n\t"
    "s_mov_b32 s81, 0x3e0293ee\n\t"
    "s_mov_b32 s78, 0\n\t"
    "s_mov_b32 s76, 0\n\t"
    "s_mov_b32 s88, 0\n\t"
    "s_waitcnt lgkmcnt(0)\n\t"
    "s_add_u32 s64, s22, 50331648\n\t"
    "s_addc_u32 s65, s23, 0\n\t"
    "s_and_b32 s65, s65, 0xffff\n\t"
    "s_mov_b32 s66, 8196\n\t"
    "s_mov_b32 s67, 0x00020000\n\t"
    "s_mov_b32 s84, 8192\n\t"
    "buffer_load_dword v0, off, s[64:67], s84 sc1\n\t"
    "s_mul_i32 s84, s29, 12582912\n\t"
    "s_mul_i32 s87, s30, 1572864\n\t"
    "s_add_u32 s84, s84, s87\n\t"
    "s_mul_i32 s87, s31, 49152\n\t"
    "s_add_u32 s84, s84, s87\n\t"
    "s_mul_i32 s87, s26, 3072\n\t"
    "s_add_u32 s84, s84, s87\n\t"
    "s_add_u32 s56, s22, s84\n\t"
    "s_addc_u32 s57, s23, 0\n\t"
    "s_and_b32 s57, s57, 0xffff\n\t"
    "s_mov_b32 s58, 39936\n\t"
    "s_mov_b32 s59, 0x00020000\n\t"
    "s_waitcnt vmcnt(0)\n\t"
    "v_readfirstlane_b32 s89, v0\n\t"
    "s_nop 3\n\t"
    "s_add_u32 s89, s89, 0x9e3779b9\n\t"
    "v_xor_b32 v249, 8, v251\n\t"
    "v_xor_b32 v250, 64, v252\n\t"
    "s_mul_i32 s84, s26, 6144\n\t"
    "s_add_u32 s84, s84, 114688\n\t"
    "s_add_u32 s84, s84, s27\n\t"
    "v_add_u32 v254, s84, v253\n\t"
    "s_waitcnt lgkmcnt(0)\n\t"
    "s_barrier\n\t"
    "ds_read_b128 v[192:195], v252 offset:0\n\t"
    "ds_read_b128 v[200:203], v252 offset:2048\n\t"
    "ds_read_b128 v[208:211], v252 offset:4096\n\t"
    "ds_read_b64_tr_b16 v[216:217], v251 offset:0\n\t"
    "ds_read_b64_tr_b16 v[218:219], v251 offset:256\n\t"
    "ds_read_b64_tr_b16 v[220:221], v249 offset:0\n\t"
    "ds_read_b64_tr_b16 v[222:223], v249 offset:256\n\t"
    "ds_read_b64_tr_b16 v[224:225], v251 offset:512\n\t"
    "ds_read_b64_tr_b16 v[226:227], v251 offset:768\n\t"
    "ds_read_b64_tr_b16 v[228:229], v249 offset:512\n\t"
    "ds_read_b64_tr_b16 v[230:231], v249 offset:768\n\t"
    "s_waitcnt lgkmcnt(6)\n\t"
    "v_mfma_f32_16x16x32_bf16 v[96:99], v[216:219], v[192:195], 0\n\t"
    "ds_read_b64_tr_b16 v[232:233], v251 offset:1024\n\t"
    "v_mfma_f32_16x16x32_bf16 v[100:103], v[216:219], v[200:203], 0\n\t"
    "ds_read_b64_tr_b16 v[234:235], v251 offset:1280\n\t"
    "v_mfma_f32_16x16x32_bf16 v[104:107], v[216:219], v[208:211], 0\n\t"
    "ds_read_b128 v[196:199], v250 offset:0\n\t"
    "s_waitcnt lgkmcnt(7)\n\t"
    "v_mfma_f32_16x16x32_bf16 v[108:111], v[220:223], v[192:195], 0\n\t"
    "ds_read_b64_tr_b16 v[236:237], v249 offset:1024\n\t"
    "v_mfma_f32_16x16x32_bf16 v[112:115], v[220:223], v[200:203], 0\n\t"
    "ds_read_b64_tr_b16 v[238:239], v249 offset:1280\n\t"
    "v_mfma_f32_16x16x32_bf16 v[116:119], v[220:223], v[208:211], 0\n\t"
    "ds_read_b128 v[204:207], v250 offset:2048\n\t"
    "s_waitcnt lgkmcnt(8)\n\t"
    "v_mfma_f32_16x16x32_bf16 v[120:123], v[224:227], v[192:195], 0\n\t"
    "ds_read_b64_tr_b16 v[240:241], v251 offset:1536\n\t"
    "v_mfma_f32_16x16x32_bf16 v[124:127], v[224:227], v[200:203], 0\n\t"
    "ds_read_b64_tr_b16 v[242:243], v251 offset:1792\n\t"
    "v_mfma_f32_16x16x32_bf16 v[128:131], v[224:227], v[208:211], 0\n\t"
    "ds_read_b128 v[212:215], v250 offset:4096\n\t"
    "s_waitcnt lgkmcnt(9)\n\t"
    "v_mfma_f32_16x16x32_bf16 v[132:135], v[228:231], v[192:195], 0\n\t"
    "ds_read_b64_tr_b16 v[216:217], v249 offset:1536\n\t"
    "v_mfma_f32_16x16x32_bf16 v[136:139], v[228:231], v[200:203], 0\n\t"
    "ds_read_b64_tr_b16 v[218:219], v249 offset:1792\n\t"
    "v_mfma_f32_16x16x32_bf16 v[140:143], v[228:231], v[208:211], 0\n\t"
    "s_waitcnt lgkmcnt(9)\n\t"
    "v_mfma_f32_16x16x32_bf16 v[144:147], v[232:235], v[192:195], 0\n\t"
    "ds_read_b64_tr_b16 v[220:221], v251 offset:8192\n\t"
    "v_mfma_f32_16x16x32_bf16 v[148:151], v[232:235], v[200:203], 0\n\t"
    "ds_read_b64_tr_b16 v[222:223], v251 offset:8448\n\t"
    "v_mfma_f32_16x16x32_bf16 v[152:155], v[232:235], v[208:211], 0\n\t"
    "s_waitcnt lgkmcnt(8)\n\t"
    "v_mfma_f32_16x16x32_bf16 v[156:159], v[236:239], v[192:195], 0\n\t"
    "ds_read_b64_tr_b16 v[224:225], v249 offset:8192\n\t"
    "v_mfma_f32_16x16x32_bf16 v[160:163], v[236:239], v[200:203], 0\n\t"
    "ds_read_b64_tr_b16 v[226:227], v249 offset:8448\n\t"
    "v_mfma_f32_16x16x32_bf16 v[164:167], v[236:239], v[208:211], 0\n\t"
    "s_waitcnt lgkmcnt(7)\n\t"
    "v_mfma_f32_16x16x32_bf16 v[168:171], v[240:243], v[192:195], 0\n\t"
    "ds_read_b64_tr_b16 v[228:229], v251 offset:8704\n\t"
    "v_mfma_f32_16x16x32_bf16 v[172:175], v[240:243], v[200:203], 0\n\t"
    "ds_read_b64_tr_b16 v[230:231], v251 offset:8960\n\t"
    "v_mfma_f32_16x16x32_bf16 v[176:179], v[240:243], v[208:211], 0\n\t"
    "s_waitcnt lgkmcnt(6)\n\t"
    "v_mfma_f32_16x16x32_bf16 v[180:183], v[216:219], v[192:195], 0\n\t"
    "ds_read_b64_tr_b16 v[232:233], v249 offset:8704\n\t"
    "v_mfma_f32_16x16x32_bf16 v[184:187], v[216:219], v[200:203], 0\n\t"
    "ds_read_b64_tr_b16 v[234:235], v249 offset:8960\n\t"
    "v_mfma_f32_16x16x32_bf16 v[188:191], v[216:219], v[208:211], 0\n\t"
    "s_waitcnt lgkmcnt(6)\n\t"
    "v_mfma_f32_16x16x32_bf16 v[96:99], v[220:223], v[196:199], v[96:99]\n\t"
    "ds_read_b64_tr_b16 v[236:237], v251 offset:9216\n\t"
    "v_mfma_f32_16x16x32_bf16 v[100:103], v[220:223], v[204:207], v[100:103]\n\t"
    "ds_read_b64_tr_b16 v[238:239], v251 offset:9472\n\t"
    "v_mfma_f32_16x16x32_bf16 v[104:107], v[220:223], v[212:215], v[104:107]\n\t"
    "s_waitcnt lgkmcnt(6)\n\t"
    "v_mfma_f32_16x16x32_bf16 v[108:111], v[224:227], v[196:199], v[108:111]\n\t"
    "ds_read_b64_tr_b16 v[240:241], v249 offset:9216\n\t"
    "v_mfma_f32_16x16x32_bf16 v[112:115], v[224:227], v[204:207], v[112:115]\n\t"
    "ds_read_b64_tr_b16 v[242:243], v249 offset:9472\n\t"
    "v_mfma_f32_16x16x32_bf16 v[116:119], v[224:227], v[212:215], v[116:119]\n\t"
    "s_waitcnt lgkmcnt(6)\n\t"
    "v_mfma_f32_16x16x32_bf16 v[120:123], v[228:231], v[196:199], v[120:123]\n\t"
    "ds_read_b64_tr_b16 v[216:217], v251 offset:9728\n\t"
    "v_mfma_f32_16x16x32_bf16 v[124:127], v[228:231], v[204:207], v[124:127]\n\t"
    "ds_read_b64_tr_b16 v[218:219], v251 offset:9984\n\t"
    "v_mfma_f32_16x16x32_bf16 v[128:131], v[228:231], v[212:215], v[128:131]\n\t"
    "s_waitcnt lgkmcnt(6)\n\t"
    "v_mfma_f32_16x16x32_bf16 v[132:135], v[232:235], v[196:199], v[132:135]\n\t"
    "ds_read_b64_tr_b16 v[220:221], v249 offset:9728\n\t"
    "v_mfma_f32_16x16x32_bf16 v[136:139], v[232:235], v[204:207], v[136:139]\n\t"
    "ds_read_b64_tr_b16 v[222:223], v249 offset:9984\n\t"
    "v_mfma_f32_16x16x32_bf16 v[140:143], v[232:235], v[212:215], v[140:143]\n\t"
    "s_waitcnt lgkmcnt(6)\n\t"
    "v_mfma_f32_16x16x32_bf16 v[144:147], v[236:239], v[196:199], v[144:147]\n\t"
    "v_mfma_f32_16x16x32_bf16 v[148:151], v[236:239], v[204:207], v[148:151]\n\t"
    "v_mfma_f32_16x16x32_bf16 v[152:155], v[236:239], v[212:215], v[152:155]\n\t"
    "s_waitcnt lgkmcnt(0)\n\t"
    "s_barrier\n\t"
    "v_mfma_f32_16x16x32_bf16 v[156:159], v[240:243], v[196:199], v[156:159]\n\t"
    "ds_read_b64_tr_b16 v[224:225], v251 offset:16384\n\t"
    "v_mfma_f32_16x16x32_bf16 v[160:163], v[240:243], v[204:207], v[160:163]\n\t"
    "ds_read_b64_tr_b16 v[226:227], v251 offset:16640\n\t"
    "v_mfma_f32_16x16x32_bf16 v[164:167], v[240:243], v[212:215], v[164:167]\n\t"
    "v_mfma_f32_16x16x32_bf16 v[168:171], v[216:219], v[196:199], v[168:171]\n\t"
    "ds_read_b64_tr_b16 v[228:229], v249 offset:16384\n\t"
    "ds_read_b64_tr_b16 v[230:231], v249 offset:16640\n\t"
    "v_mfma_f32_16x16x32_bf16 v[172:175], v[216:219], v[204:207], v[172:175]\n\t"
    "ds_read_b64_tr_b16 v[232:233], v251 offset:16896\n\t"
    "v_mfma_f32_16x16x32_bf16 v[176:179], v[216:219], v[212:215], v[176:179]\n\t"
    "ds_read_b64_tr_b16 v[234:235], v251 offset:17152\n\t"
    "v_mfma_f32_16x16x32_bf16 v[180:183], v[220:223], v[196:199], v[180:183]\n\t"
    "ds_read_b64_tr_b16 v[236:237], v249 offset:16896\n\t"
    "v_mfma_f32_16x16x32_bf16 v[184:187], v[220:223], v[204:207], v[184:187]\n\t"
    "ds_read_b64_tr_b16 v[238:239], v249 offset:17152\n\t"
    "v_mfma_f32_16x16x32_bf16 v[188:191], v[220:223], v[212:215], v[188:191]\n\t"
    "s_waitcnt lgkmcnt(6)\n\t"
    "v_mfma_f32_16x16x32_bf16 v[0:3], v[224:227], v[192:195], 0\n\t"
    "ds_read_b64_tr_b16 v[240:241], v251 offset:17408\n\t"
    "v_mfma_f32_16x16x32_bf16 v[4:7], v[224:227], v[200:203], 0\n\t"
    "ds_read_b64_tr_b16 v[242:243], v251 offset:17664\n\t"
    "v_mfma_f32_16x16x32_bf16 v[8:11], v[224:227], v[208:211], 0\n\t"
    "s_waitcnt lgkmcnt(6)\n\t"
    "v_mfma_f32_16x16x32_bf16 v[12:15], v[228:231], v[192:195], 0\n\t"
    "ds_read_b64_tr_b16 v[216:217], v249 offset:17408\n\t"
    "v_mfma_f32_16x16x32_bf16 v[16:19], v[228:231], v[200:203], 0\n\t"
    "ds_read_b64_tr_b16 v[218:219], v249 offset:17664\n\t"
    "v_mfma_f32_16x16x32_bf16 v[20:23], v[228:231], v[208:211], 0\n\t"
    "s_waitcnt lgkmcnt(6)\n\t"
    "v_mfma_f32_16x16x32_bf16 v[24:27], v[232:235], v[192:195], 0\n\t"
    "ds_read_b64_tr_b16 v[220:221], v251 offset:17920\n\t"
    "v_mfma_f32_16x16x32_bf16 v[28:31], v[232:235], v[200:203], 0\n\t"
    "ds_read_b64_tr_b16 v[222:223], v251 offset:18176\n\t"
    "v_mfma_f32_16x16x32_bf16 v[32:35], v[232:235], v[208:211], 0\n\t"
    "s_waitcnt lgkmcnt(6)\n\t"
    "v_mfma_f32_16x16x32_bf16 v[36:39], v[236:239], v[192:195], 0\n\t"
    "ds_read_b64_tr_b16 v[224:225], v249 offset:17920\n\t"
    "v_mfma_f32_16x16x32_bf16 v[40:43], v[236:239], v[200:203], 0\n\t"
    "ds_read_b64_tr_b16 v[226:227], v249 offset:18176\n\t"
    "v_mfma_f32_16x16x32_bf16 v[44:47], v[236:239], v[208:211], 0\n\t"
    "s_waitcnt lgkmcnt(6)\n\t"
    "v_mfma_f32_16x16x32_bf16 v[48:51], v[240:243], v[192:195], 0\n\t"
    "ds_read_b64_tr_b16 v[228:229], v251 offset:24576\n\t"
    "v_mfma_f32_16x16x32_bf16 v[52:55], v[240:243], v[200:203], 0\n\t"
    "ds_read_b64_tr_b16 v[230:231], v251 offset:24832\n\t"
    "v_mfma_f32_16x16x32_bf16 v[56:59], v[240:243], v[208:211], 0\n\t"
    "s_waitcnt lgkmcnt(6)\n\t"
    "v_mfma_f32_16x16x32_bf16 v[60:63], v[216:219], v[192:195], 0\n\t"
    "ds_read_b64_tr_b16 v[232:233], v249 offset:24576\n\t"
    "v_mfma_f32_16x16x32_bf16 v[64:67], v[216:219], v[200:203], 0\n\t"
    "ds_read_b64_tr_b16 v[234:235], v249 offset:24832\n\t"
    "v_mfma_f32_16x16x32_bf16 v[68:71], v[216:219], v[208:211], 0\n\t"
    "s_waitcnt lgkmcnt(6)\n\t"
    "v_mfma_f32_16x16x32_bf16 v[72:75], v[220:223], v[192:195], 0\n\t"
    "ds_read_b64_tr_b16 v[236:237], v251 offset:25088\n\t"
    "v_mfma_f32_16x16x32_bf16 v[76:79], v[220:223], v[200:203], 0\n\t"
    "ds_read_b64_tr_b16 v[238:239], v251 offset:25344\n\t"
    "v_mfma_f32_16x16x32_bf16 v[80:83], v[220:223], v[208:211], 0\n\t"
    "s_waitcnt lgkmcnt(6)\n\t"
    "v_mfma_f32_16x16x32_bf16 v[84:87], v[224:227], v[192:195], 0\n\t"
    "ds_read_b64_tr_b16 v[240:241], v249 offset:25088\n\t"
    "v_mfma_f32_16x16x32_bf16 v[88:91], v[224:227], v[200:203], 0\n\t"
    "ds_read_b64_tr_b16 v[242:243], v249 offset:25344\n\t"
    "v_mfma_f32_16x16x32_bf16 v[92:95], v[224:227], v[208:211], 0\n\t"
    "s_waitcnt lgkmcnt(6)\n\t"
    "v_mfma_f32_16x16x32_bf16 v[0:3], v[228:231], v[196:199], v[0:3]\n\t"
    "ds_read_b64_tr_b16 v[216:217], v251 offset:25600\n\t"
    "v_mfma_f32_16x16x32_bf16 v[4:7], v[228:231], v[204:207], v[4:7]\n\t"
    "ds_read_b64_tr_b16 v[218:219], v251 offset:25856\n\t"
    "v_mfma_f32_16x16x32_bf16 v[8:11], v[228:231], v[212:215], v[8:11]\n\t"
    "ds_read_b128 v[192:195], v252 offset:24576\n\t"
    "s_waitcnt lgkmcnt(7)\n\t"
    "v_mfma_f32_16x16x32_bf16 v[12:15], v[232:235], v[196:199], v[12:15]\n\t"
    "ds_read_b64_tr_b16 v[220:221], v249 offset:25600\n\t"
    "v_mfma_f32_16x16x32_bf16 v[16:19], v[232:235], v[204:207], v[16:19]\n\t"
    "ds_read_b64_tr_b16 v[222:223], v249 offset:25856\n\t"
    "v_mfma_f32_16x16x32_bf16 v[20:23], v[232:235], v[212:215], v[20:23]\n\t"
    "ds_read_b128 v[200:203], v252 offset:26624\n\t"
    "s_waitcnt lgkmcnt(8)\n\t"
    "v_mfma_f32_16x16x32_bf16 v[24:27], v[236:239], v[196:199], v[24:27]\n\t"
    "ds_read_b64_tr_b16 v[224:225], v251 offset:26112\n\t"
    "v_mfma_f32_16x16x32_bf16 v[28:31], v[236:239], v[204:207], v[28:31]\n\t"
    "ds_read_b64_tr_b16 v[226:227], v251 offset:26368\n\t"
    "v_mfma_f32_16x16x32_bf16 v[32:35], v[236:239], v[212:215], v[32:35]\n\t"
    "ds_read_b128 v[208:211], v252 offset:28672\n\t"
    "s_waitcnt lgkmcnt(9)\n\t"
    "v_mfma_f32_16x16x32_bf16 v[36:39], v[240:243], v[196:199], v[36:39]\n\t"
    "ds_read_b64_tr_b16 v[228:229], v249 offset:26112\n\t"
    "v_mfma_f32_16x16x32_bf16 v[40:43], v[240:243], v[204:207], v[40:43]\n\t"
    "ds_read_b64_tr_b16 v[230:231], v249 offset:26368\n\t"
    "v_mfma_f32_16x16x32_bf16 v[44:47], v[240:243], v[212:215], v[44:47]\n\t"
    "s_waitcnt lgkmcnt(9)\n\t"
    "v_mfma_f32_16x16x32_bf16 v[48:51], v[216:219], v[196:199], v[48:51]\n\t"
    "v_mfma_f32_16x16x32_bf16 v[52:55], v[216:219], v[204:207], v[52:55]\n\t"
    "v_mfma_f32_16x16x32_bf16 v[56:59], v[216:219], v[212:215], v[56:59]\n\t"
    "s_waitcnt lgkmcnt(0)\n\t"
    "s_barrier\n\t"
    "v_mfma_f32_16x16x32_bf16 v[60:63], v[220:223], v[196:199], v[60:63]\n\t"
    "ds_read_b64_tr_b16 v[232:233], v251 offset:32768\n\t"
    "v_mfma_f32_16x16x32_bf16 v[64:67], v[220:223], v[204:207], v[64:67]\n\t"
    "ds_read_b64_tr_b16 v[234:235], v251 offset:33024\n\t"
    "v_mfma_f32_16x16x32_bf16 v[68:71], v[220:223], v[212:215], v[68:71]\n\t"
    "v_mfma_f32_16x16x32_bf16 v[72:75], v[224:227], v[196:199], v[72:75]\n\t"
    "ds_read_b64_tr_b16 v[236:237], v249 offset:32768\n\t"
    "ds_read_b64_tr_b16 v[238:239], v249 offset:33024\n\t"
    "v_mfma_f32_16x16x32_bf16 v[76:79], v[224:227], v[204:207], v[76:79]\n\t"
    "ds_read_b64_tr_b16 v[240:241], v251 offset:33280\n\t"
    "v_mfma_f32_16x16x32_bf16 v[80:83], v[224:227], v[212:215], v[80:83]\n\t"
    "ds_read_b64_tr_b16 v[242:243], v251 offset:33536\n\t"
    "v_mfma_f32_16x16x32_bf16 v[84:87], v[228:231], v[196:199], v[84:87]\n\t"
    "ds_read_b64_tr_b16 v[216:217], v249 offset:33280\n\t"
    "v_mfma_f32_16x16x32_bf16 v[88:91], v[228:231], v[204:207], v[88:91]\n\t"
    "ds_read_b64_tr_b16 v[218:219], v249 offset:33536\n\t"
    "v_mfma_f32_16x16x32_bf16 v[92:95], v[228:231], v[212:215], v[92:95]\n\t"
    "s_waitcnt lgkmcnt(6)\n\t"
    "v_mfma_f32_16x16x32_bf16 v[96:99], v[232:235], v[192:195], v[96:99]\n\t"
    "ds_read_b64_tr_b16 v[220:221], v251 offset:33792\n\t"
    "v_mfma_f32_16x16x32_bf16 v[100:103], v[232:235], v[200:203], v[100:103]\n\t"
    "ds_read_b64_tr_b16 v[222:223], v251 offset:34048\n\t"
    "v_mfma_f32_16x16x32_bf16 v[104:107], v[232:235], v[208:211], v[104:107]\n\t"
    "ds_read_b128 v[196:199], v250 offset:24576\n\t"
    "s_waitcnt lgkmcnt(7)\n\t"
    "v_mfma_f32_16x16x32_bf16 v[108:111], v[236:239], v[192:195], v[108:111]\n\t"
    "ds_read_b64_tr_b16 v[224:225], v249 offset:33792\n\t"
    "v_mfma_f32_16x16x32_bf16 v[112:115], v[236:239], v[200:203], v[112:115]\n\t"
    "ds_read_b64_tr_b16 v[226:227], v249 offset:34048\n\t"
    "v_mfma_f32_16x16x32_bf16 v[116:119], v[236:239], v[208:211], v[116:119]\n\t"
    "ds_read_b128 v[204:207], v250 offset:26624\n\t"
    "s_waitcnt lgkmcnt(8)\n\t"
    "v_mfma_f32_16x16x32_bf16 v[120:123], v[240:243], v[192:195], v[120:123]\n\t"
    "ds_read_b64_tr_b16 v[228:229], v251 offset:34304\n\t"
    "v_mfma_f32_16x16x32_bf16 v[124:127], v[240:243], v[200:203], v[124:127]\n\t"
    "ds_read_b64_tr_b16 v[230:231], v251 offset:34560\n\t"
    "v_mfma_f32_16x16x32_bf16 v[128:131], v[240:243], v[208:211], v[128:131]\n\t"
    "ds_read_b128 v[212:215], v250 offset:28672\n\t"
    "s_waitcnt lgkmcnt(9)\n\t"
    "v_mfma_f32_16x16x32_bf16 v[132:135], v[216:219], v[192:195], v[132:135]\n\t"
    "ds_read_b64_tr_b16 v[232:233], v249 offset:34304\n\t"
    "v_mfma_f32_16x16x32_bf16 v[136:139], v[216:219], v[200:203], v[136:139]\n\t"
    "ds_read_b64_tr_b16 v[234:235], v249 offset:34560\n\t"
    "v_mfma_f32_16x16x32_bf16 v[140:143], v[216:219], v[208:211], v[140:143]\n\t"
    "s_waitcnt lgkmcnt(9)\n\t"
    "v_mfma_f32_16x16x32_bf16 v[144:147], v[220:223], v[192:195], v[144:147]\n\t"
    "ds_read_b64_tr_b16 v[236:237], v251 offset:40960\n\t"
    "v_mfma_f32_16x16x32_bf16 v[148:151], v[220:223], v[200:203], v[148:151]\n\t"
    "ds_read_b64_tr_b16 v[238:239], v251 offset:41216\n\t"
    "v_mfma_f32_16x16x32_bf16 v[152:155], v[220:223], v[208:211], v[152:155]\n\t"
    "s_waitcnt lgkmcnt(8)\n\t"
    "v_mfma_f32_16x16x32_bf16 v[156:159], v[224:227], v[192:195], v[156:159]\n\t"
    "ds_read_b64_tr_b16 v[240:241], v249 offset:40960\n\t"
    "v_mfma_f32_16x16x32_bf16 v[160:163], v[224:227], v[200:203], v[160:163]\n\t"
    "ds_read_b64_tr_b16 v[242:243], v249 offset:41216\n\t"
    "v_mfma_f32_16x16x32_bf16 v[164:167], v[224:227], v[208:211], v[164:167]\n\t"
    "s_waitcnt lgkmcnt(7)\n\t"
    "v_mfma_f32_16x16x32_bf16 v[168:171], v[228:231], v[192:195], v[168:171]\n\t"
    "ds_read_b64_tr_b16 v[216:217], v251 offset:41472\n\t"
    "v_mfma_f32_16x16x32_bf16 v[172:175], v[228:231], v[200:203], v[172:175]\n\t"
    "ds_read_b64_tr_b16 v[218:219], v251 offset:41728\n\t"
    "v_mfma_f32_16x16x32_bf16 v[176:179], v[228:231], v[208:211], v[176:179]\n\t"
    "s_waitcnt lgkmcnt(6)\n\t"
    "v_mfma_f32_16x16x32_bf16 v[180:183], v[232:235], v[192:195], v[180:183]\n\t"
    "ds_read_b64_tr_b16 v[220:221], v249 offset:41472\n\t"
    "v_mfma_f32_16x16x32_bf16 v[184:187], v[232:235], v[200:203], v[184:187]\n\t"
    "ds_read_b64_tr_b16 v[222:223], v249 offset:41728\n\t"
    "v_mfma_f32_16x16x32_bf16 v[188:191], v[232:235], v[208:211], v[188:191]\n\t"
    "s_waitcnt lgkmcnt(6)\n\t"
    "v_mfma_f32_16x16x32_bf16 v[96:99], v[236:239], v[196:199], v[96:99]\n\t"
    "ds_read_b64_tr_b16 v[224:225], v251 offset:41984\n\t"
    "v_mfma_f32_16x16x32_bf16 v[100:103], v[236:239], v[204:207], v[100:103]\n\t"
    "ds_read_b64_tr_b16 v[226:227], v251 offset:42240\n\t"
    "v_mfma_f32_16x16x32_bf16 v[104:107], v[236:239], v[212:215], v[104:107]\n\t"
    "s_waitcnt lgkmcnt(6)\n\t"
    "v_mfma_f32_16x16x32_bf16 v[108:111], v[240:243], v[196:199], v[108:111]\n\t"
    "ds_read_b64_tr_b16 v[228:229], v249 offset:41984\n\t"
    "v_mfma_f32_16x16x32_bf16 v[112:115], v[240:243], v[204:207], v[112:115]\n\t"
    "ds_read_b64_tr_b16 v[230:231], v249 offset:42240\n\t"
    "v_mfma_f32_16x16x32_bf16 v[116:119], v[240:243], v[212:215], v[116:119]\n\t"
    "s_waitcnt lgkmcnt(6)\n\t"
    "v_mfma_f32_16x16x32_bf16 v[120:123], v[216:219], v[196:199], v[120:123]\n\t"
    "ds_read_b64_tr_b16 v[232:233], v251 offset:42496\n\t"
    "v_mfma_f32_16x16x32_bf16 v[124:127], v[216:219], v[204:207], v[124:127]\n\t"
    "ds_read_b64_tr_b16 v[234:235], v251 offset:42752\n\t"
    "v_mfma_f32_16x16x32_bf16 v[128:131], v[216:219], v[212:215], v[128:131]\n\t"
    "s_waitcnt lgkmcnt(6)\n\t"
    "v_mfma_f32_16x16x32_bf16 v[132:135], v[220:223], v[196:199], v[132:135]\n\t"
    "ds_read_b64_tr_b16 v[236:237], v249 offset:42496\n\t"
    "v_mfma_f32_16x16x32_bf16 v[136:139], v[220:223], v[204:207], v[136:139]\n\t"
    "ds_read_b64_tr_b16 v[238:239], v249 offset:42752\n\t"
    "v_mfma_f32_16x16x32_bf16 v[140:143], v[220:223], v[212:215], v[140:143]\n\t"
    "s_waitcnt lgkmcnt(6)\n\t"
    "v_mfma_f32_16x16x32_bf16 v[144:147], v[224:227], v[196:199], v[144:147]\n\t"
    "v_mfma_f32_16x16x32_bf16 v[148:151], v[224:227], v[204:207], v[148:151]\n\t"
    "v_mfma_f32_16x16x32_bf16 v[152:155], v[224:227], v[212:215], v[152:155]\n\t"
    "s_waitcnt lgkmcnt(0)\n\t"
    "s_barrier\n\t"
    "v_mfma_f32_16x16x32_bf16 v[156:159], v[228:231], v[196:199], v[156:159]\n\t"
    "ds_read_b64_tr_b16 v[240:241], v251 offset:0\n\t"
    "v_mfma_f32_16x16x32_bf16 v[160:163], v[228:231], v[204:207], v[160:163]\n\t"
    "ds_read_b64_tr_b16 v[242:243], v251 offset:256\n\t"
    "v_mfma_f32_16x16x32_bf16 v[164:167], v[228:231], v[212:215], v[164:167]\n\t"
    "v_mfma_f32_16x16x32_bf16 v[168:171], v[232:235], v[196:199], v[168:171]\n\t"
    "ds_read_b64_tr_b16 v[216:217], v249 offset:0\n\t"
    "ds_read_b64_tr_b16 v[218:219], v249 offset:256\n\t"
    "v_mfma_f32_16x16x32_bf16 v[172:175], v[232:235], v[204:207], v[172:175]\n\t"
    "ds_read_b64_tr_b16 v[220:221], v251 offset:512\n\t"
    "v_mfma_f32_16x16x32_bf16 v[176:179], v[232:235], v[212:215], v[176:179]\n\t"
    "ds_read_b64_tr_b16 v[222:223], v251 offset:768\n\t"
    "v_mfma_f32_16x16x32_bf16 v[180:183], v[236:239], v[196:199], v[180:183]\n\t"
    "ds_read_b64_tr_b16 v[224:225], v249 offset:512\n\t"
    "v_mfma_f32_16x16x32_bf16 v[184:187], v[236:239], v[204:207], v[184:187]\n\t"
    "ds_read_b64_tr_b16 v[226:227], v249 offset:768\n\t"
    "v_mfma_f32_16x16x32_bf16 v[188:191], v[236:239], v[212:215], v[188:191]\n\t"
    "s_waitcnt lgkmcnt(6)\n\t"
    "v_mfma_f32_16x16x32_bf16 v[0:3], v[240:243], v[192:195], v[0:3]\n\t"
    "ds_read_b64_tr_b16 v[228:229], v251 offset:1024\n\t"
    "v_mfma_f32_16x16x32_bf16 v[4:7], v[240:243], v[200:203], v[4:7]\n\t"
    "ds_read_b64_tr_b16 v[230:231], v251 offset:1280\n\t"
    "v_mfma_f32_16x16x32_bf16 v[8:11], v[240:243], v[208:211], v[8:11]\n\t"
    "s_waitcnt lgkmcnt(6)\n\t"
    "v_mfma_f32_16x16x32_bf16 v[12:15], v[216:219], v[192:195], v[12:15]\n\t"
    "ds_read_b64_tr_b16 v[232:233], v249 offset:1024\n\t"
    "v_mfma_f32_16x16x32_bf16 v[16:19], v[216:219], v[200:203], v[16:19]\n\t"
    "ds_read_b64_tr_b16 v[234:235], v249 offset:1280\n\t"
    "v_mfma_f32_16x16x32_bf16 v[20:23], v[216:219], v[208:211], v[20:23]\n\t"
    "s_waitcnt lgkmcnt(6)\n\t"
    "v_mfma_f32_16x16x32_bf16 v[24:27], v[220:223], v[192:195], v[24:27]\n\t"
    "ds_read_b64_tr_b16 v[236:237], v251 offset:1536\n\t"
    "v_mfma_f32_16x16x32_bf16 v[28:31], v[220:223], v[200:203], v[28:31]\n\t"
    "ds_read_b64_tr_b16 v[238:239], v251 offset:1792\n\t"
    "v_mfma_f32_16x16x32_bf16 v[32:35], v[220:223], v[208:211], v[32:35]\n\t"
    "s_waitcnt lgkmcnt(6)\n\t"
    "v_mfma_f32_16x16x32_bf16 v[36:39], v[224:227], v[192:195], v[36:39]\n\t"
    "ds_read_b64_tr_b16 v[240:241], v249 offset:1536\n\t"
    "v_mfma_f32_16x16x32_bf16 v[40:43], v[224:227], v[200:203], v[40:43]\n\t"
    "ds_read_b64_tr_b16 v[242:243], v249 offset:1792\n\t"
    "v_mfma_f32_16x16x32_bf16 v[44:47], v[224:227], v[208:211], v[44:47]\n\t"
    "s_waitcnt lgkmcnt(6)\n\t"
    "v_mfma_f32_16x16x32_bf16 v[48:51], v[228:231], v[192:195], v[48:51]\n\t"
    "ds_read_b64_tr_b16 v[216:217], v251 offset:8192\n\t"
    "v_mfma_f32_16x16x32_bf16 v[52:55], v[228:231], v[200:203], v[52:55]\n\t"
    "ds_read_b64_tr_b16 v[218:219], v251 offset:8448\n\t"
    "v_mfma_f32_16x16x32_bf16 v[56:59], v[228:231], v[208:211], v[56:59]\n\t"
    "s_waitcnt lgkmcnt(6)\n\t"
    "v_mfma_f32_16x16x32_bf16 v[60:63], v[232:235], v[192:195], v[60:63]\n\t"
    "ds_read_b64_tr_b16 v[220:221], v249 offset:8192\n\t"
    "v_mfma_f32_16x16x32_bf16 v[64:67], v[232:235], v[200:203], v[64:67]\n\t"
    "ds_read_b64_tr_b16 v[222:223], v249 offset:8448\n\t"
    "v_mfma_f32_16x16x32_bf16 v[68:71], v[232:235], v[208:211], v[68:71]\n\t"
    "s_waitcnt lgkmcnt(6)\n\t"
    "v_mfma_f32_16x16x32_bf16 v[72:75], v[236:239], v[192:195], v[72:75]\n\t"
    "ds_read_b64_tr_b16 v[224:225], v251 offset:8704\n\t"
    "v_mfma_f32_16x16x32_bf16 v[76:79], v[236:239], v[200:203], v[76:79]\n\t"
    "ds_read_b64_tr_b16 v[226:227], v251 offset:8960\n\t"
    "v_mfma_f32_16x16x32_bf16 v[80:83], v[236:239], v[208:211], v[80:83]\n\t"
    "s_waitcnt lgkmcnt(6)\n\t"
    "v_mfma_f32_16x16x32_bf16 v[84:87], v[240:243], v[192:195], v[84:87]\n\t"
    "ds_read_b64_tr_b16 v[228:229], v249 offset:8704\n\t"
    "v_mfma_f32_16x16x32_bf16 v[88:91], v[240:243], v[200:203], v[88:91]\n\t"
    "ds_read_b64_tr_b16 v[230:231], v249 offset:8960\n\t"
    "v_mfma_f32_16x16x32_bf16 v[92:95], v[240:243], v[208:211], v[92:95]\n\t"
    "s_waitcnt lgkmcnt(6)\n\t"
    "v_mfma_f32_16x16x32_bf16 v[0:3], v[216:219], v[196:199], v[0:3]\n\t"
    "ds_read_b64_tr_b16 v[232:233], v251 offset:9216\n\t"
    "v_mfma_f32_16x16x32_bf16 v[4:7], v[216:219], v[204:207], v[4:7]\n\t"
    "ds_read_b64_tr_b16 v[234:235], v251 offset:9472\n\t"
    "v_mfma_f32_16x16x32_bf16 v[8:11], v[216:219], v[212:215], v[8:11]\n\t"
    "ds_read_b128 v[192:195], v252 offset:0\n\t"
    "s_waitcnt lgkmcnt(7)\n\t"
    "v_mfma_f32_16x16x32_bf16 v[12:15], v[220:223], v[196:199], v[12:15]\n\t"
    "ds_read_b64_tr_b16 v[236:237], v249 offset:9216\n\t"
    "v_mfma_f32_16x16x32_bf16 v[16:19], v[220:223], v[204:207], v[16:19]\n\t"
    "ds_read_b64_tr_b16 v[238:239], v249 offset:9472\n\t"
    "v_mfma_f32_16x16x32_bf16 v[20:23], v[220:223], v[212:215], v[20:23]\n\t"
    "ds_read_b128 v[200:203], v252 offset:2048\n\t"
    "s_waitcnt lgkmcnt(8)\n\t"
    "v_mfma_f32_16x16x32_bf16 v[24:27], v[224:227], v[196:199], v[24:27]\n\t"
    "ds_read_b64_tr_b16 v[240:241], v251 offset:9728\n\t"
    "v_mfma_f32_16x16x32_bf16 v[28:31], v[224:227], v[204:207], v[28:31]\n\t"
    "ds_read_b64_tr_b16 v[242:243], v251 offset:9984\n\t"
    "v_mfma_f32_16x16x32_bf16 v[32:35], v[224:227], v[212:215], v[32:35]\n\t"
    "ds_read_b128 v[208:211], v252 offset:4096\n\t"
    "s_waitcnt lgkmcnt(9)\n\t"
    "v_mfma_f32_16x16x32_bf16 v[36:39], v[228:231], v[196:199], v[36:39]\n\t"
    "ds_read_b64_tr_b16 v[216:217], v249 offset:9728\n\t"
    "v_mfma_f32_16x16x32_bf16 v[40:43], v[228:231], v[204:207], v[40:43]\n\t"
    "ds_read_b64_tr_b16 v[218:219], v249 offset:9984\n\t"
    "v_mfma_f32_16x16x32_bf16 v[44:47], v[228:231], v[212:215], v[44:47]\n\t"
    "s_waitcnt lgkmcnt(9)\n\t"
    "v_mfma_f32_16x16x32_bf16 v[48:51], v[232:235], v[196:199], v[48:51]\n\t"
    "v_mfma_f32_16x16x32_bf16 v[52:55], v[232:235], v[204:207], v[52:55]\n\t"
    "v_mfma_f32_16x16x32_bf16 v[56:59], v[232:235], v[212:215], v[56:59]\n\t"
    "s_waitcnt lgkmcnt(0)\n\t"
    "s_barrier\n\t"
    "v_mfma_f32_16x16x32_bf16 v[60:63], v[236:239], v[196:199], v[60:63]\n\t"
    "ds_read_b64_tr_b16 v[220:221], v251 offset:16384\n\t"
    "v_mfma_f32_16x16x32_bf16 v[64:67], v[236:239], v[204:207], v[64:67]\n\t"
    "ds_read_b64_tr_b16 v[222:223], v251 offset:16640\n\t"
    "v_mfma_f32_16x16x32_bf16 v[68:71], v[236:239], v[212:215], v[68:71]\n\t"
    "v_mfma_f32_16x16x32_bf16 v[72:75], v[240:243], v[196:199], v[72:75]\n\t"
    "ds_read_b64_tr_b16 v[224:225], v249 offset:16384\n\t"
    "ds_read_b64_tr_b16 v[226:227], v249 offset:16640\n\t"
    "v_mfma_f32_16x16x32_bf16 v[76:79], v[240:243], v[204:207], v[76:79]\n\t"
    "ds_read_b64_tr_b16 v[228:229], v251 offset:16896\n\t"
    "v_mfma_f32_16x16x32_bf16 v[80:83], v[240:243], v[212:215], v[80:83]\n\t"
    "ds_read_b64_tr_b16 v[230:231], v251 offset:17152\n\t"
    "v_mfma_f32_16x16x32_bf16 v[84:87], v[216:219], v[196:199], v[84:87]\n\t"
    "ds_read_b64_tr_b16 v[232:233], v249 offset:16896\n\t"
    "v_mfma_f32_16x16x32_bf16 v[88:91], v[216:219], v[204:207], v[88:91]\n\t"
    "ds_read_b64_tr_b16 v[234:235], v249 offset:17152\n\t"
    "v_mfma_f32_16x16x32_bf16 v[92:95], v[216:219], v[212:215], v[92:95]\n\t"
    "s_waitcnt lgkmcnt(6)\n\t"
    "v_mfma_f32_16x16x32_bf16 v[96:99], v[220:223], v[192:195], v[96:99]\n\t"
    "ds_read_b64_tr_b16 v[236:237], v251 offset:17408\n\t"
    "v_mfma_f32_16x16x32_bf16 v[100:103], v[220:223], v[200:203], v[100:103]\n\t"
    "ds_read_b64_tr_b16 v[238:239], v251 offset:17664\n\t"
    "v_mfma_f32_16x16x32_bf16 v[104:107], v[220:223], v[208:211], v[104:107]\n\t"
    "ds_read_b128 v[196:199], v250 offset:0\n\t"
    "s_waitcnt lgkmcnt(7)\n\t"
    "v_mfma_f32_16x16x32_bf16 v[108:111], v[224:227], v[192:195], v[108:111]\n\t"
    "ds_read_b64_tr_b16 v[240:241], v249 offset:17408\n\t"
    "v_mfma_f32_16x16x32_bf16 v[112:115], v[224:227], v[200:203], v[112:115]\n\t"
    "ds_read_b64_tr_b16 v[242:243], v249 offset:17664\n\t"
    "v_mfma_f32_16x16x32_bf16 v[116:119], v[224:227], v[208:211], v[116:119]\n\t"
    "ds_read_b128 v[204:207], v250 offset:2048\n\t"
    "s_waitcnt lgkmcnt(8)\n\t"
    "v_mfma_f32_16x16x32_bf16 v[120:123], v[228:231], v[192:195], v[120:123]\n\t"
    "ds_read_b64_tr_b16 v[216:217], v251 offset:17920\n\t"
    "v_mfma_f32_16x16x32_bf16 v[124:127], v[228:231], v[200:203], v[124:127]\n\t"
    "ds_read_b64_tr_b16 v[218:219], v251 offset:18176\n\t"
    "v_mfma_f32_16x16x32_bf16 v[128:131], v[228:231], v[208:211], v[128:131]\n\t"
    "ds_read_b128 v[212:215], v250 offset:4096\n\t"
    "s_waitcnt lgkmcnt(9)\n\t"
    "v_mfma_f32_16x16x32_bf16 v[132:135], v[232:235], v[192:195], v[132:135]\n\t"
    "ds_read_b64_tr_b16 v[220:221], v249 offset:17920\n\t"
    "v_mfma_f32_16x16x32_bf16 v[136:139], v[232:235], v[200:203], v[136:139]\n\t"
    "ds_read_b64_tr_b16 v[222:223], v249 offset:18176\n\t"
    "v_mfma_f32_16x16x32_bf16 v[140:143], v[232:235], v[208:211], v[140:143]\n\t"
    "s_waitcnt lgkmcnt(9)\n\t"
    "v_mfma_f32_16x16x32_bf16 v[144:147], v[236:239], v[192:195], v[144:147]\n\t"
    "ds_read_b64_tr_b16 v[224:225], v251 offset:24576\n\t"
    "v_mfma_f32_16x16x32_bf16 v[148:151], v[236:239], v[200:203], v[148:151]\n\t"
    "ds_read_b64_tr_b16 v[226:227], v251 offset:24832\n\t"
    "v_mfma_f32_16x16x32_bf16 v[152:155], v[236:239], v[208:211], v[152:155]\n\t"
    "s_waitcnt lgkmcnt(8)\n\t"
    "v_mfma_f32_16x16x32_bf16 v[156:159], v[240:243], v[192:195], v[156:159]\n\t"
    "ds_read_b64_tr_b16 v[228:229], v249 offset:24576\n\t"
    "v_mfma_f32_16x16x32_bf16 v[160:163], v[240:243], v[200:203], v[160:163]\n\t"
    "ds_read_b64_tr_b16 v[230:231], v249 offset:24832\n\t"
    "v_mfma_f32_16x16x32_bf16 v[164:167], v[240:243], v[208:211], v[164:167]\n\t"
    "s_waitcnt lgkmcnt(7)\n\t"
    "v_mfma_f32_16x16x32_bf16 v[168:171], v[216:219], v[192:195], v[168:171]\n\t"
    "ds_read_b64_tr_b16 v[232:233], v251 offset:25088\n\t"
    "v_mfma_f32_16x16x32_bf16 v[172:175], v[216:219], v[200:203], v[172:175]\n\t"
    "ds_read_b64_tr_b16 v[234:235], v251 offset:25344\n\t"
    "v_mfma_f32_16x16x32_bf16 v[176:179], v[216:219], v[208:211], v[176:179]\n\t"
    "s_waitcnt lgkmcnt(6)\n\t"
    "v_mfma_f32_16x16x32_bf16 v[180:183], v[220:223], v[192:195], v[180:183]\n\t"
    "ds_read_b64_tr_b16 v[236:237], v249 offset:25088\n\t"
    "v_mfma_f32_16x16x32_bf16 v[184:187], v[220:223], v[200:203], v[184:187]\n\t"
    "ds_read_b64_tr_b16 v[238:239], v249 offset:25344\n\t"
    "v_mfma_f32_16x16x32_bf16 v[188:191], v[220:223], v[208:211], v[188:191]\n\t"
    "s_waitcnt lgkmcnt(6)\n\t"
    "v_mfma_f32_16x16x32_bf16 v[96:99], v[224:227], v[196:199], v[96:99]\n\t"
    "ds_read_b64_tr_b16 v[240:241], v251 offset:25600\n\t"
    "v_mfma_f32_16x16x32_bf16 v[100:103], v[224:227], v[204:207], v[100:103]\n\t"
    "ds_read_b64_tr_b16 v[242:243], v251 offset:25856\n\t"
    "v_mfma_f32_16x16x32_bf16 v[104:107], v[224:227], v[212:215], v[104:107]\n\t"
    "s_waitcnt lgkmcnt(6)\n\t"
    "v_mfma_f32_16x16x32_bf16 v[108:111], v[228:231], v[196:199], v[108:111]\n\t"
    "ds_read_b64_tr_b16 v[216:217], v249 offset:25600\n\t"
    "v_mfma_f32_16x16x32_bf16 v[112:115], v[228:231], v[204:207], v[112:115]\n\t"
    "ds_read_b64_tr_b16 v[218:219], v249 offset:25856\n\t"
    "v_mfma_f32_16x16x32_bf16 v[116:119], v[228:231], v[212:215], v[116:119]\n\t"
    "s_waitcnt lgkmcnt(6)\n\t"
    "v_mfma_f32_16x16x32_bf16 v[120:123], v[232:235], v[196:199], v[120:123]\n\t"
    "ds_read_b64_tr_b16 v[220:221], v251 offset:26112\n\t"
    "v_mfma_f32_16x16x32_bf16 v[124:127], v[232:235], v[204:207], v[124:127]\n\t"
    "ds_read_b64_tr_b16 v[222:223], v251 offset:26368\n\t"
    "v_mfma_f32_16x16x32_bf16 v[128:131], v[232:235], v[212:215], v[128:131]\n\t"
    "s_waitcnt lgkmcnt(6)\n\t"
    "v_mfma_f32_16x16x32_bf16 v[132:135], v[236:239], v[196:199], v[132:135]\n\t"
    "ds_read_b64_tr_b16 v[224:225], v249 offset:26112\n\t"
    "v_mfma_f32_16x16x32_bf16 v[136:139], v[236:239], v[204:207], v[136:139]\n\t"
    "ds_read_b64_tr_b16 v[226:227], v249 offset:26368\n\t"
    "v_mfma_f32_16x16x32_bf16 v[140:143], v[236:239], v[212:215], v[140:143]\n\t"
    "s_waitcnt lgkmcnt(6)\n\t"
    "v_mfma_f32_16x16x32_bf16 v[144:147], v[240:243], v[196:199], v[144:147]\n\t"
    "v_mfma_f32_16x16x32_bf16 v[148:151], v[240:243], v[204:207], v[148:151]\n\t"
    "v_mfma_f32_16x16x32_bf16 v[152:155], v[240:243], v[212:215], v[152:155]\n\t"
    "s_waitcnt lgkmcnt(0)\n\t"
    "s_barrier\n\t"
    "v_mfma_f32_16x16x32_bf16 v[156:159], v[216:219], v[196:199], v[156:159]\n\t"
    "ds_read_b64_tr_b16 v[228:229], v251 offset:32768\n\t"
    "v_mfma_f32_16x16x32_bf16 v[160:163], v[216:219], v[204:207], v[160:163]\n\t"
    "ds_read_b64_tr_b16 v[230:231], v251 offset:33024\n\t"
    "v_mfma_f32_16x16x32_bf16 v[164:167], v[216:219], v[212:215], v[164:167]\n\t"
    "v_mfma_f32_16x16x32_bf16 v[168:171], v[220:223], v[196:199], v[168:171]\n\t"
    "ds_read_b64_tr_b16 v[232:233], v249 offset:32768\n\t"
    "ds_read_b64_tr_b16 v[234:235], v249 offset:33024\n\t"
    "v_mfma_f32_16x16x32_bf16 v[172:175], v[220:223], v[204:207], v[172:175]\n\t"
    "ds_read_b64_tr_b16 v[236:237], v251 offset:33280\n\t"
    "v_mfma_f32_16x16x32_bf16 v[176:179], v[220:223], v[212:215], v[176:179]\n\t"
    "ds_read_b64_tr_b16 v[238:239], v251 offset:33536\n\t"
    "v_mfma_f32_16x16x32_bf16 v[180:183], v[224:227], v[196:199], v[180:183]\n\t"
    "ds_read_b64_tr_b16 v[240:241], v249 offset:33280\n\t"
    "v_mfma_f32_16x16x32_bf16 v[184:187], v[224:227], v[204:207], v[184:187]\n\t"
    "ds_read_b64_tr_b16 v[242:243], v249 offset:33536\n\t"
    "v_mfma_f32_16x16x32_bf16 v[188:191], v[224:227], v[212:215], v[188:191]\n\t"
    "s_waitcnt lgkmcnt(6)\n\t"
    "v_mfma_f32_16x16x32_bf16 v[0:3], v[228:231], v[192:195], v[0:3]\n\t"
    "ds_read_b64_tr_b16 v[216:217], v251 offset:33792\n\t"
    "v_mfma_f32_16x16x32_bf16 v[4:7], v[228:231], v[200:203], v[4:7]\n\t"
    "ds_read_b64_tr_b16 v[218:219], v251 offset:34048\n\t"
    "v_mfma_f32_16x16x32_bf16 v[8:11], v[228:231], v[208:211], v[8:11]\n\t"
    "s_waitcnt lgkmcnt(6)\n\t"
    "v_mfma_f32_16x16x32_bf16 v[12:15], v[232:235], v[192:195], v[12:15]\n\t"
    "ds_read_b64_tr_b16 v[220:221], v249 offset:33792\n\t"
    "v_mfma_f32_16x16x32_bf16 v[16:19], v[232:235], v[200:203], v[16:19]\n\t"
    "ds_read_b64_tr_b16 v[222:223], v249 offset:34048\n\t"
    "v_mfma_f32_16x16x32_bf16 v[20:23], v[232:235], v[208:211], v[20:23]\n\t"
    "s_waitcnt lgkmcnt(6)\n\t"
    "v_mfma_f32_16x16x32_bf16 v[24:27], v[236:239], v[192:195], v[24:27]\n\t"
    "ds_read_b64_tr_b16 v[224:225], v251 offset:34304\n\t"
    "v_mfma_f32_16x16x32_bf16 v[28:31], v[236:239], v[200:203], v[28:31]\n\t"
    "ds_read_b64_tr_b16 v[226:227], v251 offset:34560\n\t"
    "v_mfma_f32_16x16x32_bf16 v[32:35], v[236:239], v[208:211], v[32:35]\n\t"
    "s_waitcnt lgkmcnt(6)\n\t"
    "v_mfma_f32_16x16x32_bf16 v[36:39], v[240:243], v[192:195], v[36:39]\n\t"
    "ds_read_b64_tr_b16 v[228:229], v249 offset:34304\n\t"
    "v_mfma_f32_16x16x32_bf16 v[40:43], v[240:243], v[200:203], v[40:43]\n\t"
    "ds_read_b64_tr_b16 v[230:231], v249 offset:34560\n\t"
    "v_mfma_f32_16x16x32_bf16 v[44:47], v[240:243], v[208:211], v[44:47]\n\t"
    "s_waitcnt lgkmcnt(6)\n\t"
    "v_mfma_f32_16x16x32_bf16 v[48:51], v[216:219], v[192:195], v[48:51]\n\t"
    "ds_read_b64_tr_b16 v[232:233], v251 offset:40960\n\t"
    "v_mfma_f32_16x16x32_bf16 v[52:55], v[216:219], v[200:203], v[52:55]\n\t"
    "ds_read_b64_tr_b16 v[234:235], v251 offset:41216\n\t"
    "v_mfma_f32_16x16x32_bf16 v[56:59], v[216:219], v[208:211], v[56:59]\n\t"
    "s_waitcnt lgkmcnt(6)\n\t"
    "v_mfma_f32_16x16x32_bf16 v[60:63], v[220:223], v[192:195], v[60:63]\n\t"
    "ds_read_b64_tr_b16 v[236:237], v249 offset:40960\n\t"
    "v_mfma_f32_16x16x32_bf16 v[64:67], v[220:223], v[200:203], v[64:67]\n\t"
    "ds_read_b64_tr_b16 v[238:239], v249 offset:41216\n\t"
    "v_mfma_f32_16x16x32_bf16 v[68:71], v[220:223], v[208:211], v[68:71]\n\t"
    "s_waitcnt lgkmcnt(6)\n\t"
    "v_mfma_f32_16x16x32_bf16 v[72:75], v[224:227], v[192:195], v[72:75]\n\t"
    "ds_read_b64_tr_b16 v[240:241], v251 offset:41472\n\t"
    "v_mfma_f32_16x16x32_bf16 v[76:79], v[224:227], v[200:203], v[76:79]\n\t"
    "ds_read_b64_tr_b16 v[242:243], v251 offset:41728\n\t"
    "v_mfma_f32_16x16x32_bf16 v[80:83], v[224:227], v[208:211], v[80:83]\n\t"
    "s_waitcnt lgkmcnt(6)\n\t"
    "v_mfma_f32_16x16x32_bf16 v[84:87], v[228:231], v[192:195], v[84:87]\n\t"
    "ds_read_b64_tr_b16 v[216:217], v249 offset:41472\n\t"
    "v_mfma_f32_16x16x32_bf16 v[88:91], v[228:231], v[200:203], v[88:91]\n\t"
    "ds_read_b64_tr_b16 v[218:219], v249 offset:41728\n\t"
    "v_mfma_f32_16x16x32_bf16 v[92:95], v[228:231], v[208:211], v[92:95]\n\t"
    "s_waitcnt lgkmcnt(6)\n\t"
    "v_mfma_f32_16x16x32_bf16 v[0:3], v[232:235], v[196:199], v[0:3]\n\t"
    "ds_read_b64_tr_b16 v[220:221], v251 offset:41984\n\t"
    "v_mfma_f32_16x16x32_bf16 v[4:7], v[232:235], v[204:207], v[4:7]\n\t"
    "ds_read_b64_tr_b16 v[222:223], v251 offset:42240\n\t"
    "v_mfma_f32_16x16x32_bf16 v[8:11], v[232:235], v[212:215], v[8:11]\n\t"
    "ds_read_b128 v[192:195], v252 offset:24576\n\t"
    "s_waitcnt lgkmcnt(7)\n\t"
    "v_mfma_f32_16x16x32_bf16 v[12:15], v[236:239], v[196:199], v[12:15]\n\t"
    "ds_read_b64_tr_b16 v[224:225], v249 offset:41984\n\t"
    "v_mfma_f32_16x16x32_bf16 v[16:19], v[236:239], v[204:207], v[16:19]\n\t"
    "ds_read_b64_tr_b16 v[226:227], v249 offset:42240\n\t"
    "v_mfma_f32_16x16x32_bf16 v[20:23], v[236:239], v[212:215], v[20:23]\n\t"
    "ds_read_b128 v[200:203], v252 offset:26624\n\t"
    "s_waitcnt lgkmcnt(8)\n\t"
    "v_mfma_f32_16x16x32_bf16 v[24:27], v[240:243], v[196:199], v[24:27]\n\t"
    "ds_read_b64_tr_b16 v[228:229], v251 offset:42496\n\t"
    "v_mfma_f32_16x16x32_bf16 v[28:31], v[240:243], v[204:207], v[28:31]\n\t"
    "ds_read_b64_tr_b16 v[230:231], v251 offset:42752\n\t"
    "v_mfma_f32_16x16x32_bf16 v[32:35], v[240:243], v[212:215], v[32:35]\n\t"
    "ds_read_b128 v[208:211], v252 offset:28672\n\t"
    "s_waitcnt lgkmcnt(9)\n\t"
    "v_mfma_f32_16x16x32_bf16 v[36:39], v[216:219], v[196:199], v[36:39]\n\t"
    "ds_read_b64_tr_b16 v[232:233], v249 offset:42496\n\t"
    "v_mfma_f32_16x16x32_bf16 v[40:43], v[216:219], v[204:207], v[40:43]\n\t"
    "ds_read_b64_tr_b16 v[234:235], v249 offset:42752\n\t"
    "v_mfma_f32_16x16x32_bf16 v[44:47], v[216:219], v[212:215], v[44:47]\n\t"
    "s_waitcnt lgkmcnt(9)\n\t"
    "v_mfma_f32_16x16x32_bf16 v[48:51], v[220:223], v[196:199], v[48:51]\n\t"
    "v_mfma_f32_16x16x32_bf16 v[52:55], v[220:223], v[204:207], v[52:55]\n\t"
    "v_mfma_f32_16x16x32_bf16 v[56:59], v[220:223], v[212:215], v[56:59]\n\t"
    "s_waitcnt lgkmcnt(0)\n\t"
    "s_barrier\n\t"
    "v_mfma_f32_16x16x32_bf16 v[60:63], v[224:227], v[196:199], v[60:63]\n\t"
    "ds_read_b64_tr_b16 v[236:237], v251 offset:0\n\t"
    "v_mfma_f32_16x16x32_bf16 v[64:67], v[224:227], v[204:207], v[64:67]\n\t"
    "ds_read_b64_tr_b16 v[238:239], v251 offset:256\n\t"
    "v_mfma_f32_16x16x32_bf16 v[68:71], v[224:227], v[212:215], v[68:71]\n\t"
    "v_mfma_f32_16x16x32_bf16 v[72:75], v[228:231], v[196:199], v[72:75]\n\t"
    "ds_read_b64_tr_b16 v[240:241], v249 offset:0\n\t"
    "ds_read_b64_tr_b16 v[242:243], v249 offset:256\n\t"
    "v_mfma_f32_16x16x32_bf16 v[76:79], v[228:231], v[204:207], v[76:79]\n\t"
    "ds_read_b64_tr_b16 v[216:217], v251 offset:512\n\t"
    "v_mfma_f32_16x16x32_bf16 v[80:83], v[228:231], v[212:215], v[80:83]\n\t"
    "ds_read_b64_tr_b16 v[218:219], v251 offset:768\n\t"
    "v_mfma_f32_16x16x32_bf16 v[84:87], v[232:235], v[196:199], v[84:87]\n\t"
    "ds_read_b64_tr_b16 v[220:221], v249 offset:512\n\t"
    "v_mfma_f32_16x16x32_bf16 v[88:91], v[232:235], v[204:207], v[88:91]\n\t"
    "ds_read_b64_tr_b16 v[222:223], v249 offset:768\n\t"
    "v_mfma_f32_16x16x32_bf16 v[92:95], v[232:235], v[212:215], v[92:95]\n\t"
    "s_waitcnt lgkmcnt(6)\n\t"
    "v_mfma_f32_16x16x32_bf16 v[96:99], v[236:239], v[192:195], v[96:99]\n\t"
    "ds_read_b64_tr_b16 v[224:225], v251 offset:1024\n\t"
    "v_mfma_f32_16x16x32_bf16 v[100:103], v[236:239], v[200:203], v[100:103]\n\t"
    "ds_read_b64_tr_b16 v[226:227], v251 offset:1280\n\t"
    "v_mfma_f32_16x16x32_bf16 v[104:107], v[236:239], v[208:211], v[104:107]\n\t"
    "ds_read_b128 v[196:199], v250 offset:24576\n\t"
    "s_waitcnt lgkmcnt(7)\n\t"
    "v_mfma_f32_16x16x32_bf16 v[108:111], v[240:243], v[192:195], v[108:111]\n\t"
    "ds_read_b64_tr_b16 v[228:229], v249 offset:1024\n\t"
    "v_mfma_f32_16x16x32_bf16 v[112:115], v[240:243], v[200:203], v[112:115]\n\t"
    "ds_read_b64_tr_b16 v[230:231], v249 offset:1280\n\t"
    "v_mfma_f32_16x16x32_bf16 v[116:119], v[240:243], v[208:211], v[116:119]\n\t"
    "ds_read_b128 v[204:207], v250 offset:26624\n\t"
    "s_waitcnt lgkmcnt(8)\n\t"
    "v_mfma_f32_16x16x32_bf16 v[120:123], v[216:219], v[192:195], v[120:123]\n\t"
    "ds_read_b64_tr_b16 v[232:233], v251 offset:1536\n\t"
    "v_mfma_f32_16x16x32_bf16 v[124:127], v[216:219], v[200:203], v[124:127]\n\t"
    "ds_read_b64_tr_b16 v[234:235], v251 offset:1792\n\t"
    "v_mfma_f32_16x16x32_bf16 v[128:131], v[216:219], v[208:211], v[128:131]\n\t"
    "ds_read_b128 v[212:215], v250 offset:28672\n\t"
    "s_waitcnt lgkmcnt(9)\n\t"
    "v_mfma_f32_16x16x32_bf16 v[132:135], v[220:223], v[192:195], v[132:135]\n\t"
    "ds_read_b64_tr_b16 v[236:237], v249 offset:1536\n\t"
    "v_mfma_f32_16x16x32_bf16 v[136:139], v[220:223], v[200:203], v[136:139]\n\t"
    "ds_read_b64_tr_b16 v[238:239], v249 offset:1792\n\t"
    "v_mfma_f32_16x16x32_bf16 v[140:143], v[220:223], v[208:211], v[140:143]\n\t"
    "s_waitcnt lgkmcnt(9)\n\t"
    "v_mfma_f32_16x16x32_bf16 v[144:147], v[224:227], v[192:195], v[144:147]\n\t"
    "ds_read_b64_tr_b16 v[240:241], v251 offset:8192\n\t"
    "v_mfma_f32_16x16x32_bf16 v[148:151], v[224:227], v[200:203], v[148:151]\n\t"
    "ds_read_b64_tr_b16 v[242:243], v251 offset:8448\n\t"
    "v_mfma_f32_16x16x32_bf16 v[152:155], v[224:227], v[208:211], v[152:155]\n\t"
    "s_waitcnt lgkmcnt(8)\n\t"
    "v_mfma_f32_16x16x32_bf16 v[156:159], v[228:231], v[192:195], v[156:159]\n\t"
    "ds_read_b64_tr_b16 v[216:217], v249 offset:8192\n\t"
    "v_mfma_f32_16x16x32_bf16 v[160:163], v[228:231], v[200:203], v[160:163]\n\t"
    "ds_read_b64_tr_b16 v[218:219], v249 offset:8448\n\t"
    "v_mfma_f32_16x16x32_bf16 v[164:167], v[228:231], v[208:211], v[164:167]\n\t"
    "s_waitcnt lgkmcnt(7)\n\t"
    "v_mfma_f32_16x16x32_bf16 v[168:171], v[232:235], v[192:195], v[168:171]\n\t"
    "ds_read_b64_tr_b16 v[220:221], v251 offset:8704\n\t"
    "v_mfma_f32_16x16x32_bf16 v[172:175], v[232:235], v[200:203], v[172:175]\n\t"
    "ds_read_b64_tr_b16 v[222:223], v251 offset:8960\n\t"
    "v_mfma_f32_16x16x32_bf16 v[176:179], v[232:235], v[208:211], v[176:179]\n\t"
    "s_waitcnt lgkmcnt(6)\n\t"
    "v_mfma_f32_16x16x32_bf16 v[180:183], v[236:239], v[192:195], v[180:183]\n\t"
    "ds_read_b64_tr_b16 v[224:225], v249 offset:8704\n\t"
    "v_mfma_f32_16x16x32_bf16 v[184:187], v[236:239], v[200:203], v[184:187]\n\t"
    "ds_read_b64_tr_b16 v[226:227], v249 offset:8960\n\t"
    "v_mfma_f32_16x16x32_bf16 v[188:191], v[236:239], v[208:211], v[188:191]\n\t"
    "s_waitcnt lgkmcnt(6)\n\t"
    "v_mfma_f32_16x16x32_bf16 v[96:99], v[240:243], v[196:199], v[96:99]\n\t"
    "ds_read_b64_tr_b16 v[228:229], v251 offset:9216\n\t"
    "v_mfma_f32_16x16x32_bf16 v[100:103], v[240:243], v[204:207], v[100:103]\n\t"
    "ds_read_b64_tr_b16 v[230:231], v251 offset:9472\n\t"
    "v_mfma_f32_16x16x32_bf16 v[104:107], v[240:243], v[212:215], v[104:107]\n\t"
    "s_waitcnt lgkmcnt(6)\n\t"
    "v_mfma_f32_16x16x32_bf16 v[108:111], v[216:219], v[196:199], v[108:111]\n\t"
    "ds_read_b64_tr_b16 v[232:233], v249 offset:9216\n\t"
    "v_mfma_f32_16x16x32_bf16 v[112:115], v[216:219], v[204:207], v[112:115]\n\t"
    "ds_read_b64_tr_b16 v[234:235], v249 offset:9472\n\t"
    "v_mfma_f32_16x16x32_bf16 v[116:119], v[216:219], v[212:215], v[116:119]\n\t"
    "s_waitcnt lgkmcnt(6)\n\t"
    "v_mfma_f32_16x16x32_bf16 v[120:123], v[220:223], v[196:199], v[120:123]\n\t"
    "ds_read_b64_tr_b16 v[236:237], v251 offset:9728\n\t"
    "v_mfma_f32_16x16x32_bf16 v[124:127], v[220:223], v[204:207], v[124:127]\n\t"
    "ds_read_b64_tr_b16 v[238:239], v251 offset:9984\n\t"
    "v_mfma_f32_16x16x32_bf16 v[128:131], v[220:223], v[212:215], v[128:131]\n\t"
    "s_waitcnt lgkmcnt(6)\n\t"
    "v_mfma_f32_16x16x32_bf16 v[132:135], v[224:227], v[196:199], v[132:135]\n\t"
    "ds_read_b64_tr_b16 v[240:241], v249 offset:9728\n\t"
    "v_mfma_f32_16x16x32_bf16 v[136:139], v[224:227], v[204:207], v[136:139]\n\t"
    "ds_read_b64_tr_b16 v[242:243], v249 offset:9984\n\t"
    "v_mfma_f32_16x16x32_bf16 v[140:143], v[224:227], v[212:215], v[140:143]\n\t"
    "s_waitcnt lgkmcnt(6)\n\t"
    "v_mfma_f32_16x16x32_bf16 v[144:147], v[228:231], v[196:199], v[144:147]\n\t"
    "v_mfma_f32_16x16x32_bf16 v[148:151], v[228:231], v[204:207], v[148:151]\n\t"
    "v_mfma_f32_16x16x32_bf16 v[152:155], v[228:231], v[212:215], v[152:155]\n\t"
    "s_waitcnt lgkmcnt(0)\n\t"
    "s_barrier\n\t"
    "v_mfma_f32_16x16x32_bf16 v[156:159], v[232:235], v[196:199], v[156:159]\n\t"
    "ds_read_b64_tr_b16 v[216:217], v251 offset:16384\n\t"
    "v_mfma_f32_16x16x32_bf16 v[160:163], v[232:235], v[204:207], v[160:163]\n\t"
    "ds_read_b64_tr_b16 v[218:219], v251 offset:16640\n\t"
    "v_mfma_f32_16x16x32_bf16 v[164:167], v[232:235], v[212:215], v[164:167]\n\t"
    "v_mfma_f32_16x16x32_bf16 v[168:171], v[236:239], v[196:199], v[168:171]\n\t"
    "ds_read_b64_tr_b16 v[220:221], v249 offset:16384\n\t"
    "ds_read_b64_tr_b16 v[222:223], v249 offset:16640\n\t"
    "v_mfma_f32_16x16x32_bf16 v[172:175], v[236:239], v[204:207], v[172:175]\n\t"
    "ds_read_b64_tr_b16 v[224:225], v251 offset:16896\n\t"
    "v_mfma_f32_16x16x32_bf16 v[176:179], v[236:239], v[212:215], v[176:179]\n\t"
    "ds_read_b64_tr_b16 v[226:227], v251 offset:17152\n\t"
    "v_mfma_f32_16x16x32_bf16 v[180:183], v[240:243], v[196:199], v[180:183]\n\t"
    "ds_read_b64_tr_b16 v[228:229], v249 offset:16896\n\t"
    "v_mfma_f32_16x16x32_bf16 v[184:187], v[240:243], v[204:207], v[184:187]\n\t"
    "ds_read_b64_tr_b16 v[230:231], v249 offset:17152\n\t"
    "v_mfma_f32_16x16x32_bf16 v[188:191], v[240:243], v[212:215], v[188:191]\n\t"
    "s_waitcnt lgkmcnt(6)\n\t"
    "v_mfma_f32_16x16x32_bf16 v[0:3], v[216:219], v[192:195], v[0:3]\n\t"
    "ds_read_b64_tr_b16 v[232:233], v251 offset:17408\n\t"
    "v_mfma_f32_16x16x32_bf16 v[4:7], v[216:219], v[200:203], v[4:7]\n\t"
    "ds_read_b64_tr_b16 v[234:235], v251 offset:17664\n\t"
    "v_mfma_f32_16x16x32_bf16 v[8:11], v[216:219], v[208:211], v[8:11]\n\t"
    "s_waitcnt lgkmcnt(6)\n\t"
    "v_mfma_f32_16x16x32_bf16 v[12:15], v[220:223], v[192:195], v[12:15]\n\t"
    "ds_read_b64_tr_b16 v[236:237], v249 offset:17408\n\t"
    "v_mfma_f32_16x16x32_bf16 v[16:19], v[220:223], v[200:203], v[16:19]\n\t"
    "ds_read_b64_tr_b16 v[238:239], v249 offset:17664\n\t"
    "v_mfma_f32_16x16x32_bf16 v[20:23], v[220:223], v[208:211], v[20:23]\n\t"
    "s_waitcnt lgkmcnt(6)\n\t"
    "v_mfma_f32_16x16x32_bf16 v[24:27], v[224:227], v[192:195], v[24:27]\n\t"
    "ds_read_b64_tr_b16 v[240:241], v251 offset:17920\n\t"
    "v_mfma_f32_16x16x32_bf16 v[28:31], v[224:227], v[200:203], v[28:31]\n\t"
    "ds_read_b64_tr_b16 v[242:243], v251 offset:18176\n\t"
    "v_mfma_f32_16x16x32_bf16 v[32:35], v[224:227], v[208:211], v[32:35]\n\t"
    "s_waitcnt lgkmcnt(6)\n\t"
    "v_mfma_f32_16x16x32_bf16 v[36:39], v[228:231], v[192:195], v[36:39]\n\t"
    "ds_read_b64_tr_b16 v[216:217], v249 offset:17920\n\t"
    "v_mfma_f32_16x16x32_bf16 v[40:43], v[228:231], v[200:203], v[40:43]\n\t"
    "ds_read_b64_tr_b16 v[218:219], v249 offset:18176\n\t"
    "v_mfma_f32_16x16x32_bf16 v[44:47], v[228:231], v[208:211], v[44:47]\n\t"
    "s_waitcnt lgkmcnt(6)\n\t"
    "v_mfma_f32_16x16x32_bf16 v[48:51], v[232:235], v[192:195], v[48:51]\n\t"
    "ds_read_b64_tr_b16 v[220:221], v251 offset:24576\n\t"
    "v_mfma_f32_16x16x32_bf16 v[52:55], v[232:235], v[200:203], v[52:55]\n\t"
    "ds_read_b64_tr_b16 v[222:223], v251 offset:24832\n\t"
    "v_mfma_f32_16x16x32_bf16 v[56:59], v[232:235], v[208:211], v[56:59]\n\t"
    "s_waitcnt lgkmcnt(6)\n\t"
    "v_mfma_f32_16x16x32_bf16 v[60:63], v[236:239], v[192:195], v[60:63]\n\t"
    "ds_read_b64_tr_b16 v[224:225], v249 offset:24576\n\t"
    "v_mfma_f32_16x16x32_bf16 v[64:67], v[236:239], v[200:203], v[64:67]\n\t"
    "ds_read_b64_tr_b16 v[226:227], v249 offset:24832\n\t"
    "v_mfma_f32_16x16x32_bf16 v[68:71], v[236:239], v[208:211], v[68:71]\n\t"
    "s_waitcnt lgkmcnt(6)\n\t"
    "v_mfma_f32_16x16x32_bf16 v[72:75], v[240:243], v[192:195], v[72:75]\n\t"
    "ds_read_b64_tr_b16 v[228:229], v251 offset:25088\n\t"
    "v_mfma_f32_16x16x32_bf16 v[76:79], v[240:243], v[200:203], v[76:79]\n\t"
    "ds_read_b64_tr_b16 v[230:231], v251 offset:25344\n\t"
    "v_mfma_f32_16x16x32_bf16 v[80:83], v[240:243], v[208:211], v[80:83]\n\t"
    "s_waitcnt lgkmcnt(6)\n\t"
    "v_mfma_f32_16x16x32_bf16 v[84:87], v[216:219], v[192:195], v[84:87]\n\t"
    "ds_read_b64_tr_b16 v[232:233], v249 offset:25088\n\t"
    "v_mfma_f32_16x16x32_bf16 v[88:91], v[216:219], v[200:203], v[88:91]\n\t"
    "ds_read_b64_tr_b16 v[234:235], v249 offset:25344\n\t"
    "v_mfma_f32_16x16x32_bf16 v[92:95], v[216:219], v[208:211], v[92:95]\n\t"
    "s_waitcnt lgkmcnt(6)\n\t"
    "v_mfma_f32_16x16x32_bf16 v[0:3], v[220:223], v[196:199], v[0:3]\n\t"
    "ds_read_b64_tr_b16 v[236:237], v251 offset:25600\n\t"
    "v_mfma_f32_16x16x32_bf16 v[4:7], v[220:223], v[204:207], v[4:7]\n\t"
    "ds_read_b64_tr_b16 v[238:239], v251 offset:25856\n\t"
    "v_mfma_f32_16x16x32_bf16 v[8:11], v[220:223], v[212:215], v[8:11]\n\t"
    "ds_read_b128 v[192:195], v252 offset:0\n\t"
    "s_waitcnt lgkmcnt(7)\n\t"
    "v_mfma_f32_16x16x32_bf16 v[12:15], v[224:227], v[196:199], v[12:15]\n\t"
    "ds_read_b64_tr_b16 v[240:241], v249 offset:25600\n\t"
    "v_mfma_f32_16x16x32_bf16 v[16:19], v[224:227], v[204:207], v[16:19]\n\t"
    "ds_read_b64_tr_b16 v[242:243], v249 offset:25856\n\t"
    "v_mfma_f32_16x16x32_bf16 v[20:23], v[224:227], v[212:215], v[20:23]\n\t"
    "ds_read_b128 v[200:203], v252 offset:2048\n\t"
    "s_waitcnt lgkmcnt(8)\n\t"
    "v_mfma_f32_16x16x32_bf16 v[24:27], v[228:231], v[196:199], v[24:27]\n\t"
    "ds_read_b64_tr_b16 v[216:217], v251 offset:26112\n\t"
    "v_mfma_f32_16x16x32_bf16 v[28:31], v[228:231], v[204:207], v[28:31]\n\t"
    "ds_read_b64_tr_b16 v[218:219], v251 offset:26368\n\t"
    "v_mfma_f32_16x16x32_bf16 v[32:35], v[228:231], v[212:215], v[32:35]\n\t"
    "ds_read_b128 v[208:211], v252 offset:4096\n\t"
    "s_waitcnt lgkmcnt(9)\n\t"
    "v_mfma_f32_16x16x32_bf16 v[36:39], v[232:235], v[196:199], v[36:39]\n\t"
    "ds_read_b64_tr_b16 v[220:221], v249 offset:26112\n\t"
    "v_mfma_f32_16x16x32_bf16 v[40:43], v[232:235], v[204:207], v[40:43]\n\t"
    "ds_read_b64_tr_b16 v[222:223], v249 offset:26368\n\t"
    "v_mfma_f32_16x16x32_bf16 v[44:47], v[232:235], v[212:215], v[44:47]\n\t"
    "s_waitcnt lgkmcnt(9)\n\t"
    "v_mfma_f32_16x16x32_bf16 v[48:51], v[236:239], v[196:199], v[48:51]\n\t"
    "v_mfma_f32_16x16x32_bf16 v[52:55], v[236:239], v[204:207], v[52:55]\n\t"
    "v_mfma_f32_16x16x32_bf16 v[56:59], v[236:239], v[212:215], v[56:59]\n\t"
    "s_waitcnt lgkmcnt(0)\n\t"
    "s_barrier\n\t"
    "v_mfma_f32_16x16x32_bf16 v[60:63], v[240:243], v[196:199], v[60:63]\n\t"
    "ds_read_b64_tr_b16 v[224:225], v251 offset:32768\n\t"
    "v_mfma_f32_16x16x32_bf16 v[64:67], v[240:243], v[204:207], v[64:67]\n\t"
    "ds_read_b64_tr_b16 v[226:227], v251 offset:33024\n\t"
    "v_mfma_f32_16x16x32_bf16 v[68:71], v[240:243], v[212:215], v[68:71]\n\t"
    "v_mfma_f32_16x16x32_bf16 v[72:75], v[216:219], v[196:199], v[72:75]\n\t"
    "ds_read_b64_tr_b16 v[228:229], v249 offset:32768\n\t"
    "ds_read_b64_tr_b16 v[230:231], v249 offset:33024\n\t"
    "v_mfma_f32_16x16x32_bf16 v[76:79], v[216:219], v[204:207], v[76:79]\n\t"
    "ds_read_b64_tr_b16 v[232:233], v251 offset:33280\n\t"
    "v_mfma_f32_16x16x32_bf16 v[80:83], v[216:219], v[212:215], v[80:83]\n\t"
    "ds_read_b64_tr_b16 v[234:235], v251 offset:33536\n\t"
    "v_mfma_f32_16x16x32_bf16 v[84:87], v[220:223], v[196:199], v[84:87]\n\t"
    "ds_read_b64_tr_b16 v[236:237], v249 offset:33280\n\t"
    "v_mfma_f32_16x16x32_bf16 v[88:91], v[220:223], v[204:207], v[88:91]\n\t"
    "ds_read_b64_tr_b16 v[238:239], v249 offset:33536\n\t"
    "v_mfma_f32_16x16x32_bf16 v[92:95], v[220:223], v[212:215], v[92:95]\n\t"
    "s_waitcnt lgkmcnt(6)\n\t"
    "v_mfma_f32_16x16x32_bf16 v[96:99], v[224:227], v[192:195], v[96:99]\n\t"
    "ds_read_b64_tr_b16 v[240:241], v251 offset:33792\n\t"
    "v_mfma_f32_16x16x32_bf16 v[100:103], v[224:227], v[200:203], v[100:103]\n\t"
    "ds_read_b64_tr_b16 v[242:243], v251 offset:34048\n\t"
    "v_mfma_f32_16x16x32_bf16 v[104:107], v[224:227], v[208:211], v[104:107]\n\t"
    "ds_read_b128 v[196:199], v250 offset:0\n\t"
    "s_waitcnt lgkmcnt(7)\n\t"
    "v_mfma_f32_16x16x32_bf16 v[108:111], v[228:231], v[192:195], v[108:111]\n\t"
    "ds_read_b64_tr_b16 v[216:217], v249 offset:33792\n\t"
    "v_mfma_f32_16x16x32_bf16 v[112:115], v[228:231], v[200:203], v[112:115]\n\t"
    "ds_read_b64_tr_b16 v[218:219], v249 offset:34048\n\t"
    "v_mfma_f32_16x16x32_bf16 v[116:119], v[228:231], v[208:211], v[116:119]\n\t"
    "ds_read_b128 v[204:207], v250 offset:2048\n\t"
    "s_waitcnt lgkmcnt(8)\n\t"
    "v_mfma_f32_16x16x32_bf16 v[120:123], v[232:235], v[192:195], v[120:123]\n\t"
    "ds_read_b64_tr_b16 v[220:221], v251 offset:34304\n\t"
    "v_mfma_f32_16x16x32_bf16 v[124:127], v[232:235], v[200:203], v[124:127]\n\t"
    "ds_read_b64_tr_b16 v[222:223], v251 offset:34560\n\t"
    "v_mfma_f32_16x16x32_bf16 v[128:131], v[232:235], v[208:211], v[128:131]\n\t"
    "ds_read_b128 v[212:215], v250 offset:4096\n\t"
    "s_waitcnt lgkmcnt(9)\n\t"
    "v_mfma_f32_16x16x32_bf16 v[132:135], v[236:239], v[192:195], v[132:135]\n\t"
    "ds_read_b64_tr_b16 v[224:225], v249 offset:34304\n\t"
    "v_mfma_f32_16x16x32_bf16 v[136:139], v[236:239], v[200:203], v[136:139]\n\t"
    "ds_read_b64_tr_b16 v[226:227], v249 offset:34560\n\t"
    "v_mfma_f32_16x16x32_bf16 v[140:143], v[236:239], v[208:211], v[140:143]\n\t"
    "s_waitcnt lgkmcnt(9)\n\t"
    "v_mfma_f32_16x16x32_bf16 v[144:147], v[240:243], v[192:195], v[144:147]\n\t"
    "ds_read_b64_tr_b16 v[228:229], v251 offset:40960\n\t"
    "v_mfma_f32_16x16x32_bf16 v[148:151], v[240:243], v[200:203], v[148:151]\n\t"
    "ds_read_b64_tr_b16 v[230:231], v251 offset:41216\n\t"
    "v_mfma_f32_16x16x32_bf16 v[152:155], v[240:243], v[208:211], v[152:155]\n\t"
    "s_waitcnt lgkmcnt(8)\n\t"
    "v_mfma_f32_16x16x32_bf16 v[156:159], v[216:219], v[192:195], v[156:159]\n\t"
    "ds_read_b64_tr_b16 v[232:233], v249 offset:40960\n\t"
    "v_mfma_f32_16x16x32_bf16 v[160:163], v[216:219], v[200:203], v[160:163]\n\t"
    "ds_read_b64_tr_b16 v[234:235], v249 offset:41216\n\t"
    "v_mfma_f32_16x16x32_bf16 v[164:167], v[216:219], v[208:211], v[164:167]\n\t"
    "s_waitcnt lgkmcnt(7)\n\t"
    "v_mfma_f32_16x16x32_bf16 v[168:171], v[220:223], v[192:195], v[168:171]\n\t"
    "ds_read_b64_tr_b16 v[236:237], v251 offset:41472\n\t"
    "v_mfma_f32_16x16x32_bf16 v[172:175], v[220:223], v[200:203], v[172:175]\n\t"
    "ds_read_b64_tr_b16 v[238:239], v251 offset:41728\n\t"
    "v_mfma_f32_16x16x32_bf16 v[176:179], v[220:223], v[208:211], v[176:179]\n\t"
    "s_waitcnt lgkmcnt(6)\n\t"
    "v_mfma_f32_16x16x32_bf16 v[180:183], v[224:227], v[192:195], v[180:183]\n\t"
    "ds_read_b64_tr_b16 v[240:241], v249 offset:41472\n\t"
    "v_mfma_f32_16x16x32_bf16 v[184:187], v[224:227], v[200:203], v[184:187]\n\t"
    "ds_read_b64_tr_b16 v[242:243], v249 offset:41728\n\t"
    "v_mfma_f32_16x16x32_bf16 v[188:191], v[224:227], v[208:211], v[188:191]\n\t"
    "s_waitcnt lgkmcnt(6)\n\t"
    "v_mfma_f32_16x16x32_bf16 v[96:99], v[228:231], v[196:199], v[96:99]\n\t"
    "ds_read_b64_tr_b16 v[216:217], v251 offset:41984\n\t"
    "v_mfma_f32_16x16x32_bf16 v[100:103], v[228:231], v[204:207], v[100:103]\n\t"
    "ds_read_b64_tr_b16 v[218:219], v251 offset:42240\n\t"
    "v_mfma_f32_16x16x32_bf16 v[104:107], v[228:231], v[212:215], v[104:107]\n\t"
    "s_waitcnt lgkmcnt(6)\n\t"
    "v_mfma_f32_16x16x32_bf16 v[108:111], v[232:235], v[196:199], v[108:111]\n\t"
    "ds_read_b64_tr_b16 v[220:221], v249 offset:41984\n\t"
    "v_mfma_f32_16x16x32_bf16 v[112:115], v[232:235], v[204:207], v[112:115]\n\t"
    "ds_read_b64_tr_b16 v[222:223], v249 offset:42240\n\t"
    "v_mfma_f32_16x16x32_bf16 v[116:119], v[232:235], v[212:215], v[116:119]\n\t"
    "s_waitcnt lgkmcnt(6)\n\t"
    "v_mfma_f32_16x16x32_bf16 v[120:123], v[236:239], v[196:199], v[120:123]\n\t"
    "ds_read_b64_tr_b16 v[224:225], v251 offset:42496\n\t"
    "v_mfma_f32_16x16x32_bf16 v[124:127], v[236:239], v[204:207], v[124:127]\n\t"
    "ds_read_b64_tr_b16 v[226:227], v251 offset:42752\n\t"
    "v_mfma_f32_16x16x32_bf16 v[128:131], v[236:239], v[212:215], v[128:131]\n\t"
    "s_waitcnt lgkmcnt(6)\n\t"
    "v_mfma_f32_16x16x32_bf16 v[132:135], v[240:243], v[196:199], v[132:135]\n\t"
    "ds_read_b64_tr_b16 v[228:229], v249 offset:42496\n\t"
    "v_mfma_f32_16x16x32_bf16 v[136:139], v[240:243], v[204:207], v[136:139]\n\t"
    "ds_read_b64_tr_b16 v[230:231], v249 offset:42752\n\t"
    "v_mfma_f32_16x16x32_bf16 v[140:143], v[240:243], v[212:215], v[140:143]\n\t"
    "s_waitcnt lgkmcnt(6)\n\t"
    "v_mfma_f32_16x16x32_bf16 v[144:147], v[216:219], v[196:199], v[144:147]\n\t"
    "v_mfma_f32_16x16x32_bf16 v[148:151], v[216:219], v[204:207], v[148:151]\n\t"
    "v_mfma_f32_16x16x32_bf16 v[152:155], v[216:219], v[212:215], v[152:155]\n\t"
    "s_waitcnt lgkmcnt(0)\n\t"
    "s_barrier\n\t"
    "v_mfma_f32_16x16x32_bf16 v[156:159], v[220:223], v[196:199], v[156:159]\n\t"
    "ds_read_b64_tr_b16 v[232:233], v251 offset:0\n\t"
    "v_mfma_f32_16x16x32_bf16 v[160:163], v[220:223], v[204:207], v[160:163]\n\t"
    "ds_read_b64_tr_b16 v[234:235], v251 offset:256\n\t"
    "v_mfma_f32_16x16x32_bf16 v[164:167], v[220:223], v[212:215], v[164:167]\n\t"
    "v_mfma_f32_16x16x32_bf16 v[168:171], v[224:227], v[196:199], v[168:171]\n\t"
    "ds_read_b64_tr_b16 v[236:237], v249 offset:0\n\t"
    "ds_read_b64_tr_b16 v[238:239], v249 offset:256\n\t"
    "v_mfma_f32_16x16x32_bf16 v[172:175], v[224:227], v[204:207], v[172:175]\n\t"
    "ds_read_b64_tr_b16 v[240:241], v251 offset:512\n\t"
    "v_mfma_f32_16x16x32_bf16 v[176:179], v[224:227], v[212:215], v[176:179]\n\t"
    "ds_read_b64_tr_b16 v[242:243], v251 offset:768\n\t"
    "v_mfma_f32_16x16x32_bf16 v[180:183], v[228:231], v[196:199], v[180:183]\n\t"
    "ds_read_b64_tr_b16 v[216:217], v249 offset:512\n\t"
    "v_mfma_f32_16x16x32_bf16 v[184:187], v[228:231], v[204:207], v[184:187]\n\t"
    "ds_read_b64_tr_b16 v[218:219], v249 offset:768\n\t"
    "v_mfma_f32_16x16x32_bf16 v[188:191], v[228:231], v[212:215], v[188:191]\n\t"
    "s_waitcnt lgkmcnt(6)\n\t"
    "v_mfma_f32_16x16x32_bf16 v[0:3], v[232:235], v[192:195], v[0:3]\n\t"
    "ds_read_b64_tr_b16 v[220:221], v251 offset:1024\n\t"
    "v_mfma_f32_16x16x32_bf16 v[4:7], v[232:235], v[200:203], v[4:7]\n\t"
    "ds_read_b64_tr_b16 v[222:223], v251 offset:1280\n\t"
    "v_mfma_f32_16x16x32_bf16 v[8:11], v[232:235], v[208:211], v[8:11]\n\t"
    "s_waitcnt lgkmcnt(6)\n\t"
    "v_mfma_f32_16x16x32_bf16 v[12:15], v[236:239], v[192:195], v[12:15]\n\t"
    "ds_read_b64_tr_b16 v[224:225], v249 offset:1024\n\t"
    "v_mfma_f32_16x16x32_bf16 v[16:19], v[236:239], v[200:203], v[16:19]\n\t"
    "ds_read_b64_tr_b16 v[226:227], v249 offset:1280\n\t"
    "v_mfma_f32_16x16x32_bf16 v[20:23], v[236:239], v[208:211], v[20:23]\n\t"
    "s_waitcnt lgkmcnt(6)\n\t"
    "v_mfma_f32_16x16x32_bf16 v[24:27], v[240:243], v[192:195], v[24:27]\n\t"
    "ds_read_b64_tr_b16 v[228:229], v251 offset:1536\n\t"
    "v_mfma_f32_16x16x32_bf16 v[28:31], v[240:243], v[200:203], v[28:31]\n\t"
    "ds_read_b64_tr_b16 v[230:231], v251 offset:1792\n\t"
    "v_mfma_f32_16x16x32_bf16 v[32:35], v[240:243], v[208:211], v[32:35]\n\t"
    "s_waitcnt lgkmcnt(6)\n\t"
    "v_mfma_f32_16x16x32_bf16 v[36:39], v[216:219], v[192:195], v[36:39]\n\t"
    "ds_read_b64_tr_b16 v[232:233], v249 offset:1536\n\t"
    "v_mfma_f32_16x16x32_bf16 v[40:43], v[216:219], v[200:203], v[40:43]\n\t"
    "ds_read_b64_tr_b16 v[234:235], v249 offset:1792\n\t"
    "v_mfma_f32_16x16x32_bf16 v[44:47], v[216:219], v[208:211], v[44:47]\n\t"
    "s_waitcnt lgkmcnt(6)\n\t"
    "v_mfma_f32_16x16x32_bf16 v[48:51], v[220:223], v[192:195], v[48:51]\n\t"
    "ds_read_b64_tr_b16 v[236:237], v251 offset:8192\n\t"
    "v_mfma_f32_16x16x32_bf16 v[52:55], v[220:223], v[200:203], v[52:55]\n\t"
    "ds_read_b64_tr_b16 v[238:239], v251 offset:8448\n\t"
    "v_mfma_f32_16x16x32_bf16 v[56:59], v[220:223], v[208:211], v[56:59]\n\t"
    "s_waitcnt lgkmcnt(6)\n\t"
    "v_mfma_f32_16x16x32_bf16 v[60:63], v[224:227], v[192:195], v[60:63]\n\t"
    "ds_read_b64_tr_b16 v[240:241], v249 offset:8192\n\t"
    "v_mfma_f32_16x16x32_bf16 v[64:67], v[224:227], v[200:203], v[64:67]\n\t"
    "ds_read_b64_tr_b16 v[242:243], v249 offset:8448\n\t"
    "v_mfma_f32_16x16x32_bf16 v[68:71], v[224:227], v[208:211], v[68:71]\n\t"
    "s_waitcnt lgkmcnt(6)\n\t"
    "v_mfma_f32_16x16x32_bf16 v[72:75], v[228:231], v[192:195], v[72:75]\n\t"
    "ds_read_b64_tr_b16 v[216:217], v251 offset:8704\n\t"
    "v_mfma_f32_16x16x32_bf16 v[76:79], v[228:231], v[200:203], v[76:79]\n\t"
    "ds_read_b64_tr_b16 v[218:219], v251 offset:8960\n\t"
    "v_mfma_f32_16x16x32_bf16 v[80:83], v[228:231], v[208:211], v[80:83]\n\t"
    "s_waitcnt lgkmcnt(6)\n\t"
    "v_mfma_f32_16x16x32_bf16 v[84:87], v[232:235], v[192:195], v[84:87]\n\t"
    "ds_read_b64_tr_b16 v[220:221], v249 offset:8704\n\t"
    "v_mfma_f32_16x16x32_bf16 v[88:91], v[232:235], v[200:203], v[88:91]\n\t"
    "ds_read_b64_tr_b16 v[222:223], v249 offset:8960\n\t"
    "v_mfma_f32_16x16x32_bf16 v[92:95], v[232:235], v[208:211], v[92:95]\n\t"
    "s_waitcnt lgkmcnt(6)\n\t"
    "v_mfma_f32_16x16x32_bf16 v[0:3], v[236:239], v[196:199], v[0:3]\n\t"
    "ds_read_b64_tr_b16 v[224:225], v251 offset:9216\n\t"
    "v_mfma_f32_16x16x32_bf16 v[4:7], v[236:239], v[204:207], v[4:7]\n\t"
    "ds_read_b64_tr_b16 v[226:227], v251 offset:9472\n\t"
    "v_mfma_f32_16x16x32_bf16 v[8:11], v[236:239], v[212:215], v[8:11]\n\t"
    "ds_read_b128 v[192:195], v252 offset:24576\n\t"
    "s_waitcnt lgkmcnt(7)\n\t"
    "v_mfma_f32_16x16x32_bf16 v[12:15], v[240:243], v[196:199], v[12:15]\n\t"
    "ds_read_b64_tr_b16 v[228:229], v249 offset:9216\n\t"
    "v_mfma_f32_16x16x32_bf16 v[16:19], v[240:243], v[204:207], v[16:19]\n\t"
    "ds_read_b64_tr_b16 v[230:231], v249 offset:9472\n\t"
    "v_mfma_f32_16x16x32_bf16 v[20:23], v[240:243], v[212:215], v[20:23]\n\t"
    "ds_read_b128 v[200:203], v252 offset:26624\n\t"
    "s_waitcnt lgkmcnt(8)\n\t"
    "v_mfma_f32_16x16x32_bf16 v[24:27], v[216:219], v[196:199], v[24:27]\n\t"
    "ds_read_b64_tr_b16 v[232:233], v251 offset:9728\n\t"
    "v_mfma_f32_16x16x32_bf16 v[28:31], v[216:219], v[204:207], v[28:31]\n\t"
    "ds_read_b64_tr_b16 v[234:235], v251 offset:9984\n\t"
    "v_mfma_f32_16x16x32_bf16 v[32:35], v[216:219], v[212:215], v[32:35]\n\t"
    "ds_read_b128 v[208:211], v252 offset:28672\n\t"
    "s_waitcnt lgkmcnt(9)\n\t"
    "v_mfma_f32_16x16x32_bf16 v[36:39], v[220:223], v[196:199], v[36:39]\n\t"
    "ds_read_b64_tr_b16 v[236:237], v249 offset:9728\n\t"
    "v_mfma_f32_16x16x32_bf16 v[40:43], v[220:223], v[204:207], v[40:43]\n\t"
    "ds_read_b64_tr_b16 v[238:239], v249 offset:9984\n\t"
    "v_mfma_f32_16x16x32_bf16 v[44:47], v[220:223], v[212:215], v[44:47]\n\t"
    "s_waitcnt lgkmcnt(9)\n\t"
    "v_mfma_f32_16x16x32_bf16 v[48:51], v[224:227], v[196:199], v[48:51]\n\t"
    "v_mfma_f32_16x16x32_bf16 v[52:55], v[224:227], v[204:207], v[52:55]\n\t"
    "v_mfma_f32_16x16x32_bf16 v[56:59], v[224:227], v[212:215], v[56:59]\n\t"
    "s_waitcnt lgkmcnt(0)\n\t"
    "s_barrier\n\t"
    "v_mfma_f32_16x16x32_bf16 v[60:63], v[228:231], v[196:199], v[60:63]\n\t"
    "ds_read_b64_tr_b16 v[240:241], v251 offset:16384\n\t"
    "v_mfma_f32_16x16x32_bf16 v[64:67], v[228:231], v[204:207], v[64:67]\n\t"
    "ds_read_b64_tr_b16 v[242:243], v251 offset:16640\n\t"
    "v_mfma_f32_16x16x32_bf16 v[68:71], v[228:231], v[212:215], v[68:71]\n\t"
    "v_mfma_f32_16x16x32_bf16 v[72:75], v[232:235], v[196:199], v[72:75]\n\t"
    "ds_read_b64_tr_b16 v[216:217], v249 offset:16384\n\t"
    "ds_read_b64_tr_b16 v[218:219], v249 offset:16640\n\t"
    "v_mfma_f32_16x16x32_bf16 v[76:79], v[232:235], v[204:207], v[76:79]\n\t"
    "ds_read_b64_tr_b16 v[220:221], v251 offset:16896\n\t"
    "v_mfma_f32_16x16x32_bf16 v[80:83], v[232:235], v[212:215], v[80:83]\n\t"
    "ds_read_b64_tr_b16 v[222:223], v251 offset:17152\n\t"
    "v_mfma_f32_16x16x32_bf16 v[84:87], v[236:239], v[196:199], v[84:87]\n\t"
    "ds_read_b64_tr_b16 v[224:225], v249 offset:16896\n\t"
    "v_mfma_f32_16x16x32_bf16 v[88:91], v[236:239], v[204:207], v[88:91]\n\t"
    "ds_read_b64_tr_b16 v[226:227], v249 offset:17152\n\t"
    "v_mfma_f32_16x16x32_bf16 v[92:95], v[236:239], v[212:215], v[92:95]\n\t"
    "s_waitcnt lgkmcnt(6)\n\t"
    "v_mfma_f32_16x16x32_bf16 v[96:99], v[240:243], v[192:195], v[96:99]\n\t"
    "ds_read_b64_tr_b16 v[228:229], v251 offset:17408\n\t"
    "v_mfma_f32_16x16x32_bf16 v[100:103], v[240:243], v[200:203], v[100:103]\n\t"
    "ds_read_b64_tr_b16 v[230:231], v251 offset:17664\n\t"
    "v_mfma_f32_16x16x32_bf16 v[104:107], v[240:243], v[208:211], v[104:107]\n\t"
    "ds_read_b128 v[196:199], v250 offset:24576\n\t"
    "s_waitcnt lgkmcnt(7)\n\t"
    "v_mfma_f32_16x16x32_bf16 v[108:111], v[216:219], v[192:195], v[108:111]\n\t"
    "ds_read_b64_tr_b16 v[232:233], v249 offset:17408\n\t"
    "v_mfma_f32_16x16x32_bf16 v[112:115], v[216:219], v[200:203], v[112:115]\n\t"
    "ds_read_b64_tr_b16 v[234:235], v249 offset:17664\n\t"
    "v_mfma_f32_16x16x32_bf16 v[116:119], v[216:219], v[208:211], v[116:119]\n\t"
    "ds_read_b128 v[204:207], v250 offset:26624\n\t"
    "s_waitcnt lgkmcnt(8)\n\t"
    "v_mfma_f32_16x16x32_bf16 v[120:123], v[220:223], v[192:195], v[120:123]\n\t"
    "ds_read_b64_tr_b16 v[236:237], v251 offset:17920\n\t"
    "v_mfma_f32_16x16x32_bf16 v[124:127], v[220:223], v[200:203], v[124:127]\n\t"
    "ds_read_b64_tr_b16 v[238:239], v251 offset:18176\n\t"
    "v_mfma_f32_16x16x32_bf16 v[128:131], v[220:223], v[208:211], v[128:131]\n\t"
    "ds_read_b128 v[212:215], v250 offset:28672\n\t"
    "s_waitcnt lgkmcnt(9)\n\t"
    "v_mfma_f32_16x16x32_bf16 v[132:135], v[224:227], v[192:195], v[132:135]\n\t"
    "ds_read_b64_tr_b16 v[240:241], v249 offset:17920\n\t"
    "v_mfma_f32_16x16x32_bf16 v[136:139], v[224:227], v[200:203], v[136:139]\n\t"
    "ds_read_b64_tr_b16 v[242:243], v249 offset:18176\n\t"
    "v_mfma_f32_16x16x32_bf16 v[140:143], v[224:227], v[208:211], v[140:143]\n\t"
    "s_waitcnt lgkmcnt(9)\n\t"
    "v_mfma_f32_16x16x32_bf16 v[144:147], v[228:231], v[192:195], v[144:147]\n\t"
    "ds_read_b64_tr_b16 v[216:217], v251 offset:24576\n\t"
    "v_mfma_f32_16x16x32_bf16 v[148:151], v[228:231], v[200:203], v[148:151]\n\t"
    "ds_read_b64_tr_b16 v[218:219], v251 offset:24832\n\t"
    "v_mfma_f32_16x16x32_bf16 v[152:155], v[228:231], v[208:211], v[152:155]\n\t"
    "s_waitcnt lgkmcnt(8)\n\t"
    "v_mfma_f32_16x16x32_bf16 v[156:159], v[232:235], v[192:195], v[156:159]\n\t"
    "ds_read_b64_tr_b16 v[220:221], v249 offset:24576\n\t"
    "v_mfma_f32_16x16x32_bf16 v[160:163], v[232:235], v[200:203], v[160:163]\n\t"
    "ds_read_b64_tr_b16 v[222:223], v249 offset:24832\n\t"
    "v_mfma_f32_16x16x32_bf16 v[164:167], v[232:235], v[208:211], v[164:167]\n\t"
    "s_waitcnt lgkmcnt(7)\n\t"
    "v_mfma_f32_16x16x32_bf16 v[168:171], v[236:239], v[192:195], v[168:171]\n\t"
    "ds_read_b64_tr_b16 v[224:225], v251 offset:25088\n\t"
    "v_mfma_f32_16x16x32_bf16 v[172:175], v[236:239], v[200:203], v[172:175]\n\t"
    "ds_read_b64_tr_b16 v[226:227], v251 offset:25344\n\t"
    "v_mfma_f32_16x16x32_bf16 v[176:179], v[236:239], v[208:211], v[176:179]\n\t"
    "s_waitcnt lgkmcnt(6)\n\t"
    "v_mfma_f32_16x16x32_bf16 v[180:183], v[240:243], v[192:195], v[180:183]\n\t"
    "ds_read_b64_tr_b16 v[228:229], v249 offset:25088\n\t"
    "v_mfma_f32_16x16x32_bf16 v[184:187], v[240:243], v[200:203], v[184:187]\n\t"
    "ds_read_b64_tr_b16 v[230:231], v249 offset:25344\n\t"
    "v_mfma_f32_16x16x32_bf16 v[188:191], v[240:243], v[208:211], v[188:191]\n\t"
    "s_waitcnt lgkmcnt(6)\n\t"
    "v_mfma_f32_16x16x32_bf16 v[96:99], v[216:219], v[196:199], v[96:99]\n\t"
    "ds_read_b64_tr_b16 v[232:233], v251 offset:25600\n\t"
    "v_mfma_f32_16x16x32_bf16 v[100:103], v[216:219], v[204:207], v[100:103]\n\t"
    "ds_read_b64_tr_b16 v[234:235], v251 offset:25856\n\t"
    "v_mfma_f32_16x16x32_bf16 v[104:107], v[216:219], v[212:215], v[104:107]\n\t"
    "s_waitcnt lgkmcnt(6)\n\t"
    "v_mfma_f32_16x16x32_bf16 v[108:111], v[220:223], v[196:199], v[108:111]\n\t"
    "ds_read_b64_tr_b16 v[236:237], v249 offset:25600\n\t"
    "v_mfma_f32_16x16x32_bf16 v[112:115], v[220:223], v[204:207], v[112:115]\n\t"
    "ds_read_b64_tr_b16 v[238:239], v249 offset:25856\n\t"
    "v_mfma_f32_16x16x32_bf16 v[116:119], v[220:223], v[212:215], v[116:119]\n\t"
    "s_waitcnt lgkmcnt(6)\n\t"
    "v_mfma_f32_16x16x32_bf16 v[120:123], v[224:227], v[196:199], v[120:123]\n\t"
    "ds_read_b64_tr_b16 v[240:241], v251 offset:26112\n\t"
    "v_mfma_f32_16x16x32_bf16 v[124:127], v[224:227], v[204:207], v[124:127]\n\t"
    "ds_read_b64_tr_b16 v[242:243], v251 offset:26368\n\t"
    "v_mfma_f32_16x16x32_bf16 v[128:131], v[224:227], v[212:215], v[128:131]\n\t"
    "s_waitcnt lgkmcnt(6)\n\t"
    "v_mfma_f32_16x16x32_bf16 v[132:135], v[228:231], v[196:199], v[132:135]\n\t"
    "ds_read_b64_tr_b16 v[216:217], v249 offset:26112\n\t"
    "v_mfma_f32_16x16x32_bf16 v[136:139], v[228:231], v[204:207], v[136:139]\n\t"
    "ds_read_b64_tr_b16 v[218:219], v249 offset:26368\n\t"
    "v_mfma_f32_16x16x32_bf16 v[140:143], v[228:231], v[212:215], v[140:143]\n\t"
    "s_waitcnt lgkmcnt(6)\n\t"
    "v_mfma_f32_16x16x32_bf16 v[144:147], v[232:235], v[196:199], v[144:147]\n\t"
    "v_mfma_f32_16x16x32_bf16 v[148:151], v[232:235], v[204:207], v[148:151]\n\t"
    "v_mfma_f32_16x16x32_bf16 v[152:155], v[232:235], v[212:215], v[152:155]\n\t"
    "s_waitcnt lgkmcnt(0)\n\t"
    "s_barrier\n\t"
    "v_mfma_f32_16x16x32_bf16 v[156:159], v[236:239], v[196:199], v[156:159]\n\t"
    "ds_read_b64_tr_b16 v[220:221], v251 offset:32768\n\t"
    "v_mfma_f32_16x16x32_bf16 v[160:163], v[236:239], v[204:207], v[160:163]\n\t"
    "ds_read_b64_tr_b16 v[222:223], v251 offset:33024\n\t"
    "v_mfma_f32_16x16x32_bf16 v[164:167], v[236:239], v[212:215], v[164:167]\n\t"
    "v_mfma_f32_16x16x32_bf16 v[168:171], v[240:243], v[196:199], v[168:171]\n\t"
    "ds_read_b64_tr_b16 v[224:225], v249 offset:32768\n\t"
    "ds_read_b64_tr_b16 v[226:227], v249 offset:33024\n\t"
    "v_mfma_f32_16x16x32_bf16 v[172:175], v[240:243], v[204:207], v[172:175]\n\t"
    "ds_read_b64_tr_b16 v[228:229], v251 offset:33280\n\t"
    "v_mfma_f32_16x16x32_bf16 v[176:179], v[240:243], v[212:215], v[176:179]\n\t"
    "ds_read_b64_tr_b16 v[230:231], v251 offset:33536\n\t"
    "v_mfma_f32_16x16x32_bf16 v[180:183], v[216:219], v[196:199], v[180:183]\n\t"
    "ds_read_b64_tr_b16 v[232:233], v249 offset:33280\n\t"
    "v_mfma_f32_16x16x32_bf16 v[184:187], v[216:219], v[204:207], v[184:187]\n\t"
    "ds_read_b64_tr_b16 v[234:235], v249 offset:33536\n\t"
    "v_mfma_f32_16x16x32_bf16 v[188:191], v[216:219], v[212:215], v[188:191]\n\t"
    "s_waitcnt lgkmcnt(6)\n\t"
    "v_mfma_f32_16x16x32_bf16 v[0:3], v[220:223], v[192:195], v[0:3]\n\t"
    "ds_read_b64_tr_b16 v[236:237], v251 offset:33792\n\t"
    "v_mfma_f32_16x16x32_bf16 v[4:7], v[220:223], v[200:203], v[4:7]\n\t"
    "ds_read_b64_tr_b16 v[238:239], v251 offset:34048\n\t"
    "v_mfma_f32_16x16x32_bf16 v[8:11], v[220:223], v[208:211], v[8:11]\n\t"
    "s_waitcnt lgkmcnt(6)\n\t"
    "v_mfma_f32_16x16x32_bf16 v[12:15], v[224:227], v[192:195], v[12:15]\n\t"
    "ds_read_b64_tr_b16 v[240:241], v249 offset:33792\n\t"
    "v_mfma_f32_16x16x32_bf16 v[16:19], v[224:227], v[200:203], v[16:19]\n\t"
    "ds_read_b64_tr_b16 v[242:243], v249 offset:34048\n\t"
    "v_mfma_f32_16x16x32_bf16 v[20:23], v[224:227], v[208:211], v[20:23]\n\t"
    "s_waitcnt lgkmcnt(6)\n\t"
    "v_mfma_f32_16x16x32_bf16 v[24:27], v[228:231], v[192:195], v[24:27]\n\t"
    "ds_read_b64_tr_b16 v[216:217], v251 offset:34304\n\t"
    "v_mfma_f32_16x16x32_bf16 v[28:31], v[228:231], v[200:203], v[28:31]\n\t"
    "ds_read_b64_tr_b16 v[218:219], v251 offset:34560\n\t"
    "v_mfma_f32_16x16x32_bf16 v[32:35], v[228:231], v[208:211], v[32:35]\n\t"
    "s_waitcnt lgkmcnt(6)\n\t"
    "v_mfma_f32_16x16x32_bf16 v[36:39], v[232:235], v[192:195], v[36:39]\n\t"
    "ds_read_b64_tr_b16 v[220:221], v249 offset:34304\n\t"
    "v_mfma_f32_16x16x32_bf16 v[40:43], v[232:235], v[200:203], v[40:43]\n\t"
    "ds_read_b64_tr_b16 v[222:223], v249 offset:34560\n\t"
    "v_mfma_f32_16x16x32_bf16 v[44:47], v[232:235], v[208:211], v[44:47]\n\t"
    "s_waitcnt lgkmcnt(6)\n\t"
    "v_mfma_f32_16x16x32_bf16 v[48:51], v[236:239], v[192:195], v[48:51]\n\t"
    "ds_read_b64_tr_b16 v[224:225], v251 offset:40960\n\t"
    "v_mfma_f32_16x16x32_bf16 v[52:55], v[236:239], v[200:203], v[52:55]\n\t"
    "ds_read_b64_tr_b16 v[226:227], v251 offset:41216\n\t"
    "v_mfma_f32_16x16x32_bf16 v[56:59], v[236:239], v[208:211], v[56:59]\n\t"
    "s_waitcnt lgkmcnt(6)\n\t"
    "v_mfma_f32_16x16x32_bf16 v[60:63], v[240:243], v[192:195], v[60:63]\n\t"
    "ds_read_b64_tr_b16 v[228:229], v249 offset:40960\n\t"
    "v_mfma_f32_16x16x32_bf16 v[64:67], v[240:243], v[200:203], v[64:67]\n\t"
    "ds_read_b64_tr_b16 v[230:231], v249 offset:41216\n\t"
    "v_mfma_f32_16x16x32_bf16 v[68:71], v[240:243], v[208:211], v[68:71]\n\t"
    "s_waitcnt lgkmcnt(6)\n\t"
    "v_mfma_f32_16x16x32_bf16 v[72:75], v[216:219], v[192:195], v[72:75]\n\t"
    "ds_read_b64_tr_b16 v[232:233], v251 offset:41472\n\t"
    "v_mfma_f32_16x16x32_bf16 v[76:79], v[216:219], v[200:203], v[76:79]\n\t"
    "ds_read_b64_tr_b16 v[234:235], v251 offset:41728\n\t"
    "v_mfma_f32_16x16x32_bf16 v[80:83], v[216:219], v[208:211], v[80:83]\n\t"
    "s_waitcnt lgkmcnt(6)\n\t"
    "v_mfma_f32_16x16x32_bf16 v[84:87], v[220:223], v[192:195], v[84:87]\n\t"
    "ds_read_b64_tr_b16 v[236:237], v249 offset:41472\n\t"
    "v_mfma_f32_16x16x32_bf16 v[88:91], v[220:223], v[200:203], v[88:91]\n\t"
    "ds_read_b64_tr_b16 v[238:239], v249 offset:41728\n\t"
    "v_mfma_f32_16x16x32_bf16 v[92:95], v[220:223], v[208:211], v[92:95]\n\t"
    "s_waitcnt lgkmcnt(6)\n\t"
    "v_mfma_f32_16x16x32_bf16 v[0:3], v[224:227], v[196:199], v[0:3]\n\t"
    "ds_read_b64_tr_b16 v[240:241], v251 offset:41984\n\t"
    "v_mfma_f32_16x16x32_bf16 v[4:7], v[224:227], v[204:207], v[4:7]\n\t"
    "ds_read_b64_tr_b16 v[242:243], v251 offset:42240\n\t"
    "v_mfma_f32_16x16x32_bf16 v[8:11], v[224:227], v[212:215], v[8:11]\n\t"
    "ds_read_b128 v[192:195], v252 offset:0\n\t"
    "s_waitcnt lgkmcnt(7)\n\t"
    "v_mfma_f32_16x16x32_bf16 v[12:15], v[228:231], v[196:199], v[12:15]\n\t"
    "ds_read_b64_tr_b16 v[216:217], v249 offset:41984\n\t"
    "v_mfma_f32_16x16x32_bf16 v[16:19], v[228:231], v[204:207], v[16:19]\n\t"
    "ds_read_b64_tr_b16 v[218:219], v249 offset:42240\n\t"
    "v_mfma_f32_16x16x32_bf16 v[20:23], v[228:231], v[212:215], v[20:23]\n\t"
    "ds_read_b128 v[200:203], v252 offset:2048\n\t"
    "s_waitcnt lgkmcnt(8)\n\t"
    "v_mfma_f32_16x16x32_bf16 v[24:27], v[232:235], v[196:199], v[24:27]\n\t"
    "ds_read_b64_tr_b16 v[220:221], v251 offset:42496\n\t"
    "v_mfma_f32_16x16x32_bf16 v[28:31], v[232:235], v[204:207], v[28:31]\n\t"
    "ds_read_b64_tr_b16 v[222:223], v251 offset:42752\n\t"
    "v_mfma_f32_16x16x32_bf16 v[32:35], v[232:235], v[212:215], v[32:35]\n\t"
    "ds_read_b128 v[208:211], v252 offset:4096\n\t"
    "s_waitcnt lgkmcnt(9)\n\t"
    "v_mfma_f32_16x16x32_bf16 v[36:39], v[236:239], v[196:199], v[36:39]\n\t"
    "ds_read_b64_tr_b16 v[224:225], v249 offset:42496\n\t"
    "v_mfma_f32_16x16x32_bf16 v[40:43], v[236:239], v[204:207], v[40:43]\n\t"
    "ds_read_b64_tr_b16 v[226:227], v249 offset:42752\n\t"
    "v_mfma_f32_16x16x32_bf16 v[44:47], v[236:239], v[212:215], v[44:47]\n\t"
    "s_waitcnt lgkmcnt(9)\n\t"
    "v_mfma_f32_16x16x32_bf16 v[48:51], v[240:243], v[196:199], v[48:51]\n\t"
    "v_mfma_f32_16x16x32_bf16 v[52:55], v[240:243], v[204:207], v[52:55]\n\t"
    "v_mfma_f32_16x16x32_bf16 v[56:59], v[240:243], v[212:215], v[56:59]\n\t"
    "s_waitcnt lgkmcnt(0)\n\t"
    "s_barrier\n\t"
    "v_mfma_f32_16x16x32_bf16 v[60:63], v[216:219], v[196:199], v[60:63]\n\t"
    "ds_read_b64_tr_b16 v[228:229], v251 offset:0\n\t"
    "v_mfma_f32_16x16x32_bf16 v[64:67], v[216:219], v[204:207], v[64:67]\n\t"
    "ds_read_b64_tr_b16 v[230:231], v251 offset:256\n\t"
    "v_mfma_f32_16x16x32_bf16 v[68:71], v[216:219], v[212:215], v[68:71]\n\t"
    "v_mfma_f32_16x16x32_bf16 v[72:75], v[220:223], v[196:199], v[72:75]\n\t"
    "ds_read_b64_tr_b16 v[232:233], v249 offset:0\n\t"
    "ds_read_b64_tr_b16 v[234:235], v249 offset:256\n\t"
    "v_mfma_f32_16x16x32_bf16 v[76:79], v[220:223], v[204:207], v[76:79]\n\t"
    "ds_read_b64_tr_b16 v[236:237], v251 offset:512\n\t"
    "v_mfma_f32_16x16x32_bf16 v[80:83], v[220:223], v[212:215], v[80:83]\n\t"
    "ds_read_b64_tr_b16 v[238:239], v251 offset:768\n\t"
    "v_mfma_f32_16x16x32_bf16 v[84:87], v[224:227], v[196:199], v[84:87]\n\t"
    "ds_read_b64_tr_b16 v[240:241], v249 offset:512\n\t"
    "v_mfma_f32_16x16x32_bf16 v[88:91], v[224:227], v[204:207], v[88:91]\n\t"
    "ds_read_b64_tr_b16 v[242:243], v249 offset:768\n\t"
    "v_mfma_f32_16x16x32_bf16 v[92:95], v[224:227], v[212:215], v[92:95]\n\t"
    "s_waitcnt lgkmcnt(6)\n\t"
    "v_mfma_f32_16x16x32_bf16 v[96:99], v[228:231], v[192:195], v[96:99]\n\t"
    "ds_read_b64_tr_b16 v[216:217], v251 offset:1024\n\t"
    "v_mfma_f32_16x16x32_bf16 v[100:103], v[228:231], v[200:203], v[100:103]\n\t"
    "ds_read_b64_tr_b16 v[218:219], v251 offset:1280\n\t"
    "v_mfma_f32_16x16x32_bf16 v[104:107], v[228:231], v[208:211], v[104:107]\n\t"
    "ds_read_b128 v[196:199], v250 offset:0\n\t"
    "s_waitcnt lgkmcnt(7)\n\t"
    "v_mfma_f32_16x16x32_bf16 v[108:111], v[232:235], v[192:195], v[108:111]\n\t"
    "ds_read_b64_tr_b16 v[220:221], v249 offset:1024\n\t"
    "v_mfma_f32_16x16x32_bf16 v[112:115], v[232:235], v[200:203], v[112:115]\n\t"
    "ds_read_b64_tr_b16 v[222:223], v249 offset:1280\n\t"
    "v_mfma_f32_16x16x32_bf16 v[116:119], v[232:235], v[208:211], v[116:119]\n\t"
    "ds_read_b128 v[204:207], v250 offset:2048\n\t"
    "s_waitcnt lgkmcnt(8)\n\t"
    "v_mfma_f32_16x16x32_bf16 v[120:123], v[236:239], v[192:195], v[120:123]\n\t"
    "ds_read_b64_tr_b16 v[224:225], v251 offset:1536\n\t"
    "v_mfma_f32_16x16x32_bf16 v[124:127], v[236:239], v[200:203], v[124:127]\n\t"
    "ds_read_b64_tr_b16 v[226:227], v251 offset:1792\n\t"
    "v_mfma_f32_16x16x32_bf16 v[128:131], v[236:239], v[208:211], v[128:131]\n\t"
    "ds_read_b128 v[212:215], v250 offset:4096\n\t"
    "s_waitcnt lgkmcnt(9)\n\t"
    "v_mfma_f32_16x16x32_bf16 v[132:135], v[240:243], v[192:195], v[132:135]\n\t"
    "ds_read_b64_tr_b16 v[228:229], v249 offset:1536\n\t"
    "v_mfma_f32_16x16x32_bf16 v[136:139], v[240:243], v[200:203], v[136:139]\n\t"
    "ds_read_b64_tr_b16 v[230:231], v249 offset:1792\n\t"
    "v_mfma_f32_16x16x32_bf16 v[140:143], v[240:243], v[208:211], v[140:143]\n\t"
    "s_waitcnt lgkmcnt(9)\n\t"
    "v_mfma_f32_16x16x32_bf16 v[144:147], v[216:219], v[192:195], v[144:147]\n\t"
    "ds_read_b64_tr_b16 v[232:233], v251 offset:8192\n\t"
    "v_mfma_f32_16x16x32_bf16 v[148:151], v[216:219], v[200:203], v[148:151]\n\t"
    "ds_read_b64_tr_b16 v[234:235], v251 offset:8448\n\t"
    "v_mfma_f32_16x16x32_bf16 v[152:155], v[216:219], v[208:211], v[152:155]\n\t"
    "s_waitcnt lgkmcnt(8)\n\t"
    "v_mfma_f32_16x16x32_bf16 v[156:159], v[220:223], v[192:195], v[156:159]\n\t"
    "ds_read_b64_tr_b16 v[236:237], v249 offset:8192\n\t"
    "v_mfma_f32_16x16x32_bf16 v[160:163], v[220:223], v[200:203], v[160:163]\n\t"
    "ds_read_b64_tr_b16 v[238:239], v249 offset:8448\n\t"
    "v_mfma_f32_16x16x32_bf16 v[164:167], v[220:223], v[208:211], v[164:167]\n\t"
    "s_waitcnt lgkmcnt(7)\n\t"
    "v_mfma_f32_16x16x32_bf16 v[168:171], v[224:227], v[192:195], v[168:171]\n\t"
    "ds_read_b64_tr_b16 v[240:241], v251 offset:8704\n\t"
    "v_mfma_f32_16x16x32_bf16 v[172:175], v[224:227], v[200:203], v[172:175]\n\t"
    "ds_read_b64_tr_b16 v[242:243], v251 offset:8960\n\t"
    "v_mfma_f32_16x16x32_bf16 v[176:179], v[224:227], v[208:211], v[176:179]\n\t"
    "s_waitcnt lgkmcnt(6)\n\t"
    "v_mfma_f32_16x16x32_bf16 v[180:183], v[228:231], v[192:195], v[180:183]\n\t"
    "ds_read_b64_tr_b16 v[216:217], v249 offset:8704\n\t"
    "v_mfma_f32_16x16x32_bf16 v[184:187], v[228:231], v[200:203], v[184:187]\n\t"
    "ds_read_b64_tr_b16 v[218:219], v249 offset:8960\n\t"
    "v_mfma_f32_16x16x32_bf16 v[188:191], v[228:231], v[208:211], v[188:191]\n\t"
    "s_waitcnt lgkmcnt(6)\n\t"
    "v_mfma_f32_16x16x32_bf16 v[96:99], v[232:235], v[196:199], v[96:99]\n\t"
    "ds_read_b64_tr_b16 v[220:221], v251 offset:9216\n\t"
    "v_mfma_f32_16x16x32_bf16 v[100:103], v[232:235], v[204:207], v[100:103]\n\t"
    "ds_read_b64_tr_b16 v[222:223], v251 offset:9472\n\t"
    "v_mfma_f32_16x16x32_bf16 v[104:107], v[232:235], v[212:215], v[104:107]\n\t"
    "s_waitcnt lgkmcnt(6)\n\t"
    "v_mfma_f32_16x16x32_bf16 v[108:111], v[236:239], v[196:199], v[108:111]\n\t"
    "ds_read_b64_tr_b16 v[224:225], v249 offset:9216\n\t"
    "v_mfma_f32_16x16x32_bf16 v[112:115], v[236:239], v[204:207], v[112:115]\n\t"
    "ds_read_b64_tr_b16 v[226:227], v249 offset:9472\n\t"
    "v_mfma_f32_16x16x32_bf16 v[116:119], v[236:239], v[212:215], v[116:119]\n\t"
    "s_waitcnt lgkmcnt(6)\n\t"
    "v_mfma_f32_16x16x32_bf16 v[120:123], v[240:243], v[196:199], v[120:123]\n\t"
    "ds_read_b64_tr_b16 v[228:229], v251 offset:9728\n\t"
    "v_mfma_f32_16x16x32_bf16 v[124:127], v[240:243], v[204:207], v[124:127]\n\t"
    "ds_read_b64_tr_b16 v[230:231], v251 offset:9984\n\t"
    "v_mfma_f32_16x16x32_bf16 v[128:131], v[240:243], v[212:215], v[128:131]\n\t"
    "s_waitcnt lgkmcnt(6)\n\t"
    "v_mfma_f32_16x16x32_bf16 v[132:135], v[216:219], v[196:199], v[132:135]\n\t"
    "ds_read_b64_tr_b16 v[232:233], v249 offset:9728\n\t"
    "v_mfma_f32_16x16x32_bf16 v[136:139], v[216:219], v[204:207], v[136:139]\n\t"
    "ds_read_b64_tr_b16 v[234:235], v249 offset:9984\n\t"
    "v_mfma_f32_16x16x32_bf16 v[140:143], v[216:219], v[212:215], v[140:143]\n\t"
    "s_waitcnt lgkmcnt(6)\n\t"
    "v_mfma_f32_16x16x32_bf16 v[144:147], v[220:223], v[196:199], v[144:147]\n\t"
    "v_mfma_f32_16x16x32_bf16 v[148:151], v[220:223], v[204:207], v[148:151]\n\t"
    "v_mfma_f32_16x16x32_bf16 v[152:155], v[220:223], v[212:215], v[152:155]\n\t"
    "s_waitcnt lgkmcnt(0)\n\t"
    "s_barrier\n\t"
    "v_mfma_f32_16x16x32_bf16 v[156:159], v[224:227], v[196:199], v[156:159]\n\t"
    "ds_read_b64_tr_b16 v[236:237], v251 offset:16384\n\t"
    "v_mfma_f32_16x16x32_bf16 v[160:163], v[224:227], v[204:207], v[160:163]\n\t"
    "ds_read_b64_tr_b16 v[238:239], v251 offset:16640\n\t"
    "v_mfma_f32_16x16x32_bf16 v[164:167], v[224:227], v[212:215], v[164:167]\n\t"
    "v_mfma_f32_16x16x32_bf16 v[168:171], v[228:231], v[196:199], v[168:171]\n\t"
    "ds_read_b64_tr_b16 v[240:241], v249 offset:16384\n\t"
    "ds_read_b64_tr_b16 v[242:243], v249 offset:16640\n\t"
    "v_mfma_f32_16x16x32_bf16 v[172:175], v[228:231], v[204:207], v[172:175]\n\t"
    "ds_read_b64_tr_b16 v[216:217], v251 offset:16896\n\t"
    "v_mfma_f32_16x16x32_bf16 v[176:179], v[228:231], v[212:215], v[176:179]\n\t"
    "ds_read_b64_tr_b16 v[218:219], v251 offset:17152\n\t"
    "v_mfma_f32_16x16x32_bf16 v[180:183], v[232:235], v[196:199], v[180:183]\n\t"
    "ds_read_b64_tr_b16 v[220:221], v249 offset:16896\n\t"
    "v_mfma_f32_16x16x32_bf16 v[184:187], v[232:235], v[204:207], v[184:187]\n\t"
    "ds_read_b64_tr_b16 v[222:223], v249 offset:17152\n\t"
    "v_mfma_f32_16x16x32_bf16 v[188:191], v[232:235], v[212:215], v[188:191]\n\t"
    "s_waitcnt lgkmcnt(6)\n\t"
    "v_mfma_f32_16x16x32_bf16 v[0:3], v[236:239], v[192:195], v[0:3]\n\t"
    "ds_read_b64_tr_b16 v[224:225], v251 offset:17408\n\t"
    "v_mfma_f32_16x16x32_bf16 v[4:7], v[236:239], v[200:203], v[4:7]\n\t"
    "ds_read_b64_tr_b16 v[226:227], v251 offset:17664\n\t"
    "v_mfma_f32_16x16x32_bf16 v[8:11], v[236:239], v[208:211], v[8:11]\n\t"
    "s_waitcnt lgkmcnt(6)\n\t"
    "v_mfma_f32_16x16x32_bf16 v[12:15], v[240:243], v[192:195], v[12:15]\n\t"
    "ds_read_b64_tr_b16 v[228:229], v249 offset:17408\n\t"
    "v_mfma_f32_16x16x32_bf16 v[16:19], v[240:243], v[200:203], v[16:19]\n\t"
    "ds_read_b64_tr_b16 v[230:231], v249 offset:17664\n\t"
    "v_mfma_f32_16x16x32_bf16 v[20:23], v[240:243], v[208:211], v[20:23]\n\t"
    "s_waitcnt lgkmcnt(6)\n\t"
    "v_mfma_f32_16x16x32_bf16 v[24:27], v[216:219], v[192:195], v[24:27]\n\t"
    "ds_read_b64_tr_b16 v[232:233], v251 offset:17920\n\t"
    "v_mfma_f32_16x16x32_bf16 v[28:31], v[216:219], v[200:203], v[28:31]\n\t"
    "ds_read_b64_tr_b16 v[234:235], v251 offset:18176\n\t"
    "v_mfma_f32_16x16x32_bf16 v[32:35], v[216:219], v[208:211], v[32:35]\n\t"
    "s_waitcnt lgkmcnt(6)\n\t"
    "v_mfma_f32_16x16x32_bf16 v[36:39], v[220:223], v[192:195], v[36:39]\n\t"
    "ds_read_b64_tr_b16 v[236:237], v249 offset:17920\n\t"
    "v_mfma_f32_16x16x32_bf16 v[40:43], v[220:223], v[200:203], v[40:43]\n\t"
    "ds_read_b64_tr_b16 v[238:239], v249 offset:18176\n\t"
    "v_mfma_f32_16x16x32_bf16 v[44:47], v[220:223], v[208:211], v[44:47]\n\t"
    "s_waitcnt lgkmcnt(6)\n\t"
    "v_mfma_f32_16x16x32_bf16 v[48:51], v[224:227], v[192:195], v[48:51]\n\t"
    "ds_read_b64_tr_b16 v[240:241], v251 offset:24576\n\t"
    "v_mfma_f32_16x16x32_bf16 v[52:55], v[224:227], v[200:203], v[52:55]\n\t"
    "ds_read_b64_tr_b16 v[242:243], v251 offset:24832\n\t"
    "v_mfma_f32_16x16x32_bf16 v[56:59], v[224:227], v[208:211], v[56:59]\n\t"
    "s_waitcnt lgkmcnt(6)\n\t"
    "v_mfma_f32_16x16x32_bf16 v[60:63], v[228:231], v[192:195], v[60:63]\n\t"
    "ds_read_b64_tr_b16 v[216:217], v249 offset:24576\n\t"
    "v_mfma_f32_16x16x32_bf16 v[64:67], v[228:231], v[200:203], v[64:67]\n\t"
    "ds_read_b64_tr_b16 v[218:219], v249 offset:24832\n\t"
    "v_mfma_f32_16x16x32_bf16 v[68:71], v[228:231], v[208:211], v[68:71]\n\t"
    "s_waitcnt lgkmcnt(6)\n\t"
    "v_mfma_f32_16x16x32_bf16 v[72:75], v[232:235], v[192:195], v[72:75]\n\t"
    "ds_read_b64_tr_b16 v[220:221], v251 offset:25088\n\t"
    "v_mfma_f32_16x16x32_bf16 v[76:79], v[232:235], v[200:203], v[76:79]\n\t"
    "ds_read_b64_tr_b16 v[222:223], v251 offset:25344\n\t"
    "v_mfma_f32_16x16x32_bf16 v[80:83], v[232:235], v[208:211], v[80:83]\n\t"
    "s_waitcnt lgkmcnt(6)\n\t"
    "v_mfma_f32_16x16x32_bf16 v[84:87], v[236:239], v[192:195], v[84:87]\n\t"
    "ds_read_b64_tr_b16 v[224:225], v249 offset:25088\n\t"
    "v_mfma_f32_16x16x32_bf16 v[88:91], v[236:239], v[200:203], v[88:91]\n\t"
    "ds_read_b64_tr_b16 v[226:227], v249 offset:25344\n\t"
    "v_mfma_f32_16x16x32_bf16 v[92:95], v[236:239], v[208:211], v[92:95]\n\t"
    "s_waitcnt lgkmcnt(6)\n\t"
    "v_mfma_f32_16x16x32_bf16 v[0:3], v[240:243], v[196:199], v[0:3]\n\t"
    "ds_read_b64_tr_b16 v[228:229], v251 offset:25600\n\t"
    "v_mfma_f32_16x16x32_bf16 v[4:7], v[240:243], v[204:207], v[4:7]\n\t"
    "ds_read_b64_tr_b16 v[230:231], v251 offset:25856\n\t"
    "v_mfma_f32_16x16x32_bf16 v[8:11], v[240:243], v[212:215], v[8:11]\n\t"
    "ds_read_b128 v[192:195], v252 offset:24576\n\t"
    "s_waitcnt lgkmcnt(7)\n\t"
    "v_mfma_f32_16x16x32_bf16 v[12:15], v[216:219], v[196:199], v[12:15]\n\t"
    "ds_read_b64_tr_b16 v[232:233], v249 offset:25600\n\t"
    "v_mfma_f32_16x16x32_bf16 v[16:19], v[216:219], v[204:207], v[16:19]\n\t"
    "ds_read_b64_tr_b16 v[234:235], v249 offset:25856\n\t"
    "v_mfma_f32_16x16x32_bf16 v[20:23], v[216:219], v[212:215], v[20:23]\n\t"
    "ds_read_b128 v[200:203], v252 offset:26624\n\t"
    "s_waitcnt lgkmcnt(8)\n\t"
    "v_mfma_f32_16x16x32_bf16 v[24:27], v[220:223], v[196:199], v[24:27]\n\t"
    "ds_read_b64_tr_b16 v[236:237], v251 offset:26112\n\t"
    "v_mfma_f32_16x16x32_bf16 v[28:31], v[220:223], v[204:207], v[28:31]\n\t"
    "ds_read_b64_tr_b16 v[238:239], v251 offset:26368\n\t"
    "v_mfma_f32_16x16x32_bf16 v[32:35], v[220:223], v[212:215], v[32:35]\n\t"
    "ds_read_b128 v[208:211], v252 offset:28672\n\t"
    "s_waitcnt lgkmcnt(9)\n\t"
    "v_mfma_f32_16x16x32_bf16 v[36:39], v[224:227], v[196:199], v[36:39]\n\t"
    "ds_read_b64_tr_b16 v[240:241], v249 offset:26112\n\t"
    "v_mfma_f32_16x16x32_bf16 v[40:43], v[224:227], v[204:207], v[40:43]\n\t"
    "ds_read_b64_tr_b16 v[242:243], v249 offset:26368\n\t"
    "v_mfma_f32_16x16x32_bf16 v[44:47], v[224:227], v[212:215], v[44:47]\n\t"
    "s_waitcnt lgkmcnt(9)\n\t"
    "v_mfma_f32_16x16x32_bf16 v[48:51], v[228:231], v[196:199], v[48:51]\n\t"
    "v_mfma_f32_16x16x32_bf16 v[52:55], v[228:231], v[204:207], v[52:55]\n\t"
    "v_mfma_f32_16x16x32_bf16 v[56:59], v[228:231], v[212:215], v[56:59]\n\t"
    "s_waitcnt lgkmcnt(0)\n\t"
    "s_barrier\n\t"
    "v_mfma_f32_16x16x32_bf16 v[60:63], v[232:235], v[196:199], v[60:63]\n\t"
    "ds_read_b64_tr_b16 v[216:217], v251 offset:32768\n\t"
    "v_mfma_f32_16x16x32_bf16 v[64:67], v[232:235], v[204:207], v[64:67]\n\t"
    "ds_read_b64_tr_b16 v[218:219], v251 offset:33024\n\t"
    "v_mfma_f32_16x16x32_bf16 v[68:71], v[232:235], v[212:215], v[68:71]\n\t"
    "v_mfma_f32_16x16x32_bf16 v[72:75], v[236:239], v[196:199], v[72:75]\n\t"
    "ds_read_b64_tr_b16 v[220:221], v249 offset:32768\n\t"
    "ds_read_b64_tr_b16 v[222:223], v249 offset:33024\n\t"
    "v_mfma_f32_16x16x32_bf16 v[76:79], v[236:239], v[204:207], v[76:79]\n\t"
    "ds_read_b64_tr_b16 v[224:225], v251 offset:33280\n\t"
    "v_mfma_f32_16x16x32_bf16 v[80:83], v[236:239], v[212:215], v[80:83]\n\t"
    "ds_read_b64_tr_b16 v[226:227], v251 offset:33536\n\t"
    "v_mfma_f32_16x16x32_bf16 v[84:87], v[240:243], v[196:199], v[84:87]\n\t"
    "ds_read_b64_tr_b16 v[228:229], v249 offset:33280\n\t"
    "v_mfma_f32_16x16x32_bf16 v[88:91], v[240:243], v[204:207], v[88:91]\n\t"
    "ds_read_b64_tr_b16 v[230:231], v249 offset:33536\n\t"
    "v_mfma_f32_16x16x32_bf16 v[92:95], v[240:243], v[212:215], v[92:95]\n\t"
    "s_waitcnt lgkmcnt(6)\n\t"
    "v_mfma_f32_16x16x32_bf16 v[96:99], v[216:219], v[192:195], v[96:99]\n\t"
    "ds_read_b64_tr_b16 v[232:233], v251 offset:33792\n\t"
    "v_mfma_f32_16x16x32_bf16 v[100:103], v[216:219], v[200:203], v[100:103]\n\t"
    "ds_read_b64_tr_b16 v[234:235], v251 offset:34048\n\t"
    "v_mfma_f32_16x16x32_bf16 v[104:107], v[216:219], v[208:211], v[104:107]\n\t"
    "ds_read_b128 v[196:199], v250 offset:24576\n\t"
    "s_waitcnt lgkmcnt(7)\n\t"
    "v_mfma_f32_16x16x32_bf16 v[108:111], v[220:223], v[192:195], v[108:111]\n\t"
    "ds_read_b64_tr_b16 v[236:237], v249 offset:33792\n\t"
    "v_mfma_f32_16x16x32_bf16 v[112:115], v[220:223], v[200:203], v[112:115]\n\t"
    "ds_read_b64_tr_b16 v[238:239], v249 offset:34048\n\t"
    "v_mfma_f32_16x16x32_bf16 v[116:119], v[220:223], v[208:211], v[116:119]\n\t"
    "ds_read_b128 v[204:207], v250 offset:26624\n\t"
    "s_waitcnt lgkmcnt(8)\n\t"
    "v_mfma_f32_16x16x32_bf16 v[120:123], v[224:227], v[192:195], v[120:123]\n\t"
    "ds_read_b64_tr_b16 v[240:241], v251 offset:34304\n\t"
    "v_mfma_f32_16x16x32_bf16 v[124:127], v[224:227], v[200:203], v[124:127]\n\t"
    "ds_read_b64_tr_b16 v[242:243], v251 offset:34560\n\t"
    "v_mfma_f32_16x16x32_bf16 v[128:131], v[224:227], v[208:211], v[128:131]\n\t"
    "ds_read_b128 v[212:215], v250 offset:28672\n\t"
    "s_waitcnt lgkmcnt(9)\n\t"
    "v_mfma_f32_16x16x32_bf16 v[132:135], v[228:231], v[192:195], v[132:135]\n\t"
    "ds_read_b64_tr_b16 v[216:217], v249 offset:34304\n\t"
    "v_mfma_f32_16x16x32_bf16 v[136:139], v[228:231], v[200:203], v[136:139]\n\t"
    "ds_read_b64_tr_b16 v[218:219], v249 offset:34560\n\t"
    "v_mfma_f32_16x16x32_bf16 v[140:143], v[228:231], v[208:211], v[140:143]\n\t"
    "s_waitcnt lgkmcnt(9)\n\t"
    "v_mfma_f32_16x16x32_bf16 v[144:147], v[232:235], v[192:195], v[144:147]\n\t"
    "ds_read_b64_tr_b16 v[220:221], v251 offset:40960\n\t"
    "v_mfma_f32_16x16x32_bf16 v[148:151], v[232:235], v[200:203], v[148:151]\n\t"
    "ds_read_b64_tr_b16 v[222:223], v251 offset:41216\n\t"
    "v_mfma_f32_16x16x32_bf16 v[152:155], v[232:235], v[208:211], v[152:155]\n\t"
    "s_waitcnt lgkmcnt(8)\n\t"
    "v_mfma_f32_16x16x32_bf16 v[156:159], v[236:239], v[192:195], v[156:159]\n\t"
    "ds_read_b64_tr_b16 v[224:225], v249 offset:40960\n\t"
    "v_mfma_f32_16x16x32_bf16 v[160:163], v[236:239], v[200:203], v[160:163]\n\t"
    "ds_read_b64_tr_b16 v[226:227], v249 offset:41216\n\t"
    "v_mfma_f32_16x16x32_bf16 v[164:167], v[236:239], v[208:211], v[164:167]\n\t"
    "s_waitcnt lgkmcnt(7)\n\t"
    "v_mfma_f32_16x16x32_bf16 v[168:171], v[240:243], v[192:195], v[168:171]\n\t"
    "ds_read_b64_tr_b16 v[228:229], v251 offset:41472\n\t"
    "v_mfma_f32_16x16x32_bf16 v[172:175], v[240:243], v[200:203], v[172:175]\n\t"
    "ds_read_b64_tr_b16 v[230:231], v251 offset:41728\n\t"
    "v_mfma_f32_16x16x32_bf16 v[176:179], v[240:243], v[208:211], v[176:179]\n\t"
    "s_waitcnt lgkmcnt(6)\n\t"
    "v_mfma_f32_16x16x32_bf16 v[180:183], v[216:219], v[192:195], v[180:183]\n\t"
    "ds_read_b64_tr_b16 v[232:233], v249 offset:41472\n\t"
    "v_mfma_f32_16x16x32_bf16 v[184:187], v[216:219], v[200:203], v[184:187]\n\t"
    "ds_read_b64_tr_b16 v[234:235], v249 offset:41728\n\t"
    "v_mfma_f32_16x16x32_bf16 v[188:191], v[216:219], v[208:211], v[188:191]\n\t"
    "s_waitcnt lgkmcnt(6)\n\t"
    "v_mfma_f32_16x16x32_bf16 v[96:99], v[220:223], v[196:199], v[96:99]\n\t"
    "ds_read_b64_tr_b16 v[236:237], v251 offset:41984\n\t"
    "v_mfma_f32_16x16x32_bf16 v[100:103], v[220:223], v[204:207], v[100:103]\n\t"
    "ds_read_b64_tr_b16 v[238:239], v251 offset:42240\n\t"
    "v_mfma_f32_16x16x32_bf16 v[104:107], v[220:223], v[212:215], v[104:107]\n\t"
    "s_waitcnt lgkmcnt(6)\n\t"
    "v_mfma_f32_16x16x32_bf16 v[108:111], v[224:227], v[196:199], v[108:111]\n\t"
    "ds_read_b64_tr_b16 v[240:241], v249 offset:41984\n\t"
    "v_mfma_f32_16x16x32_bf16 v[112:115], v[224:227], v[204:207], v[112:115]\n\t"
    "ds_read_b64_tr_b16 v[242:243], v249 offset:42240\n\t"
    "v_mfma_f32_16x16x32_bf16 v[116:119], v[224:227], v[212:215], v[116:119]\n\t"
    "s_waitcnt lgkmcnt(6)\n\t"
    "v_mfma_f32_16x16x32_bf16 v[120:123], v[228:231], v[196:199], v[120:123]\n\t"
    "ds_read_b64_tr_b16 v[216:217], v251 offset:42496\n\t"
    "v_mfma_f32_16x16x32_bf16 v[124:127], v[228:231], v[204:207], v[124:127]\n\t"
    "ds_read_b64_tr_b16 v[218:219], v251 offset:42752\n\t"
    "v_mfma_f32_16x16x32_bf16 v[128:131], v[228:231], v[212:215], v[128:131]\n\t"
    "s_waitcnt lgkmcnt(6)\n\t"
    "v_mfma_f32_16x16x32_bf16 v[132:135], v[232:235], v[196:199], v[132:135]\n\t"
    "ds_read_b64_tr_b16 v[220:221], v249 offset:42496\n\t"
    "v_mfma_f32_16x16x32_bf16 v[136:139], v[232:235], v[204:207], v[136:139]\n\t"
    "ds_read_b64_tr_b16 v[222:223], v249 offset:42752\n\t"
    "v_mfma_f32_16x16x32_bf16 v[140:143], v[232:235], v[212:215], v[140:143]\n\t"
    "s_waitcnt lgkmcnt(6)\n\t"
    "v_mfma_f32_16x16x32_bf16 v[144:147], v[236:239], v[196:199], v[144:147]\n\t"
    "v_mfma_f32_16x16x32_bf16 v[148:151], v[236:239], v[204:207], v[148:151]\n\t"
    "v_mfma_f32_16x16x32_bf16 v[152:155], v[236:239], v[212:215], v[152:155]\n\t"
    "s_waitcnt lgkmcnt(0)\n\t"
    "s_barrier\n\t"
    "v_mfma_f32_16x16x32_bf16 v[156:159], v[240:243], v[196:199], v[156:159]\n\t"
    "ds_read_b64_tr_b16 v[224:225], v251 offset:0\n\t"
    "v_mfma_f32_16x16x32_bf16 v[160:163], v[240:243], v[204:207], v[160:163]\n\t"
    "ds_read_b64_tr_b16 v[226:227], v251 offset:256\n\t"
    "v_mfma_f32_16x16x32_bf16 v[164:167], v[240:243], v[212:215], v[164:167]\n\t"
    "v_mfma_f32_16x16x32_bf16 v[168:171], v[216:219], v[196:199], v[168:171]\n\t"
    "ds_read_b64_tr_b16 v[228:229], v249 offset:0\n\t"
    "ds_read_b64_tr_b16 v[230:231], v249 offset:256\n\t"
    "v_mfma_f32_16x16x32_bf16 v[172:175], v[216:219], v[204:207], v[172:175]\n\t"
    "ds_read_b64_tr_b16 v[232:233], v251 offset:512\n\t"
    "v_mfma_f32_16x16x32_bf16 v[176:179], v[216:219], v[212:215], v[176:179]\n\t"
    "ds_read_b64_tr_b16 v[234:235], v251 offset:768\n\t"
    "v_mfma_f32_16x16x32_bf16 v[180:183], v[220:223], v[196:199], v[180:183]\n\t"
    "ds_read_b64_tr_b16 v[236:237], v249 offset:512\n\t"
    "v_mfma_f32_16x16x32_bf16 v[184:187], v[220:223], v[204:207], v[184:187]\n\t"
    "ds_read_b64_tr_b16 v[238:239], v249 offset:768\n\t"
    "v_mfma_f32_16x16x32_bf16 v[188:191], v[220:223], v[212:215], v[188:191]\n\t"
    "s_waitcnt lgkmcnt(6)\n\t"
    "v_mfma_f32_16x16x32_bf16 v[0:3], v[224:227], v[192:195], v[0:3]\n\t"
    "ds_read_b64_tr_b16 v[240:241], v251 offset:1024\n\t"
    "v_mfma_f32_16x16x32_bf16 v[4:7], v[224:227], v[200:203], v[4:7]\n\t"
    "ds_read_b64_tr_b16 v[242:243], v251 offset:1280\n\t"
    "v_mfma_f32_16x16x32_bf16 v[8:11], v[224:227], v[208:211], v[8:11]\n\t"
    "s_waitcnt lgkmcnt(6)\n\t"
    "v_mfma_f32_16x16x32_bf16 v[12:15], v[228:231], v[192:195], v[12:15]\n\t"
    "ds_read_b64_tr_b16 v[216:217], v249 offset:1024\n\t"
    "v_mfma_f32_16x16x32_bf16 v[16:19], v[228:231], v[200:203], v[16:19]\n\t"
    "ds_read_b64_tr_b16 v[218:219], v249 offset:1280\n\t"
    "v_mfma_f32_16x16x32_bf16 v[20:23], v[228:231], v[208:211], v[20:23]\n\t"
    "s_waitcnt lgkmcnt(6)\n\t"
    "v_mfma_f32_16x16x32_bf16 v[24:27], v[232:235], v[192:195], v[24:27]\n\t"
    "ds_read_b64_tr_b16 v[220:221], v251 offset:1536\n\t"
    "v_mfma_f32_16x16x32_bf16 v[28:31], v[232:235], v[200:203], v[28:31]\n\t"
    "ds_read_b64_tr_b16 v[222:223], v251 offset:1792\n\t"
    "v_mfma_f32_16x16x32_bf16 v[32:35], v[232:235], v[208:211], v[32:35]\n\t"
    "s_waitcnt lgkmcnt(6)\n\t"
    "v_mfma_f32_16x16x32_bf16 v[36:39], v[236:239], v[192:195], v[36:39]\n\t"
    "ds_read_b64_tr_b16 v[224:225], v249 offset:1536\n\t"
    "v_mfma_f32_16x16x32_bf16 v[40:43], v[236:239], v[200:203], v[40:43]\n\t"
    "ds_read_b64_tr_b16 v[226:227], v249 offset:1792\n\t"
    "v_mfma_f32_16x16x32_bf16 v[44:47], v[236:239], v[208:211], v[44:47]\n\t"
    "s_waitcnt lgkmcnt(6)\n\t"
    "v_mfma_f32_16x16x32_bf16 v[48:51], v[240:243], v[192:195], v[48:51]\n\t"
    "ds_read_b64_tr_b16 v[228:229], v251 offset:8192\n\t"
    "v_mfma_f32_16x16x32_bf16 v[52:55], v[240:243], v[200:203], v[52:55]\n\t"
    "ds_read_b64_tr_b16 v[230:231], v251 offset:8448\n\t"
    "v_mfma_f32_16x16x32_bf16 v[56:59], v[240:243], v[208:211], v[56:59]\n\t"
    "s_waitcnt lgkmcnt(6)\n\t"
    "v_mfma_f32_16x16x32_bf16 v[60:63], v[216:219], v[192:195], v[60:63]\n\t"
    "ds_read_b64_tr_b16 v[232:233], v249 offset:8192\n\t"
    "v_mfma_f32_16x16x32_bf16 v[64:67], v[216:219], v[200:203], v[64:67]\n\t"
    "ds_read_b64_tr_b16 v[234:235], v249 offset:8448\n\t"
    "v_mfma_f32_16x16x32_bf16 v[68:71], v[216:219], v[208:211], v[68:71]\n\t"
    "s_waitcnt lgkmcnt(6)\n\t"
    "v_mfma_f32_16x16x32_bf16 v[72:75], v[220:223], v[192:195], v[72:75]\n\t"
    "ds_read_b64_tr_b16 v[236:237], v251 offset:8704\n\t"
    "v_mfma_f32_16x16x32_bf16 v[76:79], v[220:223], v[200:203], v[76:79]\n\t"
    "ds_read_b64_tr_b16 v[238:239], v251 offset:8960\n\t"
    "v_mfma_f32_16x16x32_bf16 v[80:83], v[220:223], v[208:211], v[80:83]\n\t"
    "s_waitcnt lgkmcnt(6)\n\t"
    "v_mfma_f32_16x16x32_bf16 v[84:87], v[224:227], v[192:195], v[84:87]\n\t"
    "ds_read_b64_tr_b16 v[240:241], v249 offset:8704\n\t"
    "v_mfma_f32_16x16x32_bf16 v[88:91], v[224:227], v[200:203], v[88:91]\n\t"
    "ds_read_b64_tr_b16 v[242:243], v249 offset:8960\n\t"
    "v_mfma_f32_16x16x32_bf16 v[92:95], v[224:227], v[208:211], v[92:95]\n\t"
    "s_waitcnt lgkmcnt(6)\n\t"
    "v_mfma_f32_16x16x32_bf16 v[0:3], v[228:231], v[196:199], v[0:3]\n\t"
    "ds_read_b64_tr_b16 v[216:217], v251 offset:9216\n\t"
    "v_mfma_f32_16x16x32_bf16 v[4:7], v[228:231], v[204:207], v[4:7]\n\t"
    "ds_read_b64_tr_b16 v[218:219], v251 offset:9472\n\t"
    "v_mfma_f32_16x16x32_bf16 v[8:11], v[228:231], v[212:215], v[8:11]\n\t"
    "ds_read_b128 v[192:195], v252 offset:0\n\t"
    "s_waitcnt lgkmcnt(7)\n\t"
    "v_mfma_f32_16x16x32_bf16 v[12:15], v[232:235], v[196:199], v[12:15]\n\t"
    "ds_read_b64_tr_b16 v[220:221], v249 offset:9216\n\t"
    "v_mfma_f32_16x16x32_bf16 v[16:19], v[232:235], v[204:207], v[16:19]\n\t"
    "ds_read_b64_tr_b16 v[222:223], v249 offset:9472\n\t"
    "v_mfma_f32_16x16x32_bf16 v[20:23], v[232:235], v[212:215], v[20:23]\n\t"
    "ds_read_b128 v[200:203], v252 offset:2048\n\t"
    "s_waitcnt lgkmcnt(8)\n\t"
    "v_mfma_f32_16x16x32_bf16 v[24:27], v[236:239], v[196:199], v[24:27]\n\t"
    "ds_read_b64_tr_b16 v[224:225], v251 offset:9728\n\t"
    "v_mfma_f32_16x16x32_bf16 v[28:31], v[236:239], v[204:207], v[28:31]\n\t"
    "ds_read_b64_tr_b16 v[226:227], v251 offset:9984\n\t"
    "v_mfma_f32_16x16x32_bf16 v[32:35], v[236:239], v[212:215], v[32:35]\n\t"
    "ds_read_b128 v[208:211], v252 offset:4096\n\t"
    "s_waitcnt lgkmcnt(9)\n\t"
    "v_mfma_f32_16x16x32_bf16 v[36:39], v[240:243], v[196:199], v[36:39]\n\t"
    "ds_read_b64_tr_b16 v[228:229], v249 offset:9728\n\t"
    "v_mfma_f32_16x16x32_bf16 v[40:43], v[240:243], v[204:207], v[40:43]\n\t"
    "ds_read_b64_tr_b16 v[230:231], v249 offset:9984\n\t"
    "v_mfma_f32_16x16x32_bf16 v[44:47], v[240:243], v[212:215], v[44:47]\n\t"
    "s_waitcnt lgkmcnt(9)\n\t"
    "v_mfma_f32_16x16x32_bf16 v[48:51], v[216:219], v[196:199], v[48:51]\n\t"
    "v_mfma_f32_16x16x32_bf16 v[52:55], v[216:219], v[204:207], v[52:55]\n\t"
    "v_mfma_f32_16x16x32_bf16 v[56:59], v[216:219], v[212:215], v[56:59]\n\t"
    "s_waitcnt lgkmcnt(0)\n\t"
    "s_barrier\n\t"
    "v_mfma_f32_16x16x32_bf16 v[60:63], v[220:223], v[196:199], v[60:63]\n\t"
    "ds_read_b64_tr_b16 v[232:233], v251 offset:16384\n\t"
    "v_mfma_f32_16x16x32_bf16 v[64:67], v[220:223], v[204:207], v[64:67]\n\t"
    "ds_read_b64_tr_b16 v[234:235], v251 offset:16640\n\t"
    "v_mfma_f32_16x16x32_bf16 v[68:71], v[220:223], v[212:215], v[68:71]\n\t"
    "v_mfma_f32_16x16x32_bf16 v[72:75], v[224:227], v[196:199], v[72:75]\n\t"
    "ds_read_b64_tr_b16 v[236:237], v249 offset:16384\n\t"
    "ds_read_b64_tr_b16 v[238:239], v249 offset:16640\n\t"
    "v_mfma_f32_16x16x32_bf16 v[76:79], v[224:227], v[204:207], v[76:79]\n\t"
    "ds_read_b64_tr_b16 v[240:241], v251 offset:16896\n\t"
    "v_mfma_f32_16x16x32_bf16 v[80:83], v[224:227], v[212:215], v[80:83]\n\t"
    "ds_read_b64_tr_b16 v[242:243], v251 offset:17152\n\t"
    "v_mfma_f32_16x16x32_bf16 v[84:87], v[228:231], v[196:199], v[84:87]\n\t"
    "ds_read_b64_tr_b16 v[216:217], v249 offset:16896\n\t"
    "v_mfma_f32_16x16x32_bf16 v[88:91], v[228:231], v[204:207], v[88:91]\n\t"
    "ds_read_b64_tr_b16 v[218:219], v249 offset:17152\n\t"
    "v_mfma_f32_16x16x32_bf16 v[92:95], v[228:231], v[212:215], v[92:95]\n\t"
    "s_waitcnt lgkmcnt(6)\n\t"
    "v_mfma_f32_16x16x32_bf16 v[96:99], v[232:235], v[192:195], v[96:99]\n\t"
    "ds_read_b64_tr_b16 v[220:221], v251 offset:17408\n\t"
    "v_mfma_f32_16x16x32_bf16 v[100:103], v[232:235], v[200:203], v[100:103]\n\t"
    "ds_read_b64_tr_b16 v[222:223], v251 offset:17664\n\t"
    "v_mfma_f32_16x16x32_bf16 v[104:107], v[232:235], v[208:211], v[104:107]\n\t"
    "ds_read_b128 v[196:199], v250 offset:0\n\t"
    "s_waitcnt lgkmcnt(7)\n\t"
    "v_mfma_f32_16x16x32_bf16 v[108:111], v[236:239], v[192:195], v[108:111]\n\t"
    "ds_read_b64_tr_b16 v[224:225], v249 offset:17408\n\t"
    "v_mfma_f32_16x16x32_bf16 v[112:115], v[236:239], v[200:203], v[112:115]\n\t"
    "ds_read_b64_tr_b16 v[226:227], v249 offset:17664\n\t"
    "v_mfma_f32_16x16x32_bf16 v[116:119], v[236:239], v[208:211], v[116:119]\n\t"
    "ds_read_b128 v[204:207], v250 offset:2048\n\t"
    "s_waitcnt lgkmcnt(8)\n\t"
    "v_mfma_f32_16x16x32_bf16 v[120:123], v[240:243], v[192:195], v[120:123]\n\t"
    "ds_read_b64_tr_b16 v[228:229], v251 offset:17920\n\t"
    "v_mfma_f32_16x16x32_bf16 v[124:127], v[240:243], v[200:203], v[124:127]\n\t"
    "ds_read_b64_tr_b16 v[230:231], v251 offset:18176\n\t"
    "v_mfma_f32_16x16x32_bf16 v[128:131], v[240:243], v[208:211], v[128:131]\n\t"
    "ds_read_b128 v[212:215], v250 offset:4096\n\t"
    "s_waitcnt lgkmcnt(9)\n\t"
    "v_mfma_f32_16x16x32_bf16 v[132:135], v[216:219], v[192:195], v[132:135]\n\t"
    "ds_read_b64_tr_b16 v[232:233], v249 offset:17920\n\t"
    "v_mfma_f32_16x16x32_bf16 v[136:139], v[216:219], v[200:203], v[136:139]\n\t"
    "ds_read_b64_tr_b16 v[234:235], v249 offset:18176\n\t"
    "v_mfma_f32_16x16x32_bf16 v[140:143], v[216:219], v[208:211], v[140:143]\n\t"
    "s_waitcnt lgkmcnt(9)\n\t"
    "v_mfma_f32_16x16x32_bf16 v[144:147], v[220:223], v[192:195], v[144:147]\n\t"
    "ds_read_b64_tr_b16 v[236:237], v251 offset:24576\n\t"
    "v_mfma_f32_16x16x32_bf16 v[148:151], v[220:223], v[200:203], v[148:151]\n\t"
    "ds_read_b64_tr_b16 v[238:239], v251 offset:24832\n\t"
    "v_mfma_f32_16x16x32_bf16 v[152:155], v[220:223], v[208:211], v[152:155]\n\t"
    "s_waitcnt lgkmcnt(8)\n\t"
    "v_mfma_f32_16x16x32_bf16 v[156:159], v[224:227], v[192:195], v[156:159]\n\t"
    "ds_read_b64_tr_b16 v[240:241], v249 offset:24576\n\t"
    "v_mfma_f32_16x16x32_bf16 v[160:163], v[224:227], v[200:203], v[160:163]\n\t"
    "ds_read_b64_tr_b16 v[242:243], v249 offset:24832\n\t"
    "v_mfma_f32_16x16x32_bf16 v[164:167], v[224:227], v[208:211], v[164:167]\n\t"
    "s_waitcnt lgkmcnt(7)\n\t"
    "v_mfma_f32_16x16x32_bf16 v[168:171], v[228:231], v[192:195], v[168:171]\n\t"
    "ds_read_b64_tr_b16 v[216:217], v251 offset:25088\n\t"
    "v_mfma_f32_16x16x32_bf16 v[172:175], v[228:231], v[200:203], v[172:175]\n\t"
    "ds_read_b64_tr_b16 v[218:219], v251 offset:25344\n\t"
    "v_mfma_f32_16x16x32_bf16 v[176:179], v[228:231], v[208:211], v[176:179]\n\t"
    "s_waitcnt lgkmcnt(6)\n\t"
    "v_mfma_f32_16x16x32_bf16 v[180:183], v[232:235], v[192:195], v[180:183]\n\t"
    "ds_read_b64_tr_b16 v[220:221], v249 offset:25088\n\t"
    "v_mfma_f32_16x16x32_bf16 v[184:187], v[232:235], v[200:203], v[184:187]\n\t"
    "ds_read_b64_tr_b16 v[222:223], v249 offset:25344\n\t"
    "v_mfma_f32_16x16x32_bf16 v[188:191], v[232:235], v[208:211], v[188:191]\n\t"
    "s_waitcnt lgkmcnt(6)\n\t"
    "v_mfma_f32_16x16x32_bf16 v[96:99], v[236:239], v[196:199], v[96:99]\n\t"
    "ds_read_b64_tr_b16 v[224:225], v251 offset:25600\n\t"
    "v_mfma_f32_16x16x32_bf16 v[100:103], v[236:239], v[204:207], v[100:103]\n\t"
    "ds_read_b64_tr_b16 v[226:227], v251 offset:25856\n\t"
    "v_mfma_f32_16x16x32_bf16 v[104:107], v[236:239], v[212:215], v[104:107]\n\t"
    "s_waitcnt lgkmcnt(6)\n\t"
    "v_mfma_f32_16x16x32_bf16 v[108:111], v[240:243], v[196:199], v[108:111]\n\t"
    "ds_read_b64_tr_b16 v[228:229], v249 offset:25600\n\t"
    "v_mfma_f32_16x16x32_bf16 v[112:115], v[240:243], v[204:207], v[112:115]\n\t"
    "ds_read_b64_tr_b16 v[230:231], v249 offset:25856\n\t"
    "v_mfma_f32_16x16x32_bf16 v[116:119], v[240:243], v[212:215], v[116:119]\n\t"
    "s_waitcnt lgkmcnt(6)\n\t"
    "v_mfma_f32_16x16x32_bf16 v[120:123], v[216:219], v[196:199], v[120:123]\n\t"
    "ds_read_b64_tr_b16 v[232:233], v251 offset:26112\n\t"
    "v_mfma_f32_16x16x32_bf16 v[124:127], v[216:219], v[204:207], v[124:127]\n\t"
    "ds_read_b64_tr_b16 v[234:235], v251 offset:26368\n\t"
    "v_mfma_f32_16x16x32_bf16 v[128:131], v[216:219], v[212:215], v[128:131]\n\t"
    "s_waitcnt lgkmcnt(6)\n\t"
    "v_mfma_f32_16x16x32_bf16 v[132:135], v[220:223], v[196:199], v[132:135]\n\t"
    "ds_read_b64_tr_b16 v[236:237], v249 offset:26112\n\t"
    "v_mfma_f32_16x16x32_bf16 v[136:139], v[220:223], v[204:207], v[136:139]\n\t"
    "ds_read_b64_tr_b16 v[238:239], v249 offset:26368\n\t"
    "v_mfma_f32_16x16x32_bf16 v[140:143], v[220:223], v[212:215], v[140:143]\n\t"
    "s_waitcnt lgkmcnt(6)\n\t"
    "v_mfma_f32_16x16x32_bf16 v[144:147], v[224:227], v[196:199], v[144:147]\n\t"
    "v_mfma_f32_16x16x32_bf16 v[148:151], v[224:227], v[204:207], v[148:151]\n\t"
    "v_mfma_f32_16x16x32_bf16 v[152:155], v[224:227], v[212:215], v[152:155]\n\t"
    "s_waitcnt lgkmcnt(0)\n\t"
    "s_barrier\n\t"
    "v_mfma_f32_16x16x32_bf16 v[156:159], v[228:231], v[196:199], v[156:159]\n\t"
    "ds_read_b64_tr_b16 v[240:241], v251 offset:32768\n\t"
    "v_mfma_f32_16x16x32_bf16 v[160:163], v[228:231], v[204:207], v[160:163]\n\t"
    "ds_read_b64_tr_b16 v[242:243], v251 offset:33024\n\t"
    "v_mfma_f32_16x16x32_bf16 v[164:167], v[228:231], v[212:215], v[164:167]\n\t"
    "v_mfma_f32_16x16x32_bf16 v[168:171], v[232:235], v[196:199], v[168:171]\n\t"
    "ds_read_b64_tr_b16 v[216:217], v249 offset:32768\n\t"
    "ds_read_b64_tr_b16 v[218:219], v249 offset:33024\n\t"
    "v_mfma_f32_16x16x32_bf16 v[172:175], v[232:235], v[204:207], v[172:175]\n\t"
    "ds_read_b64_tr_b16 v[220:221], v251 offset:33280\n\t"
    "v_mfma_f32_16x16x32_bf16 v[176:179], v[232:235], v[212:215], v[176:179]\n\t"
    "ds_read_b64_tr_b16 v[222:223], v251 offset:33536\n\t"
    "v_mfma_f32_16x16x32_bf16 v[180:183], v[236:239], v[196:199], v[180:183]\n\t"
    "ds_read_b64_tr_b16 v[224:225], v249 offset:33280\n\t"
    "v_mfma_f32_16x16x32_bf16 v[184:187], v[236:239], v[204:207], v[184:187]\n\t"
    "ds_read_b64_tr_b16 v[226:227], v249 offset:33536\n\t"
    "v_mfma_f32_16x16x32_bf16 v[188:191], v[236:239], v[212:215], v[188:191]\n\t"
    "s_waitcnt lgkmcnt(6)\n\t"
    "v_mfma_f32_16x16x32_bf16 v[0:3], v[240:243], v[192:195], v[0:3]\n\t"
    "ds_read_b64_tr_b16 v[228:229], v251 offset:33792\n\t"
    "v_mfma_f32_16x16x32_bf16 v[4:7], v[240:243], v[200:203], v[4:7]\n\t"
    "ds_read_b64_tr_b16 v[230:231], v251 offset:34048\n\t"
    "v_mfma_f32_16x16x32_bf16 v[8:11], v[240:243], v[208:211], v[8:11]\n\t"
    "s_waitcnt lgkmcnt(6)\n\t"
    "v_mfma_f32_16x16x32_bf16 v[12:15], v[216:219], v[192:195], v[12:15]\n\t"
    "ds_read_b64_tr_b16 v[232:233], v249 offset:33792\n\t"
    "v_mfma_f32_16x16x32_bf16 v[16:19], v[216:219], v[200:203], v[16:19]\n\t"
    "ds_read_b64_tr_b16 v[234:235], v249 offset:34048\n\t"
    "v_mfma_f32_16x16x32_bf16 v[20:23], v[216:219], v[208:211], v[20:23]\n\t"
    "s_waitcnt lgkmcnt(6)\n\t"
    "v_mfma_f32_16x16x32_bf16 v[24:27], v[220:223], v[192:195], v[24:27]\n\t"
    "ds_read_b64_tr_b16 v[236:237], v251 offset:34304\n\t"
    "v_mfma_f32_16x16x32_bf16 v[28:31], v[220:223], v[200:203], v[28:31]\n\t"
    "ds_read_b64_tr_b16 v[238:239], v251 offset:34560\n\t"
    "v_mfma_f32_16x16x32_bf16 v[32:35], v[220:223], v[208:211], v[32:35]\n\t"
    "s_waitcnt lgkmcnt(6)\n\t"
    "v_mfma_f32_16x16x32_bf16 v[36:39], v[224:227], v[192:195], v[36:39]\n\t"
    "ds_read_b64_tr_b16 v[240:241], v249 offset:34304\n\t"
    "v_mfma_f32_16x16x32_bf16 v[40:43], v[224:227], v[200:203], v[40:43]\n\t"
    "ds_read_b64_tr_b16 v[242:243], v249 offset:34560\n\t"
    "v_mfma_f32_16x16x32_bf16 v[44:47], v[224:227], v[208:211], v[44:47]\n\t"
    "s_waitcnt lgkmcnt(6)\n\t"
    "v_mfma_f32_16x16x32_bf16 v[48:51], v[228:231], v[192:195], v[48:51]\n\t"
    "ds_read_b64_tr_b16 v[216:217], v251 offset:40960\n\t"
    "v_mfma_f32_16x16x32_bf16 v[52:55], v[228:231], v[200:203], v[52:55]\n\t"
    "ds_read_b64_tr_b16 v[218:219], v251 offset:41216\n\t"
    "v_mfma_f32_16x16x32_bf16 v[56:59], v[228:231], v[208:211], v[56:59]\n\t"
    "s_waitcnt lgkmcnt(6)\n\t"
    "v_mfma_f32_16x16x32_bf16 v[60:63], v[232:235], v[192:195], v[60:63]\n\t"
    "ds_read_b64_tr_b16 v[220:221], v249 offset:40960\n\t"
    "v_mfma_f32_16x16x32_bf16 v[64:67], v[232:235], v[200:203], v[64:67]\n\t"
    "ds_read_b64_tr_b16 v[222:223], v249 offset:41216\n\t"
    "v_mfma_f32_16x16x32_bf16 v[68:71], v[232:235], v[208:211], v[68:71]\n\t"
    "s_waitcnt lgkmcnt(6)\n\t"
    "v_mfma_f32_16x16x32_bf16 v[72:75], v[236:239], v[192:195], v[72:75]\n\t"
    "ds_read_b64_tr_b16 v[224:225], v251 offset:41472\n\t"
    "v_mfma_f32_16x16x32_bf16 v[76:79], v[236:239], v[200:203], v[76:79]\n\t"
    "ds_read_b64_tr_b16 v[226:227], v251 offset:41728\n\t"
    "v_mfma_f32_16x16x32_bf16 v[80:83], v[236:239], v[208:211], v[80:83]\n\t"
    "s_waitcnt lgkmcnt(6)\n\t"
    "v_mfma_f32_16x16x32_bf16 v[84:87], v[240:243], v[192:195], v[84:87]\n\t"
    "ds_read_b64_tr_b16 v[228:229], v249 offset:41472\n\t"
    "v_mfma_f32_16x16x32_bf16 v[88:91], v[240:243], v[200:203], v[88:91]\n\t"
    "ds_read_b64_tr_b16 v[230:231], v249 offset:41728\n\t"
    "v_mfma_f32_16x16x32_bf16 v[92:95], v[240:243], v[208:211], v[92:95]\n\t"
    "s_waitcnt lgkmcnt(6)\n\t"
    "v_mfma_f32_16x16x32_bf16 v[0:3], v[216:219], v[196:199], v[0:3]\n\t"
    "ds_read_b64_tr_b16 v[232:233], v251 offset:41984\n\t"
    "v_mfma_f32_16x16x32_bf16 v[4:7], v[216:219], v[204:207], v[4:7]\n\t"
    "ds_read_b64_tr_b16 v[234:235], v251 offset:42240\n\t"
    "v_mfma_f32_16x16x32_bf16 v[8:11], v[216:219], v[212:215], v[8:11]\n\t"
    "ds_read_b128 v[192:195], v252 offset:24576\n\t"
    "s_waitcnt lgkmcnt(7)\n\t"
    "v_mfma_f32_16x16x32_bf16 v[12:15], v[220:223], v[196:199], v[12:15]\n\t"
    "ds_read_b64_tr_b16 v[236:237], v249 offset:41984\n\t"
    "v_mfma_f32_16x16x32_bf16 v[16:19], v[220:223], v[204:207], v[16:19]\n\t"
    "ds_read_b64_tr_b16 v[238:239], v249 offset:42240\n\t"
    "v_mfma_f32_16x16x32_bf16 v[20:23], v[220:223], v[212:215], v[20:23]\n\t"
    "ds_read_b128 v[200:203], v252 offset:26624\n\t"
    "s_waitcnt lgkmcnt(8)\n\t"
    "v_mfma_f32_16x16x32_bf16 v[24:27], v[224:227], v[196:199], v[24:27]\n\t"
    "ds_read_b64_tr_b16 v[240:241], v251 offset:42496\n\t"
    "v_mfma_f32_16x16x32_bf16 v[28:31], v[224:227], v[204:207], v[28:31]\n\t"
    "ds_read_b64_tr_b16 v[242:243], v251 offset:42752\n\t"
    "v_mfma_f32_16x16x32_bf16 v[32:35], v[224:227], v[212:215], v[32:35]\n\t"
    "ds_read_b128 v[208:211], v252 offset:28672\n\t"
    "s_waitcnt lgkmcnt(9)\n\t"
    "v_mfma_f32_16x16x32_bf16 v[36:39], v[228:231], v[196:199], v[36:39]\n\t"
    "ds_read_b64_tr_b16 v[216:217], v249 offset:42496\n\t"
    "v_mfma_f32_16x16x32_bf16 v[40:43], v[228:231], v[204:207], v[40:43]\n\t"
    "ds_read_b64_tr_b16 v[218:219], v249 offset:42752\n\t"
    "v_mfma_f32_16x16x32_bf16 v[44:47], v[228:231], v[212:215], v[44:47]\n\t"
    "s_waitcnt lgkmcnt(9)\n\t"
    "v_mfma_f32_16x16x32_bf16 v[48:51], v[232:235], v[196:199], v[48:51]\n\t"
    "v_mfma_f32_16x16x32_bf16 v[52:55], v[232:235], v[204:207], v[52:55]\n\t"
    "v_mfma_f32_16x16x32_bf16 v[56:59], v[232:235], v[212:215], v[56:59]\n\t"
    "s_waitcnt lgkmcnt(0)\n\t"
    "s_barrier\n\t"
    "v_mfma_f32_16x16x32_bf16 v[60:63], v[236:239], v[196:199], v[60:63]\n\t"
    "ds_read_b64_tr_b16 v[220:221], v251 offset:0\n\t"
    "v_mfma_f32_16x16x32_bf16 v[64:67], v[236:239], v[204:207], v[64:67]\n\t"
    "ds_read_b64_tr_b16 v[222:223], v251 offset:256\n\t"
    "v_mfma_f32_16x16x32_bf16 v[68:71], v[236:239], v[212:215], v[68:71]\n\t"
    "v_mfma_f32_16x16x32_bf16 v[72:75], v[240:243], v[196:199], v[72:75]\n\t"
    "ds_read_b64_tr_b16 v[224:225], v249 offset:0\n\t"
    "ds_read_b64_tr_b16 v[226:227], v249 offset:256\n\t"
    "v_mfma_f32_16x16x32_bf16 v[76:79], v[240:243], v[204:207], v[76:79]\n\t"
    "ds_read_b64_tr_b16 v[228:229], v251 offset:512\n\t"
    "v_mfma_f32_16x16x32_bf16 v[80:83], v[240:243], v[212:215], v[80:83]\n\t"
    "ds_read_b64_tr_b16 v[230:231], v251 offset:768\n\t"
    "v_mfma_f32_16x16x32_bf16 v[84:87], v[216:219], v[196:199], v[84:87]\n\t"
    "ds_read_b64_tr_b16 v[232:233], v249 offset:512\n\t"
    "v_mfma_f32_16x16x32_bf16 v[88:91], v[216:219], v[204:207], v[88:91]\n\t"
    "ds_read_b64_tr_b16 v[234:235], v249 offset:768\n\t"
    "v_mfma_f32_16x16x32_bf16 v[92:95], v[216:219], v[212:215], v[92:95]\n\t"
    "s_waitcnt lgkmcnt(6)\n\t"
    "v_mfma_f32_16x16x32_bf16 v[96:99], v[220:223], v[192:195], v[96:99]\n\t"
    "ds_read_b64_tr_b16 v[236:237], v251 offset:1024\n\t"
    "v_mfma_f32_16x16x32_bf16 v[100:103], v[220:223], v[200:203], v[100:103]\n\t"
    "ds_read_b64_tr_b16 v[238:239], v251 offset:1280\n\t"
    "v_mfma_f32_16x16x32_bf16 v[104:107], v[220:223], v[208:211], v[104:107]\n\t"
    "ds_read_b128 v[196:199], v250 offset:24576\n\t"
    "s_waitcnt lgkmcnt(7)\n\t"
    "v_mfma_f32_16x16x32_bf16 v[108:111], v[224:227], v[192:195], v[108:111]\n\t"
    "ds_read_b64_tr_b16 v[240:241], v249 offset:1024\n\t"
    "v_mfma_f32_16x16x32_bf16 v[112:115], v[224:227], v[200:203], v[112:115]\n\t"
    "ds_read_b64_tr_b16 v[242:243], v249 offset:1280\n\t"
    "v_mfma_f32_16x16x32_bf16 v[116:119], v[224:227], v[208:211], v[116:119]\n\t"
    "ds_read_b128 v[204:207], v250 offset:26624\n\t"
    "s_waitcnt lgkmcnt(8)\n\t"
    "v_mfma_f32_16x16x32_bf16 v[120:123], v[228:231], v[192:195], v[120:123]\n\t"
    "ds_read_b64_tr_b16 v[216:217], v251 offset:1536\n\t"
    "v_mfma_f32_16x16x32_bf16 v[124:127], v[228:231], v[200:203], v[124:127]\n\t"
    "ds_read_b64_tr_b16 v[218:219], v251 offset:1792\n\t"
    "v_mfma_f32_16x16x32_bf16 v[128:131], v[228:231], v[208:211], v[128:131]\n\t"
    "ds_read_b128 v[212:215], v250 offset:28672\n\t"
    "s_waitcnt lgkmcnt(9)\n\t"
    "v_mfma_f32_16x16x32_bf16 v[132:135], v[232:235], v[192:195], v[132:135]\n\t"
    "ds_read_b64_tr_b16 v[220:221], v249 offset:1536\n\t"
    "v_mfma_f32_16x16x32_bf16 v[136:139], v[232:235], v[200:203], v[136:139]\n\t"
    "ds_read_b64_tr_b16 v[222:223], v249 offset:1792\n\t"
    "v_mfma_f32_16x16x32_bf16 v[140:143], v[232:235], v[208:211], v[140:143]\n\t"
    "s_waitcnt lgkmcnt(9)\n\t"
    "v_mfma_f32_16x16x32_bf16 v[144:147], v[236:239], v[192:195], v[144:147]\n\t"
    "ds_read_b64_tr_b16 v[224:225], v251 offset:8192\n\t"
    "v_mfma_f32_16x16x32_bf16 v[148:151], v[236:239], v[200:203], v[148:151]\n\t"
    "ds_read_b64_tr_b16 v[226:227], v251 offset:8448\n\t"
    "v_mfma_f32_16x16x32_bf16 v[152:155], v[236:239], v[208:211], v[152:155]\n\t"
    "s_waitcnt lgkmcnt(8)\n\t"
    "v_mfma_f32_16x16x32_bf16 v[156:159], v[240:243], v[192:195], v[156:159]\n\t"
    "ds_read_b64_tr_b16 v[228:229], v249 offset:8192\n\t"
    "v_mfma_f32_16x16x32_bf16 v[160:163], v[240:243], v[200:203], v[160:163]\n\t"
    "ds_read_b64_tr_b16 v[230:231], v249 offset:8448\n\t"
    "v_mfma_f32_16x16x32_bf16 v[164:167], v[240:243], v[208:211], v[164:167]\n\t"
    "s_waitcnt lgkmcnt(7)\n\t"
    "v_mfma_f32_16x16x32_bf16 v[168:171], v[216:219], v[192:195], v[168:171]\n\t"
    "ds_read_b64_tr_b16 v[232:233], v251 offset:8704\n\t"
    "v_mfma_f32_16x16x32_bf16 v[172:175], v[216:219], v[200:203], v[172:175]\n\t"
    "ds_read_b64_tr_b16 v[234:235], v251 offset:8960\n\t"
    "v_mfma_f32_16x16x32_bf16 v[176:179], v[216:219], v[208:211], v[176:179]\n\t"
    "s_waitcnt lgkmcnt(6)\n\t"
    "v_mfma_f32_16x16x32_bf16 v[180:183], v[220:223], v[192:195], v[180:183]\n\t"
    "ds_read_b64_tr_b16 v[236:237], v249 offset:8704\n\t"
    "v_mfma_f32_16x16x32_bf16 v[184:187], v[220:223], v[200:203], v[184:187]\n\t"
    "ds_read_b64_tr_b16 v[238:239], v249 offset:8960\n\t"
    "v_mfma_f32_16x16x32_bf16 v[188:191], v[220:223], v[208:211], v[188:191]\n\t"
    "s_waitcnt lgkmcnt(6)\n\t"
    "v_mfma_f32_16x16x32_bf16 v[96:99], v[224:227], v[196:199], v[96:99]\n\t"
    "ds_read_b64_tr_b16 v[240:241], v251 offset:9216\n\t"
    "v_mfma_f32_16x16x32_bf16 v[100:103], v[224:227], v[204:207], v[100:103]\n\t"
    "ds_read_b64_tr_b16 v[242:243], v251 offset:9472\n\t"
    "v_mfma_f32_16x16x32_bf16 v[104:107], v[224:227], v[212:215], v[104:107]\n\t"
    "s_waitcnt lgkmcnt(6)\n\t"
    "v_mfma_f32_16x16x32_bf16 v[108:111], v[228:231], v[196:199], v[108:111]\n\t"
    "ds_read_b64_tr_b16 v[216:217], v249 offset:9216\n\t"
    "v_mfma_f32_16x16x32_bf16 v[112:115], v[228:231], v[204:207], v[112:115]\n\t"
    "ds_read_b64_tr_b16 v[218:219], v249 offset:9472\n\t"
    "v_mfma_f32_16x16x32_bf16 v[116:119], v[228:231], v[212:215], v[116:119]\n\t"
    "s_waitcnt lgkmcnt(6)\n\t"
    "v_mfma_f32_16x16x32_bf16 v[120:123], v[232:235], v[196:199], v[120:123]\n\t"
    "ds_read_b64_tr_b16 v[220:221], v251 offset:9728\n\t"
    "v_mfma_f32_16x16x32_bf16 v[124:127], v[232:235], v[204:207], v[124:127]\n\t"
    "ds_read_b64_tr_b16 v[222:223], v251 offset:9984\n\t"
    "v_mfma_f32_16x16x32_bf16 v[128:131], v[232:235], v[212:215], v[128:131]\n\t"
    "s_waitcnt lgkmcnt(6)\n\t"
    "v_mfma_f32_16x16x32_bf16 v[132:135], v[236:239], v[196:199], v[132:135]\n\t"
    "ds_read_b64_tr_b16 v[224:225], v249 offset:9728\n\t"
    "v_mfma_f32_16x16x32_bf16 v[136:139], v[236:239], v[204:207], v[136:139]\n\t"
    "ds_read_b64_tr_b16 v[226:227], v249 offset:9984\n\t"
    "v_mfma_f32_16x16x32_bf16 v[140:143], v[236:239], v[212:215], v[140:143]\n\t"
    "s_waitcnt lgkmcnt(6)\n\t"
    "v_mfma_f32_16x16x32_bf16 v[144:147], v[240:243], v[196:199], v[144:147]\n\t"
    "v_mfma_f32_16x16x32_bf16 v[148:151], v[240:243], v[204:207], v[148:151]\n\t"
    "v_mfma_f32_16x16x32_bf16 v[152:155], v[240:243], v[212:215], v[152:155]\n\t"
    "s_waitcnt lgkmcnt(0)\n\t"
    "s_barrier\n\t"
    "v_mfma_f32_16x16x32_bf16 v[156:159], v[216:219], v[196:199], v[156:159]\n\t"
    "ds_read_b64_tr_b16 v[228:229], v251 offset:16384\n\t"
    "v_mfma_f32_16x16x32_bf16 v[160:163], v[216:219], v[204:207], v[160:163]\n\t"
    "ds_read_b64_tr_b16 v[230:231], v251 offset:16640\n\t"
    "v_mfma_f32_16x16x32_bf16 v[164:167], v[216:219], v[212:215], v[164:167]\n\t"
    "v_mfma_f32_16x16x32_bf16 v[168:171], v[220:223], v[196:199], v[168:171]\n\t"
    "ds_read_b64_tr_b16 v[232:233], v249 offset:16384\n\t"
    "ds_read_b64_tr_b16 v[234:235], v249 offset:16640\n\t"
    "v_mfma_f32_16x16x32_bf16 v[172:175], v[220:223], v[204:207], v[172:175]\n\t"
    "ds_read_b64_tr_b16 v[236:237], v251 offset:16896\n\t"
    "v_mfma_f32_16x16x32_bf16 v[176:179], v[220:223], v[212:215], v[176:179]\n\t"
    "ds_read_b64_tr_b16 v[238:239], v251 offset:17152\n\t"
    "v_mfma_f32_16x16x32_bf16 v[180:183], v[224:227], v[196:199], v[180:183]\n\t"
    "ds_read_b64_tr_b16 v[240:241], v249 offset:16896\n\t"
    "v_mfma_f32_16x16x32_bf16 v[184:187], v[224:227], v[204:207], v[184:187]\n\t"
    "ds_read_b64_tr_b16 v[242:243], v249 offset:17152\n\t"
    "v_mfma_f32_16x16x32_bf16 v[188:191], v[224:227], v[212:215], v[188:191]\n\t"
    "s_waitcnt lgkmcnt(6)\n\t"
    "v_mfma_f32_16x16x32_bf16 v[0:3], v[228:231], v[192:195], v[0:3]\n\t"
    "ds_read_b64_tr_b16 v[216:217], v251 offset:17408\n\t"
    "v_mfma_f32_16x16x32_bf16 v[4:7], v[228:231], v[200:203], v[4:7]\n\t"
    "ds_read_b64_tr_b16 v[218:219], v251 offset:17664\n\t"
    "v_mfma_f32_16x16x32_bf16 v[8:11], v[228:231], v[208:211], v[8:11]\n\t"
    "s_waitcnt lgkmcnt(6)\n\t"
    "v_mfma_f32_16x16x32_bf16 v[12:15], v[232:235], v[192:195], v[12:15]\n\t"
    "ds_read_b64_tr_b16 v[220:221], v249 offset:17408\n\t"
    "ds_read_b64_tr_b16 v[222:223], v249 offset:17664\n\t"
    "v_pk_mul_f32 v[96:97], v[96:97], s[80:81]\n\t"
    "v_pk_mul_f32 v[98:99], v[98:99], s[80:81]\n\t"
    "v_pk_mul_f32 v[108:109], v[108:109], s[80:81]\n\t"
    "v_mfma_f32_16x16x32_bf16 v[16:19], v[232:235], v[200:203], v[16:19]\n\t"
    "v_pk_mul_f32 v[110:111], v[110:111], s[80:81]\n\t"
    "v_cvt_pk_bf16_f32 v96, v96, v97\n\t"
    "v_cvt_pk_bf16_f32 v97, v98, v99\n\t"
    "v_cvt_pk_bf16_f32 v98, v108, v109\n\t"
    "v_cvt_pk_bf16_f32 v99, v110, v111\n\t"
    "v_mfma_f32_16x16x32_bf16 v[20:23], v[232:235], v[208:211], v[20:23]\n\t"
    "v_cndmask_b32 v108, v96, v98, s[82:83]\n\t"
    "v_cndmask_b32 v110, v98, v96, s[82:83]\n\t"
    "v_cndmask_b32 v109, v97, v99, s[82:83]\n\t"
    "v_cndmask_b32 v111, v99, v97, s[82:83]\n\t"
    "s_waitcnt lgkmcnt(6)\n\t"
    "v_mfma_f32_16x16x32_bf16 v[24:27], v[236:239], v[192:195], v[24:27]\n\t"
    "ds_read_b64_tr_b16 v[224:225], v251 offset:17920\n\t"
    "ds_read_b64_tr_b16 v[226:227], v251 offset:18176\n\t"
    "ds_write_b128 v254, v[108:111] offset:0\n\t"
    "v_pk_mul_f32 v[100:101], v[100:101], s[80:81]\n\t"
    "v_pk_mul_f32 v[102:103], v[102:103], s[80:81]\n\t"
    "v_mfma_f32_16x16x32_bf16 v[28:31], v[236:239], v[200:203], v[28:31]\n\t"
    "v_pk_mul_f32 v[112:113], v[112:113], s[80:81]\n\t"
    "v_pk_mul_f32 v[114:115], v[114:115], s[80:81]\n\t"
    "v_cvt_pk_bf16_f32 v100, v100, v101\n\t"
    "v_cvt_pk_bf16_f32 v101, v102, v103\n\t"
    "v_cvt_pk_bf16_f32 v102, v112, v113\n\t"
    "v_mfma_f32_16x16x32_bf16 v[32:35], v[236:239], v[208:211], v[32:35]\n\t"
    "v_cvt_pk_bf16_f32 v103, v114, v115\n\t"
    "v_cndmask_b32 v112, v100, v102, s[82:83]\n\t"
    "v_cndmask_b32 v114, v102, v100, s[82:83]\n\t"
    "v_cndmask_b32 v113, v101, v103, s[82:83]\n\t"
    "s_waitcnt lgkmcnt(7)\n\t"
    "v_mfma_f32_16x16x32_bf16 v[36:39], v[240:243], v[192:195], v[36:39]\n\t"
    "ds_read_b64_tr_b16 v[228:229], v249 offset:17920\n\t"
    "ds_read_b64_tr_b16 v[230:231], v249 offset:18176\n\t"
    "v_cndmask_b32 v115, v103, v101, s[82:83]\n\t"
    "ds_write_b128 v254, v[112:115] offset:4096\n\t"
    "v_pk_mul_f32 v[104:105], v[104:105], s[80:81]\n\t"
    "v_mfma_f32_16x16x32_bf16 v[40:43], v[240:243], v[200:203], v[40:43]\n\t"
    "v_pk_mul_f32 v[106:107], v[106:107], s[80:81]\n\t"
    "v_pk_mul_f32 v[116:117], v[116:117], s[80:81]\n\t"
    "v_pk_mul_f32 v[118:119], v[118:119], s[80:81]\n\t"
    "v_cvt_pk_bf16_f32 v104, v104, v105\n\t"
    "v_cvt_pk_bf16_f32 v105, v106, v107\n\t"
    "v_mfma_f32_16x16x32_bf16 v[44:47], v[240:243], v[208:211], v[44:47]\n\t"
    "v_cvt_pk_bf16_f32 v106, v116, v117\n\t"
    "v_cvt_pk_bf16_f32 v107, v118, v119\n\t"
    "v_cndmask_b32 v116, v104, v106, s[82:83]\n\t"
    "v_cndmask_b32 v118, v106, v104, s[82:83]\n\t"
    "s_waitcnt lgkmcnt(8)\n\t"
    "v_mfma_f32_16x16x32_bf16 v[48:51], v[216:219], v[192:195], v[48:51]\n\t"
    "ds_read_b64_tr_b16 v[232:233], v251 offset:24576\n\t"
    "ds_read_b64_tr_b16 v[234:235], v251 offset:24832\n\t"
    "v_cndmask_b32 v117, v105, v107, s[82:83]\n\t"
    "v_cndmask_b32 v119, v107, v105, s[82:83]\n\t"
    "ds_write_b128 v254, v[116:119] offset:26624\n\t"
    "v_mfma_f32_16x16x32_bf16 v[52:55], v[216:219], v[200:203], v[52:55]\n\t"
    "v_pk_mul_f32 v[120:121], v[120:121], s[80:81]\n\t"
    "v_pk_mul_f32 v[122:123], v[122:123], s[80:81]\n\t"
    "v_pk_mul_f32 v[132:133], v[132:133], s[80:81]\n\t"
    "v_pk_mul_f32 v[134:135], v[134:135], s[80:81]\n\t"
    "v_cvt_pk_bf16_f32 v120, v120, v121\n\t"
    "v_mfma_f32_16x16x32_bf16 v[56:59], v[216:219], v[208:211], v[56:59]\n\t"
    "v_cvt_pk_bf16_f32 v121, v122, v123\n\t"
    "v_cvt_pk_bf16_f32 v122, v132, v133\n\t"
    "v_cvt_pk_bf16_f32 v123, v134, v135\n\t"
    "v_cndmask_b32 v132, v120, v122, s[82:83]\n\t"
    "s_waitcnt lgkmcnt(9)\n\t"
    "v_mfma_f32_16x16x32_bf16 v[60:63], v[220:223], v[192:195], v[60:63]\n\t"
    "ds_read_b64_tr_b16 v[236:237], v249 offset:24576\n\t"
    "ds_read_b64_tr_b16 v[238:239], v249 offset:24832\n\t"
    "v_cndmask_b32 v134, v122, v120, s[82:83]\n\t"
    "v_cndmask_b32 v133, v121, v123, s[82:83]\n\t"
    "v_cndmask_b32 v135, v123, v121, s[82:83]\n\t"
    "v_mfma_f32_16x16x32_bf16 v[64:67], v[220:223], v[200:203], v[64:67]\n\t"
    "ds_write_b128 v254, v[132:135] offset:1024\n\t"
    "v_pk_mul_f32 v[124:125], v[124:125], s[80:81]\n\t"
    "v_pk_mul_f32 v[126:127], v[126:127], s[80:81]\n\t"
    "v_pk_mul_f32 v[136:137], v[136:137], s[80:81]\n\t"
    "v_pk_mul_f32 v[138:139], v[138:139], s[80:81]\n\t"
    "v_mfma_f32_16x16x32_bf16 v[68:71], v[220:223], v[208:211], v[68:71]\n\t"
    "v_cvt_pk_bf16_f32 v124, v124, v125\n\t"
    "v_cvt_pk_bf16_f32 v125, v126, v127\n\t"
    "v_cvt_pk_bf16_f32 v126, v136, v137\n\t"
    "v_cvt_pk_bf16_f32 v127, v138, v139\n\t"
    "s_waitcnt lgkmcnt(10)\n\t"
    "v_mfma_f32_16x16x32_bf16 v[72:75], v[224:227], v[192:195], v[72:75]\n\t"
    "ds_read_b64_tr_b16 v[240:241], v251 offset:25088\n\t"
    "ds_read_b64_tr_b16 v[242:243], v251 offset:25344\n\t"
    "v_cndmask_b32 v136, v124, v126, s[82:83]\n\t"
    "v_cndmask_b32 v138, v126, v124, s[82:83]\n\t"
    "v_cndmask_b32 v137, v125, v127, s[82:83]\n\t"
    "v_mfma_f32_16x16x32_bf16 v[76:79], v[224:227], v[200:203], v[76:79]\n\t"
    "v_cndmask_b32 v139, v127, v125, s[82:83]\n\t"
    "ds_write_b128 v254, v[136:139] offset:5120\n\t"
    "v_pk_mul_f32 v[128:129], v[128:129], s[80:81]\n\t"
    "v_pk_mul_f32 v[130:131], v[130:131], s[80:81]\n\t"
    "v_pk_mul_f32 v[140:141], v[140:141], s[80:81]\n\t"
    "v_mfma_f32_16x16x32_bf16 v[80:83], v[224:227], v[208:211], v[80:83]\n\t"
    "v_pk_mul_f32 v[142:143], v[142:143], s[80:81]\n\t"
    "v_cvt_pk_bf16_f32 v128, v128, v129\n\t"
    "v_cvt_pk_bf16_f32 v129, v130, v131\n\t"
    "v_cvt_pk_bf16_f32 v130, v140, v141\n\t"
    "s_waitcnt lgkmcnt(10)\n\t"
    "v_mfma_f32_16x16x32_bf16 v[84:87], v[228:231], v[192:195], v[84:87]\n\t"
    "ds_read_b64_tr_b16 v[216:217], v249 offset:25088\n\t"
    "ds_read_b64_tr_b16 v[218:219], v249 offset:25344\n\t"
    "v_cvt_pk_bf16_f32 v131, v142, v143\n\t"
    "v_cndmask_b32 v140, v128, v130, s[82:83]\n\t"
    "v_cndmask_b32 v142, v130, v128, s[82:83]\n\t"
    "v_mfma_f32_16x16x32_bf16 v[88:91], v[228:231], v[200:203], v[88:91]\n\t"
    "v_cndmask_b32 v141, v129, v131, s[82:83]\n\t"
    "v_cndmask_b32 v143, v131, v129, s[82:83]\n\t"
    "ds_write_b128 v254, v[140:143] offset:27648\n\t"
    "v_pk_mul_f32 v[144:145], v[144:145], s[80:81]\n\t"
    "v_pk_mul_f32 v[146:147], v[146:147], s[80:81]\n\t"
    "v_mfma_f32_16x16x32_bf16 v[92:95], v[228:231], v[208:211], v[92:95]\n\t"
    "v_pk_mul_f32 v[156:157], v[156:157], s[80:81]\n\t"
    "v_pk_mul_f32 v[158:159], v[158:159], s[80:81]\n\t"
    "v_cvt_pk_bf16_f32 v144, v144, v145\n\t"
    "v_cvt_pk_bf16_f32 v145, v146, v147\n\t"
    "s_waitcnt lgkmcnt(10)\n\t"
    "v_mfma_f32_16x16x32_bf16 v[0:3], v[232:235], v[196:199], v[0:3]\n\t"
    "ds_read_b64_tr_b16 v[220:221], v251 offset:25600\n\t"
    "ds_read_b64_tr_b16 v[222:223], v251 offset:25856\n\t"
    "v_cvt_pk_bf16_f32 v146, v156, v157\n\t"
    "v_cvt_pk_bf16_f32 v147, v158, v159\n\t"
    "v_cndmask_b32 v156, v144, v146, s[82:83]\n\t"
    "v_mfma_f32_16x16x32_bf16 v[4:7], v[232:235], v[204:207], v[4:7]\n\t"
    "v_cndmask_b32 v158, v146, v144, s[82:83]\n\t"
    "v_cndmask_b32 v157, v145, v147, s[82:83]\n\t"
    "v_cndmask_b32 v159, v147, v145, s[82:83]\n\t"
    "ds_write_b128 v254, v[156:159] offset:2048\n\t"
    "v_pk_mul_f32 v[148:149], v[148:149], s[80:81]\n\t"
    "v_mfma_f32_16x16x32_bf16 v[8:11], v[232:235], v[212:215], v[8:11]\n\t"
    "v_pk_mul_f32 v[150:151], v[150:151], s[80:81]\n\t"
    "v_pk_mul_f32 v[160:161], v[160:161], s[80:81]\n\t"
    "v_pk_mul_f32 v[162:163], v[162:163], s[80:81]\n\t"
    "v_cvt_pk_bf16_f32 v148, v148, v149\n\t"
    "s_waitcnt lgkmcnt(10)\n\t"
    "v_mfma_f32_16x16x32_bf16 v[12:15], v[236:239], v[196:199], v[12:15]\n\t"
    "ds_read_b64_tr_b16 v[224:225], v249 offset:25600\n\t"
    "ds_read_b64_tr_b16 v[226:227], v249 offset:25856\n\t"
    "v_cvt_pk_bf16_f32 v149, v150, v151\n\t"
    "v_cvt_pk_bf16_f32 v150, v160, v161\n\t"
    "v_cvt_pk_bf16_f32 v151, v162, v163\n\t"
    "v_mfma_f32_16x16x32_bf16 v[16:19], v[236:239], v[204:207], v[16:19]\n\t"
    "v_cndmask_b32 v160, v148, v150, s[82:83]\n\t"
    "v_cndmask_b32 v162, v150, v148, s[82:83]\n\t"
    "v_cndmask_b32 v161, v149, v151, s[82:83]\n\t"
    "v_cndmask_b32 v163, v151, v149, s[82:83]\n\t"
    "ds_write_b128 v254, v[160:163] offset:24576\n\t"
    "v_mfma_f32_16x16x32_bf16 v[20:23], v[236:239], v[212:215], v[20:23]\n\t"
    "v_pk_mul_f32 v[152:153], v[152:153], s[80:81]\n\t"
    "v_pk_mul_f32 v[154:155], v[154:155], s[80:81]\n\t"
    "v_pk_mul_f32 v[164:165], v[164:165], s[80:81]\n\t"
    "v_pk_mul_f32 v[166:167], v[166:167], s[80:81]\n\t"
    "s_waitcnt lgkmcnt(10)\n\t"
    "v_mfma_f32_16x16x32_bf16 v[24:27], v[240:243], v[196:199], v[24:27]\n\t"
    "ds_read_b64_tr_b16 v[228:229], v251 offset:26112\n\t"
    "ds_read_b64_tr_b16 v[230:231], v251 offset:26368\n\t"
    "v_cvt_pk_bf16_f32 v152, v152, v153\n\t"
    "v_cvt_pk_bf16_f32 v153, v154, v155\n\t"
    "v_cvt_pk_bf16_f32 v154, v164, v165\n\t"
    "v_mfma_f32_16x16x32_bf16 v[28:31], v[240:243], v[204:207], v[28:31]\n\t"
    "v_cvt_pk_bf16_f32 v155, v166, v167\n\t"
    "v_cndmask_b32 v164, v152, v154, s[82:83]\n\t"
    "v_cndmask_b32 v166, v154, v152, s[82:83]\n\t"
    "v_cndmask_b32 v165, v153, v155, s[82:83]\n\t"
    "v_cndmask_b32 v167, v155, v153, s[82:83]\n\t"
    "v_mfma_f32_16x16x32_bf16 v[32:35], v[240:243], v[212:215], v[32:35]\n\t"
    "ds_write_b128 v254, v[164:167] offset:28672\n\t"
    "v_pk_mul_f32 v[168:169], v[168:169], s[80:81]\n\t"
    "v_pk_mul_f32 v[170:171], v[170:171], s[80:81]\n\t"
    "v_pk_mul_f32 v[180:181], v[180:181], s[80:81]\n\t"
    "s_waitcnt lgkmcnt(10)\n\t"
    "v_mfma_f32_16x16x32_bf16 v[36:39], v[216:219], v[196:199], v[36:39]\n\t"
    "ds_read_b64_tr_b16 v[232:233], v249 offset:26112\n\t"
    "ds_read_b64_tr_b16 v[234:235], v249 offset:26368\n\t"
    "v_pk_mul_f32 v[182:183], v[182:183], s[80:81]\n\t"
    "v_cvt_pk_bf16_f32 v168, v168, v169\n\t"
    "v_cvt_pk_bf16_f32 v169, v170, v171\n\t"
    "v_mfma_f32_16x16x32_bf16 v[40:43], v[216:219], v[204:207], v[40:43]\n\t"
    "v_cvt_pk_bf16_f32 v170, v180, v181\n\t"
    "v_cvt_pk_bf16_f32 v171, v182, v183\n\t"
    "v_cndmask_b32 v180, v168, v170, s[82:83]\n\t"
    "v_cndmask_b32 v182, v170, v168, s[82:83]\n\t"
    "v_cndmask_b32 v181, v169, v171, s[82:83]\n\t"
    "v_mfma_f32_16x16x32_bf16 v[44:47], v[216:219], v[212:215], v[44:47]\n\t"
    "v_cndmask_b32 v183, v171, v169, s[82:83]\n\t"
    "ds_write_b128 v254, v[180:183] offset:3072\n\t"
    "v_pk_mul_f32 v[172:173], v[172:173], s[80:81]\n\t"
    "v_pk_mul_f32 v[174:175], v[174:175], s[80:81]\n\t"
    "s_waitcnt lgkmcnt(10)\n\t"
    "v_mfma_f32_16x16x32_bf16 v[48:51], v[220:223], v[196:199], v[48:51]\n\t"
    "s_waitcnt lgkmcnt(0)\n\t"
    "s_barrier\n\t"
    "v_pk_mul_f32 v[184:185], v[184:185], s[80:81]\n\t"
    "v_pk_mul_f32 v[186:187], v[186:187], s[80:81]\n\t"
    "v_mfma_f32_16x16x32_bf16 v[52:55], v[220:223], v[204:207], v[52:55]\n\t"
    "v_cvt_pk_bf16_f32 v172, v172, v173\n\t"
    "v_cvt_pk_bf16_f32 v173, v174, v175\n\t"
    "v_cvt_pk_bf16_f32 v174, v184, v185\n\t"
    "v_cvt_pk_bf16_f32 v175, v186, v187\n\t"
    "v_cndmask_b32 v184, v172, v174, s[82:83]\n\t"
    "v_mfma_f32_16x16x32_bf16 v[56:59], v[220:223], v[212:215], v[56:59]\n\t"
    "v_cndmask_b32 v186, v174, v172, s[82:83]\n\t"
    "v_cndmask_b32 v185, v173, v175, s[82:83]\n\t"
    "v_cndmask_b32 v187, v175, v173, s[82:83]\n\t"
    "ds_write_b128 v254, v[184:187] offset:25600\n\t"
    "v_pk_mul_f32 v[176:177], v[176:177], s[80:81]\n\t"
    "v_mfma_f32_16x16x32_bf16 v[60:63], v[224:227], v[196:199], v[60:63]\n\t"
    "v_pk_mul_f32 v[178:179], v[178:179], s[80:81]\n\t"
    "v_pk_mul_f32 v[188:189], v[188:189], s[80:81]\n\t"
    "v_pk_mul_f32 v[190:191], v[190:191], s[80:81]\n\t"
    "v_cvt_pk_bf16_f32 v176, v176, v177\n\t"
    "v_cvt_pk_bf16_f32 v177, v178, v179\n\t"
    "v_mfma_f32_16x16x32_bf16 v[64:67], v[224:227], v[204:207], v[64:67]\n\t"
    "v_cvt_pk_bf16_f32 v178, v188, v189\n\t"
    "v_cvt_pk_bf16_f32 v179, v190, v191\n\t"
    "v_cndmask_b32 v188, v176, v178, s[82:83]\n\t"
    "v_cndmask_b32 v190, v178, v176, s[82:83]\n\t"
    "v_cndmask_b32 v189, v177, v179, s[82:83]\n\t"
    "v_mfma_f32_16x16x32_bf16 v[68:71], v[224:227], v[212:215], v[68:71]\n\t"
    "v_cndmask_b32 v191, v179, v177, s[82:83]\n\t"
    "ds_write_b128 v254, v[188:191] offset:29696\n\t"
    "v_add_u32 v247, s88, v245\n\t"
    "v_xor_b32 v248, 64, v247\n\t"
    "v_mfma_f32_16x16x32_bf16 v[72:75], v[228:231], v[196:199], v[72:75]\n\t"
    "v_mfma_f32_16x16x32_bf16 v[76:79], v[228:231], v[204:207], v[76:79]\n\t"
    "v_mfma_f32_16x16x32_bf16 v[80:83], v[228:231], v[212:215], v[80:83]\n\t"
    "v_mfma_f32_16x16x32_bf16 v[84:87], v[232:235], v[196:199], v[84:87]\n\t"
    "v_mfma_f32_16x16x32_bf16 v[88:91], v[232:235], v[204:207], v[88:91]\n\t"
    "v_mfma_f32_16x16x32_bf16 v[92:95], v[232:235], v[212:215], v[92:95]\n\t"
    "ds_read_b128 v[204:207], v247 offset:0\n\t"
    "ds_read_b128 v[208:211], v247 offset:4096\n\t"
    "ds_read_b128 v[212:215], v248 offset:0\n\t"
    "ds_read_b128 v[216:219], v248 offset:4096\n\t"
    "ds_read_b128 v[220:223], v247 offset:1024\n\t"
    "v_pk_mul_f32 v[0:1], v[0:1], s[80:81]\n\t"
    "v_pk_mul_f32 v[2:3], v[2:3], s[80:81]\n\t"
    "v_pk_mul_f32 v[12:13], v[12:13], s[80:81]\n\t"
    "v_pk_mul_f32 v[14:15], v[14:15], s[80:81]\n\t"
    "v_cvt_pk_bf16_f32 v0, v0, v1\n\t"
    "v_cvt_pk_bf16_f32 v1, v2, v3\n\t"
    "v_cvt_pk_bf16_f32 v2, v12, v13\n\t"
    "v_cvt_pk_bf16_f32 v3, v14, v15\n\t"
    "v_cndmask_b32 v96, v0, v2, s[82:83]\n\t"
    "v_cndmask_b32 v98, v2, v0, s[82:83]\n\t"
    "v_cndmask_b32 v97, v1, v3, s[82:83]\n\t"
    "v_cndmask_b32 v99, v3, v1, s[82:83]\n\t"
    "v_pk_mul_f32 v[4:5], v[4:5], s[80:81]\n\t"
    "v_pk_mul_f32 v[6:7], v[6:7], s[80:81]\n\t"
    "v_pk_mul_f32 v[16:17], v[16:17], s[80:81]\n\t"
    "v_pk_mul_f32 v[18:19], v[18:19], s[80:81]\n\t"
    "v_cvt_pk_bf16_f32 v4, v4, v5\n\t"
    "v_cvt_pk_bf16_f32 v5, v6, v7\n\t"
    "v_cvt_pk_bf16_f32 v6, v16, v17\n\t"
    "v_cvt_pk_bf16_f32 v7, v18, v19\n\t"
    "v_cndmask_b32 v112, v4, v6, s[82:83]\n\t"
    "v_cndmask_b32 v114, v6, v4, s[82:83]\n\t"
    "v_cndmask_b32 v113, v5, v7, s[82:83]\n\t"
    "v_cndmask_b32 v115, v7, v5, s[82:83]\n\t"
    "v_pk_mul_f32 v[8:9], v[8:9], s[80:81]\n\t"
    "v_pk_mul_f32 v[10:11], v[10:11], s[80:81]\n\t"
    "v_pk_mul_f32 v[20:21], v[20:21], s[80:81]\n\t"
    "v_pk_mul_f32 v[22:23], v[22:23], s[80:81]\n\t"
    "v_cvt_pk_bf16_f32 v8, v8, v9\n\t"
    "v_cvt_pk_bf16_f32 v9, v10, v11\n\t"
    "v_cvt_pk_bf16_f32 v10, v20, v21\n\t"
    "v_cvt_pk_bf16_f32 v11, v22, v23\n\t"
    "v_cndmask_b32 v128, v8, v10, s[82:83]\n\t"
    "v_cndmask_b32 v130, v10, v8, s[82:83]\n\t"
    "v_cndmask_b32 v129, v9, v11, s[82:83]\n\t"
    "v_cndmask_b32 v131, v11, v9, s[82:83]\n\t"
    "s_waitcnt lgkmcnt(4)\n\t"
    "v_mfma_f32_16x16x32_bf16 v[144:147], v[204:207], v[96:99], 0\n\t"
    "ds_read_b128 v[224:227], v247 offset:5120\n\t"
    "v_pk_mul_f32 v[24:25], v[24:25], s[80:81]\n\t"
    "v_pk_mul_f32 v[26:27], v[26:27], s[80:81]\n\t"
    "v_pk_mul_f32 v[36:37], v[36:37], s[80:81]\n\t"
    "v_pk_mul_f32 v[38:39], v[38:39], s[80:81]\n\t"
    "v_cvt_pk_bf16_f32 v24, v24, v25\n\t"
    "v_cvt_pk_bf16_f32 v25, v26, v27\n\t"
    "v_mfma_f32_16x16x32_bf16 v[152:155], v[204:207], v[112:115], 0\n\t"
    "v_cvt_pk_bf16_f32 v26, v36, v37\n\t"
    "v_cvt_pk_bf16_f32 v27, v38, v39\n\t"
    "v_cndmask_b32 v100, v24, v26, s[82:83]\n\t"
    "v_cndmask_b32 v102, v26, v24, s[82:83]\n\t"
    "v_cndmask_b32 v101, v25, v27, s[82:83]\n\t"
    "v_cndmask_b32 v103, v27, v25, s[82:83]\n\t"
    "v_mfma_f32_16x16x32_bf16 v[160:163], v[204:207], v[128:131], 0\n\t"
    "v_pk_mul_f32 v[28:29], v[28:29], s[80:81]\n\t"
    "v_pk_mul_f32 v[30:31], v[30:31], s[80:81]\n\t"
    "v_pk_mul_f32 v[40:41], v[40:41], s[80:81]\n\t"
    "v_pk_mul_f32 v[42:43], v[42:43], s[80:81]\n\t"
    "v_cvt_pk_bf16_f32 v28, v28, v29\n\t"
    "v_cvt_pk_bf16_f32 v29, v30, v31\n\t"
    "s_waitcnt lgkmcnt(4)\n\t"
    "v_mfma_f32_16x16x32_bf16 v[148:151], v[208:211], v[96:99], 0\n\t"
    "ds_read_b128 v[228:231], v248 offset:1024\n\t"
    "v_cvt_pk_bf16_f32 v30, v40, v41\n\t"
    "v_cvt_pk_bf16_f32 v31, v42, v43\n\t"
    "v_cndmask_b32 v116, v28, v30, s[82:83]\n\t"
    "v_cndmask_b32 v118, v30, v28, s[82:83]\n\t"
    "v_cndmask_b32 v117, v29, v31, s[82:83]\n\t"
    "v_cndmask_b32 v119, v31, v29, s[82:83]\n\t"
    "v_mfma_f32_16x16x32_bf16 v[156:159], v[208:211], v[112:115], 0\n\t"
    "v_pk_mul_f32 v[32:33], v[32:33], s[80:81]\n\t"
    "v_pk_mul_f32 v[34:35], v[34:35], s[80:81]\n\t"
    "v_pk_mul_f32 v[44:45], v[44:45], s[80:81]\n\t"
    "v_pk_mul_f32 v[46:47], v[46:47], s[80:81]\n\t"
    "v_cvt_pk_bf16_f32 v32, v32, v33\n\t"
    "v_cvt_pk_bf16_f32 v33, v34, v35\n\t"
    "v_mfma_f32_16x16x32_bf16 v[164:167], v[208:211], v[128:131], 0\n\t"
    "v_cvt_pk_bf16_f32 v34, v44, v45\n\t"
    "v_cvt_pk_bf16_f32 v35, v46, v47\n\t"
    "v_cndmask_b32 v132, v32, v34, s[82:83]\n\t"
    "v_cndmask_b32 v134, v34, v32, s[82:83]\n\t"
    "v_cndmask_b32 v133, v33, v35, s[82:83]\n\t"
    "v_cndmask_b32 v135, v35, v33, s[82:83]\n\t"
    "s_waitcnt lgkmcnt(4)\n\t"
    "v_mfma_f32_16x16x32_bf16 v[144:147], v[212:215], v[100:103], v[144:147]\n\t"
    "ds_read_b128 v[232:235], v248 offset:5120\n\t"
    "v_pk_mul_f32 v[48:49], v[48:49], s[80:81]\n\t"
    "v_pk_mul_f32 v[50:51], v[50:51], s[80:81]\n\t"
    "v_pk_mul_f32 v[60:61], v[60:61], s[80:81]\n\t"
    "v_pk_mul_f32 v[62:63], v[62:63], s[80:81]\n\t"
    "v_cvt_pk_bf16_f32 v48, v48, v49\n\t"
    "v_cvt_pk_bf16_f32 v49, v50, v51\n\t"
    "v_mfma_f32_16x16x32_bf16 v[152:155], v[212:215], v[116:119], v[152:155]\n\t"
    "v_cvt_pk_bf16_f32 v50, v60, v61\n\t"
    "v_cvt_pk_bf16_f32 v51, v62, v63\n\t"
    "v_cndmask_b32 v104, v48, v50, s[82:83]\n\t"
    "v_cndmask_b32 v106, v50, v48, s[82:83]\n\t"
    "v_cndmask_b32 v105, v49, v51, s[82:83]\n\t"
    "v_cndmask_b32 v107, v51, v49, s[82:83]\n\t"
    "v_mfma_f32_16x16x32_bf16 v[160:163], v[212:215], v[132:135], v[160:163]\n\t"
    "v_pk_mul_f32 v[52:53], v[52:53], s[80:81]\n\t"
    "v_pk_mul_f32 v[54:55], v[54:55], s[80:81]\n\t"
    "v_pk_mul_f32 v[64:65], v[64:65], s[80:81]\n\t"
    "v_pk_mul_f32 v[66:67], v[66:67], s[80:81]\n\t"
    "v_cvt_pk_bf16_f32 v52, v52, v53\n\t"
    "v_cvt_pk_bf16_f32 v53, v54, v55\n\t"
    "s_waitcnt lgkmcnt(4)\n\t"
    "v_mfma_f32_16x16x32_bf16 v[148:151], v[216:219], v[100:103], v[148:151]\n\t"
    "ds_read_b128 v[204:207], v247 offset:8192\n\t"
    "v_cvt_pk_bf16_f32 v54, v64, v65\n\t"
    "v_cvt_pk_bf16_f32 v55, v66, v67\n\t"
    "v_cndmask_b32 v120, v52, v54, s[82:83]\n\t"
    "v_cndmask_b32 v122, v54, v52, s[82:83]\n\t"
    "v_cndmask_b32 v121, v53, v55, s[82:83]\n\t"
    "v_cndmask_b32 v123, v55, v53, s[82:83]\n\t"
    "v_mfma_f32_16x16x32_bf16 v[156:159], v[216:219], v[116:119], v[156:159]\n\t"
    "v_pk_mul_f32 v[56:57], v[56:57], s[80:81]\n\t"
    "v_pk_mul_f32 v[58:59], v[58:59], s[80:81]\n\t"
    "v_pk_mul_f32 v[68:69], v[68:69], s[80:81]\n\t"
    "v_pk_mul_f32 v[70:71], v[70:71], s[80:81]\n\t"
    "v_cvt_pk_bf16_f32 v56, v56, v57\n\t"
    "v_cvt_pk_bf16_f32 v57, v58, v59\n\t"
    "v_mfma_f32_16x16x32_bf16 v[164:167], v[216:219], v[132:135], v[164:167]\n\t"
    "v_cvt_pk_bf16_f32 v58, v68, v69\n\t"
    "v_cvt_pk_bf16_f32 v59, v70, v71\n\t"
    "v_cndmask_b32 v136, v56, v58, s[82:83]\n\t"
    "v_cndmask_b32 v138, v58, v56, s[82:83]\n\t"
    "v_cndmask_b32 v137, v57, v59, s[82:83]\n\t"
    "v_cndmask_b32 v139, v59, v57, s[82:83]\n\t"
    "s_waitcnt lgkmcnt(4)\n\t"
    "v_mfma_f32_16x16x32_bf16 v[144:147], v[220:223], v[104:107], v[144:147]\n\t"
    "ds_read_b128 v[208:211], v248 offset:8192\n\t"
    "v_pk_mul_f32 v[72:73], v[72:73], s[80:81]\n\t"
    "v_pk_mul_f32 v[74:75], v[74:75], s[80:81]\n\t"
    "v_pk_mul_f32 v[84:85], v[84:85], s[80:81]\n\t"
    "v_pk_mul_f32 v[86:87], v[86:87], s[80:81]\n\t"
    "v_cvt_pk_bf16_f32 v72, v72, v73\n\t"
    "v_cvt_pk_bf16_f32 v73, v74, v75\n\t"
    "v_mfma_f32_16x16x32_bf16 v[152:155], v[220:223], v[120:123], v[152:155]\n\t"
    "v_cvt_pk_bf16_f32 v74, v84, v85\n\t"
    "v_cvt_pk_bf16_f32 v75, v86, v87\n\t"
    "v_cndmask_b32 v108, v72, v74, s[82:83]\n\t"
    "v_cndmask_b32 v110, v74, v72, s[82:83]\n\t"
    "v_cndmask_b32 v109, v73, v75, s[82:83]\n\t"
    "v_cndmask_b32 v111, v75, v73, s[82:83]\n\t"
    "v_mfma_f32_16x16x32_bf16 v[160:163], v[220:223], v[136:139], v[160:163]\n\t"
    "v_pk_mul_f32 v[76:77], v[76:77], s[80:81]\n\t"
    "v_pk_mul_f32 v[78:79], v[78:79], s[80:81]\n\t"
    "v_pk_mul_f32 v[88:89], v[88:89], s[80:81]\n\t"
    "v_pk_mul_f32 v[90:91], v[90:91], s[80:81]\n\t"
    "v_cvt_pk_bf16_f32 v76, v76, v77\n\t"
    "v_cvt_pk_bf16_f32 v77, v78, v79\n\t"
    "s_waitcnt lgkmcnt(4)\n\t"
    "v_mfma_f32_16x16x32_bf16 v[148:151], v[224:227], v[104:107], v[148:151]\n\t"
    "ds_read_b128 v[212:215], v247 offset:9216\n\t"
    "v_cvt_pk_bf16_f32 v78, v88, v89\n\t"
    "v_cvt_pk_bf16_f32 v79, v90, v91\n\t"
    "v_cndmask_b32 v124, v76, v78, s[82:83]\n\t"
    "v_cndmask_b32 v126, v78, v76, s[82:83]\n\t"
    "v_cndmask_b32 v125, v77, v79, s[82:83]\n\t"
    "v_cndmask_b32 v127, v79, v77, s[82:83]\n\t"
    "v_mfma_f32_16x16x32_bf16 v[156:159], v[224:227], v[120:123], v[156:159]\n\t"
    "v_pk_mul_f32 v[80:81], v[80:81], s[80:81]\n\t"
    "v_pk_mul_f32 v[82:83], v[82:83], s[80:81]\n\t"
    "v_pk_mul_f32 v[92:93], v[92:93], s[80:81]\n\t"
    "v_pk_mul_f32 v[94:95], v[94:95], s[80:81]\n\t"
    "v_cvt_pk_bf16_f32 v80, v80, v81\n\t"
    "v_cvt_pk_bf16_f32 v81, v82, v83\n\t"
    "v_mfma_f32_16x16x32_bf16 v[164:167], v[224:227], v[136:139], v[164:167]\n\t"
    "v_cvt_pk_bf16_f32 v82, v92, v93\n\t"
    "v_cvt_pk_bf16_f32 v83, v94, v95\n\t"
    "v_cndmask_b32 v140, v80, v82, s[82:83]\n\t"
    "v_cndmask_b32 v142, v82, v80, s[82:83]\n\t"
    "v_cndmask_b32 v141, v81, v83, s[82:83]\n\t"
    "v_cndmask_b32 v143, v83, v81, s[82:83]\n\t"
    "s_waitcnt lgkmcnt(4)\n\t"
    "v_mfma_f32_16x16x32_bf16 v[144:147], v[228:231], v[108:111], v[144:147]\n\t"
    "ds_read_b128 v[216:219], v248 offset:9216\n\t"
    "v_mfma_f32_16x16x32_bf16 v[152:155], v[228:231], v[124:127], v[152:155]\n\t"
    "v_mfma_f32_16x16x32_bf16 v[160:163], v[228:231], v[140:143], v[160:163]\n\t"
    "s_waitcnt lgkmcnt(4)\n\t"
    "v_mfma_f32_16x16x32_bf16 v[148:151], v[232:235], v[108:111], v[148:151]\n\t"
    "ds_read_b128 v[220:223], v247 offset:12288\n\t"
    "v_mfma_f32_16x16x32_bf16 v[156:159], v[232:235], v[124:127], v[156:159]\n\t"
    "v_mfma_f32_16x16x32_bf16 v[164:167], v[232:235], v[140:143], v[164:167]\n\t"
    "v_max3_f32 v239, v144, v145, v146\n\t"
    "s_nop 0\n\t"
    "v_max3_f32 v240, v152, v153, v154\n\t"
    "v_max3_f32 v241, v160, v161, v162\n\t"
    "s_nop 0\n\t"
    "v_max3_f32 v239, v239, v147, v148\n\t"
    "s_nop 0\n\t"
    "v_max3_f32 v240, v240, v155, v156\n\t"
    "v_max3_f32 v241, v241, v163, v164\n\t"
    "v_max3_f32 v239, v239, v149, v150\n\t"
    "v_max3_f32 v240, v240, v157, v158\n\t"
    "v_max3_f32 v241, v241, v165, v166\n\t"
    "v_max_f32 v239, v239, v151\n\t"
    "v_max_f32 v240, v240, v159\n\t"
    "v_max_f32 v241, v241, v167\n\t"
    "v_mov_b32 v242, v239\n\t"
    "v_mov_b32 v243, v240\n\t"
    "v_mov_b32 v244, v241\n\t"
    "v_permlane16_swap_b32 v239, v242\n\t"
    "v_permlane16_swap_b32 v240, v243\n\t"
    "v_permlane16_swap_b32 v241, v244\n\t"
    "v_max_f32 v239, v239, v242\n\t"
    "v_max_f32 v240, v240, v243\n\t"
    "v_max_f32 v241, v241, v244\n\t"
    "v_mov_b32 v242, v239\n\t"
    "v_mov_b32 v243, v240\n\t"
    "v_mov_b32 v244, v241\n\t"
    "v_permlane32_swap_b32 v239, v242\n\t"
    "v_permlane32_swap_b32 v240, v243\n\t"
    "v_permlane32_swap_b32 v241, v244\n\t"
    "v_max_f32 v239, v239, v242\n\t"
    "v_max_f32 v240, v240, v243\n\t"
    "v_max_f32 v241, v241, v244\n\t"
    "v_sub_f32 v192, 0, v239\n\t"
    "v_sub_f32 v193, 0, v239\n\t"
    "v_sub_f32 v194, 0, v239\n\t"
    "v_sub_f32 v195, 0, v239\n\t"
    "v_sub_f32 v196, 0, v240\n\t"
    "v_sub_f32 v197, 0, v240\n\t"
    "v_sub_f32 v198, 0, v240\n\t"
    "v_sub_f32 v199, 0, v240\n\t"
    "v_sub_f32 v200, 0, v241\n\t"
    "v_sub_f32 v201, 0, v241\n\t"
    "v_sub_f32 v202, 0, v241\n\t"
    "v_sub_f32 v203, 0, v241\n\t"
    "s_waitcnt lgkmcnt(4)\n\t"
    "v_mfma_f32_16x16x32_bf16 v[168:171], v[204:207], v[96:99], v[192:195]\n\t"
    "ds_read_b128 v[224:227], v248 offset:12288\n\t"
    "s_add_u32 s88, s88, 16384\n\t"
    "s_cmp_eq_u32 s88, 98304\n\t"
    "s_cselect_b32 s88, 0, s88\n\t"
    "v_add_u32 v249, s88, v246\n\t"
    "v_xor_b32 v250, 32, v249\n\t"
    "v_sub_f32 v144, v144, v239\n\t"
    "v_mfma_f32_16x16x32_bf16 v[176:179], v[204:207], v[112:115], v[196:199]\n\t"
    "v_sub_f32 v145, v145, v239\n\t"
    "v_sub_f32 v146, v146, v239\n\t"
    "v_sub_f32 v147, v147, v239\n\t"
    "v_sub_f32 v148, v148, v239\n\t"
    "v_sub_f32 v149, v149, v239\n\t"
    "v_mfma_f32_16x16x32_bf16 v[184:187], v[204:207], v[128:131], v[200:203]\n\t"
    "v_sub_f32 v150, v150, v239\n\t"
    "v_sub_f32 v151, v151, v239\n\t"
    "v_mov_b32 v236, 0\n\t"
    "v_sub_f32 v152, v152, v240\n\t"
    "v_sub_f32 v153, v153, v240\n\t"
    "s_waitcnt lgkmcnt(4)\n\t"
    "v_mfma_f32_16x16x32_bf16 v[168:171], v[208:211], v[100:103], v[168:171]\n\t"
    "ds_read_b128 v[228:231], v247 offset:13312\n\t"
    "v_sub_f32 v154, v154, v240\n\t"
    "v_sub_f32 v155, v155, v240\n\t"
    "v_sub_f32 v156, v156, v240\n\t"
    "v_mfma_f32_16x16x32_bf16 v[176:179], v[208:211], v[116:119], v[176:179]\n\t"
    "v_sub_f32 v157, v157, v240\n\t"
    "v_sub_f32 v158, v158, v240\n\t"
    "v_sub_f32 v159, v159, v240\n\t"
    "v_mov_b32 v237, 0\n\t"
    "v_mfma_f32_16x16x32_bf16 v[184:187], v[208:211], v[132:135], v[184:187]\n\t"
    "v_sub_f32 v160, v160, v241\n\t"
    "v_sub_f32 v161, v161, v241\n\t"
    "v_sub_f32 v162, v162, v241\n\t"
    "v_sub_f32 v163, v163, v241\n\t"
    "s_waitcnt lgkmcnt(4)\n\t"
    "v_mfma_f32_16x16x32_bf16 v[168:171], v[212:215], v[104:107], v[168:171]\n\t"
    "ds_read_b128 v[232:235], v248 offset:13312\n\t"
    "v_sub_f32 v164, v164, v241\n\t"
    "v_sub_f32 v165, v165, v241\n\t"
    "v_sub_f32 v166, v166, v241\n\t"
    "v_mfma_f32_16x16x32_bf16 v[176:179], v[212:215], v[120:123], v[176:179]\n\t"
    "v_sub_f32 v167, v167, v241\n\t"
    "v_mov_b32 v238, 0\n\t"
    "v_exp_f32 v144, v144\n\t"
    "v_exp_f32 v145, v145\n\t"
    "v_mfma_f32_16x16x32_bf16 v[184:187], v[212:215], v[136:139], v[184:187]\n\t"
    "v_add_f32 v236, v236, v144\n\t"
    "v_exp_f32 v146, v146\n\t"
    "v_add_f32 v236, v236, v145\n\t"
    "v_cvt_pk_bf16_f32 v144, v144, v145\n\t"
    "s_waitcnt lgkmcnt(4)\n\t"
    "v_mfma_f32_16x16x32_bf16 v[168:171], v[216:219], v[108:111], v[168:171]\n\t"
    "v_add_u32 v247, s88, v245\n\t"
    "v_xor_b32 v248, 64, v247\n\t"
    "v_exp_f32 v147, v147\n\t"
    "v_add_f32 v236, v236, v146\n\t"
    "v_exp_f32 v148, v148\n\t"
    "v_mfma_f32_16x16x32_bf16 v[176:179], v[216:219], v[124:127], v[176:179]\n\t"
    "v_add_f32 v236, v236, v147\n\t"
    "v_cvt_pk_bf16_f32 v145, v146, v147\n\t"
    "v_exp_f32 v149, v149\n\t"
    "v_add_f32 v236, v236, v148\n\t"
    "v_mfma_f32_16x16x32_bf16 v[184:187], v[216:219], v[140:143], v[184:187]\n\t"
    "v_exp_f32 v150, v150\n\t"
    "v_add_f32 v236, v236, v149\n\t"
    "v_cvt_pk_bf16_f32 v146, v148, v149\n\t"
    "v_exp_f32 v151, v151\n\t"
    "s_waitcnt lgkmcnt(3)\n\t"
    "v_mfma_f32_16x16x32_bf16 v[172:175], v[220:223], v[96:99], v[192:195]\n\t"
    "s_waitcnt lgkmcnt(0)\n\t"
    "s_barrier\n\t"
    "v_add_f32 v236, v236, v150\n\t"
    "v_exp_f32 v152, v152\n\t"
    "v_add_f32 v236, v236, v151\n\t"
    "v_mfma_f32_16x16x32_bf16 v[180:183], v[220:223], v[112:115], v[196:199]\n\t"
    "v_cvt_pk_bf16_f32 v147, v150, v151\n\t"
    "v_exp_f32 v153, v153\n\t"
    "v_add_f32 v237, v237, v152\n\t"
    "v_exp_f32 v154, v154\n\t"
    "v_mfma_f32_16x16x32_bf16 v[188:191], v[220:223], v[128:131], v[200:203]\n\t"
    "v_add_f32 v237, v237, v153\n\t"
    "v_cvt_pk_bf16_f32 v152, v152, v153\n\t"
    "v_exp_f32 v155, v155\n\t"
    "v_add_f32 v237, v237, v154\n\t"
    "v_mfma_f32_16x16x32_bf16 v[172:175], v[224:227], v[100:103], v[172:175]\n\t"
    "ds_read_b64_tr_b16 v[204:205], v249 offset:0\n\t"
    "ds_read_b64_tr_b16 v[206:207], v249 offset:4096\n\t"
    "ds_read_b64_tr_b16 v[208:209], v250 offset:0\n\t"
    "ds_read_b64_tr_b16 v[210:211], v250 offset:4096\n\t"
    "v_exp_f32 v156, v156\n\t"
    "v_mfma_f32_16x16x32_bf16 v[180:183], v[224:227], v[116:119], v[180:183]\n\t"
    "v_add_f32 v237, v237, v155\n\t"
    "v_cvt_pk_bf16_f32 v153, v154, v155\n\t"
    "v_exp_f32 v157, v157\n\t"
    "v_add_f32 v237, v237, v156\n\t"
    "v_exp_f32 v158, v158\n\t"
    "v_mfma_f32_16x16x32_bf16 v[188:191], v[224:227], v[132:135], v[188:191]\n\t"
    "v_add_f32 v237, v237, v157\n\t"
    "v_cvt_pk_bf16_f32 v154, v156, v157\n\t"
    "v_exp_f32 v159, v159\n\t"
    "v_add_f32 v237, v237, v158\n\t"
    "v_exp_f32 v160, v160\n\t"
    "v_mfma_f32_16x16x32_bf16 v[172:175], v[228:231], v[104:107], v[172:175]\n\t"
    "ds_read_b64_tr_b16 v[212:213], v249 offset:512\n\t"
    "ds_read_b64_tr_b16 v[214:215], v249 offset:4608\n\t"
    "ds_read_b64_tr_b16 v[216:217], v250 offset:512\n\t"
    "ds_read_b64_tr_b16 v[218:219], v250 offset:4608\n\t"
    "v_add_f32 v237, v237, v159\n\t"
    "v_mfma_f32_16x16x32_bf16 v[180:183], v[228:231], v[120:123], v[180:183]\n\t"
    "v_cvt_pk_bf16_f32 v155, v158, v159\n\t"
    "v_exp_f32 v161, v161\n\t"
    "v_add_f32 v238, v238, v160\n\t"
    "v_exp_f32 v162, v162\n\t"
    "v_add_f32 v238, v238, v161\n\t"
    "v_mfma_f32_16x16x32_bf16 v[188:191], v[228:231], v[136:139], v[188:191]\n\t"
    "v_cvt_pk_bf16_f32 v160, v160, v161\n\t"
    "v_exp_f32 v163, v163\n\t"
    "v_add_f32 v238, v238, v162\n\t"
    "v_exp_f32 v164, v164\n\t"
    "v_add_f32 v238, v238, v163\n\t"
    "v_mfma_f32_16x16x32_bf16 v[172:175], v[232:235], v[108:111], v[172:175]\n\t"
    "ds_read_b64_tr_b16 v[220:221], v249 offset:1024\n\t"
    "ds_read_b64_tr_b16 v[222:223], v249 offset:5120\n\t"
    "v_cvt_pk_bf16_f32 v161, v162, v163\n\t"
    "v_exp_f32 v165, v165\n\t"
    "v_mfma_f32_16x16x32_bf16 v[180:183], v[232:235], v[124:127], v[180:183]\n\t"
    "v_add_f32 v238, v238, v164\n\t"
    "v_exp_f32 v166, v166\n\t"
    "v_add_f32 v238, v238, v165\n\t"
    "v_cvt_pk_bf16_f32 v162, v164, v165\n\t"
    "v_mfma_f32_16x16x32_bf16 v[188:191], v[232:235], v[140:143], v[188:191]\n\t"
    "v_exp_f32 v167, v167\n\t"
    "v_add_f32 v238, v238, v166\n\t"
    "v_add_f32 v238, v238, v167\n\t"
    "v_cvt_pk_bf16_f32 v163, v166, v167\n\t"
    "s_branch L_att_%=\n\t"
    "L_item_%=:\n\t"
    "s_mul_i32 s84, s26, 6144\n\t"
    "s_add_u32 s84, s84, 114688\n\t"
    "s_add_u32 s84, s84, s27\n\t"
    "v_add_u32 v254, s84, v253\n\t"
    "ds_read_b128 v[96:99], v254 offset:0\n\t"
    "ds_read_b128 v[112:115], v254 offset:4096\n\t"
    "ds_read_b128 v[128:131], v254 offset:26624\n\t"
    "ds_read_b128 v[100:103], v254 offset:1024\n\t"
    "ds_read_b128 v[116:119], v254 offset:5120\n\t"
    "ds_read_b128 v[132:135], v254 offset:27648\n\t"
    "ds_read_b128 v[104:107], v254 offset:2048\n\t"
    "ds_read_b128 v[120:123], v254 offset:24576\n\t"
    "ds_read_b128 v[136:139], v254 offset:28672\n\t"
    "ds_read_b128 v[108:111], v254 offset:3072\n\t"
    "ds_read_b128 v[124:127], v254 offset:25600\n\t"
    "ds_read_b128 v[140:143], v254 offset:29696\n\t"
    "v_add_u32 v247, s88, v245\n\t"
    "v_xor_b32 v248, 64, v247\n\t"
    "s_waitcnt lgkmcnt(0)\n\t"
    "s_barrier\n\t"
    "ds_read_b128 v[204:207], v247 offset:0\n\t"
    "ds_read_b128 v[208:211], v247 offset:4096\n\t"
    "ds_read_b128 v[212:215], v248 offset:0\n\t"
    "ds_read_b128 v[216:219], v248 offset:4096\n\t"
    "ds_read_b128 v[220:223], v247 offset:1024\n\t"
    "s_waitcnt lgkmcnt(4)\n\t"
    "v_mfma_f32_16x16x32_bf16 v[144:147], v[204:207], v[96:99], 0\n\t"
    "ds_read_b128 v[224:227], v247 offset:5120\n\t"
    "v_mfma_f32_16x16x32_bf16 v[152:155], v[204:207], v[112:115], 0\n\t"
    "s_mov_b32 s86, 0\n\t"
    "buffer_store_dwordx4 v[0:3], v253, s[56:59], s86 offen offset:0 sc1\n\t"
    "v_mfma_f32_16x16x32_bf16 v[160:163], v[204:207], v[128:131], 0\n\t"
    "s_waitcnt lgkmcnt(4)\n\t"
    "v_mfma_f32_16x16x32_bf16 v[148:151], v[208:211], v[96:99], 0\n\t"
    "ds_read_b128 v[228:231], v248 offset:1024\n\t"
    "v_mfma_f32_16x16x32_bf16 v[156:159], v[208:211], v[112:115], 0\n\t"
    "buffer_store_dwordx4 v[4:7], v253, s[56:59], s86 offen offset:1024 sc1\n\t"
    "v_mfma_f32_16x16x32_bf16 v[164:167], v[208:211], v[128:131], 0\n\t"
    "s_waitcnt lgkmcnt(4)\n\t"
    "v_mfma_f32_16x16x32_bf16 v[144:147], v[212:215], v[100:103], v[144:147]\n\t"
    "ds_read_b128 v[232:235], v248 offset:5120\n\t"
    "v_mfma_f32_16x16x32_bf16 v[152:155], v[212:215], v[116:119], v[152:155]\n\t"
    "buffer_store_dwordx4 v[8:11], v253, s[56:59], s86 offen offset:2048 sc1\n\t"
    "v_mfma_f32_16x16x32_bf16 v[160:163], v[212:215], v[132:135], v[160:163]\n\t"
    "s_waitcnt lgkmcnt(4)\n\t"
    "v_mfma_f32_16x16x32_bf16 v[148:151], v[216:219], v[100:103], v[148:151]\n\t"
    "ds_read_b128 v[204:207], v247 offset:8192\n\t"
    "v_mfma_f32_16x16x32_bf16 v[156:159], v[216:219], v[116:119], v[156:159]\n\t"
    "s_mov_b32 s86, 12288\n\t"
    "buffer_store_dwordx4 v[24:27], v253, s[56:59], s86 offen offset:0 sc1\n\t"
    "v_mfma_f32_16x16x32_bf16 v[164:167], v[216:219], v[132:135], v[164:167]\n\t"
    "s_waitcnt lgkmcnt(4)\n\t"
    "v_mfma_f32_16x16x32_bf16 v[144:147], v[220:223], v[104:107], v[144:147]\n\t"
    "ds_read_b128 v[208:211], v248 offset:8192\n\t"
    "v_mfma_f32_16x16x32_bf16 v[152:155], v[220:223], v[120:123], v[152:155]\n\t"
    "buffer_store_dwordx4 v[28:31], v253, s[56:59], s86 offen offset:1024 sc1\n\t"
    "v_mfma_f32_16x16x32_bf16 v[160:163], v[220:223], v[136:139], v[160:163]\n\t"
    "s_waitcnt lgkmcnt(4)\n\t"
    "v_mfma_f32_16x16x32_bf16 v[148:151], v[224:227], v[104:107], v[148:151]\n\t"
    "ds_read_b128 v[212:215], v247 offset:9216\n\t"
    "v_mfma_f32_16x16x32_bf16 v[156:159], v[224:227], v[120:123], v[156:159]\n\t"
    "buffer_store_dwordx4 v[32:35], v253, s[56:59], s86 offen offset:2048 sc1\n\t"
    "v_mfma_f32_16x16x32_bf16 v[164:167], v[224:227], v[136:139], v[164:167]\n\t"
    "s_waitcnt lgkmcnt(4)\n\t"
    "v_mfma_f32_16x16x32_bf16 v[144:147], v[228:231], v[108:111], v[144:147]\n\t"
    "ds_read_b128 v[216:219], v248 offset:9216\n\t"
    "v_mfma_f32_16x16x32_bf16 v[152:155], v[228:231], v[124:127], v[152:155]\n\t"
    "s_mov_b32 s86, 24576\n\t"
    "buffer_store_dwordx4 v[48:51], v253, s[56:59], s86 offen offset:0 sc1\n\t"
    "v_mfma_f32_16x16x32_bf16 v[160:163], v[228:231], v[140:143], v[160:163]\n\t"
    "s_waitcnt lgkmcnt(4)\n\t"
    "v_mfma_f32_16x16x32_bf16 v[148:151], v[232:235], v[108:111], v[148:151]\n\t"
    "ds_read_b128 v[220:223], v247 offset:12288\n\t"
    "v_mfma_f32_16x16x32_bf16 v[156:159], v[232:235], v[124:127], v[156:159]\n\t"
    "buffer_store_dwordx4 v[52:55], v253, s[56:59], s86 offen offset:1024 sc1\n\t"
    "v_mfma_f32_16x16x32_bf16 v[164:167], v[232:235], v[140:143], v[164:167]\n\t"
    "v_max3_f32 v239, v144, v145, v146\n\t"
    "v_max3_f32 v240, v152, v153, v154\n\t"
    "v_max3_f32 v241, v160, v161, v162\n\t"
    "s_nop 0\n\t"
    "v_max3_f32 v239, v239, v147, v148\n\t"
    "s_nop 0\n\t"
    "v_max3_f32 v240, v240, v155, v156\n\t"
    "s_nop 0\n\t"
    "v_max3_f32 v241, v241, v163, v164\n\t"
    "v_max3_f32 v239, v239, v149, v150\n\t"
    "v_max3_f32 v240, v240, v157, v158\n\t"
    "v_max3_f32 v241, v241, v165, v166\n\t"
    "v_max_f32 v239, v239, v151\n\t"
    "v_max_f32 v240, v240, v159\n\t"
    "v_max_f32 v241, v241, v167\n\t"
    "v_mov_b32 v242, v239\n\t"
    "v_mov_b32 v243, v240\n\t"
    "v_mov_b32 v244, v241\n\t"
    "v_permlane16_swap_b32 v239, v242\n\t"
    "v_permlane16_swap_b32 v240, v243\n\t"
    "v_permlane16_swap_b32 v241, v244\n\t"
    "v_max_f32 v239, v239, v242\n\t"
    "v_max_f32 v240, v240, v243\n\t"
    "v_max_f32 v241, v241, v244\n\t"
    "v_mov_b32 v242, v239\n\t"
    "v_mov_b32 v243, v240\n\t"
    "v_mov_b32 v244, v241\n\t"
    "v_permlane32_swap_b32 v239, v242\n\t"
    "v_permlane32_swap_b32 v240, v243\n\t"
    "v_permlane32_swap_b32 v241, v244\n\t"
    "v_max_f32 v239, v239, v242\n\t"
    "v_max_f32 v240, v240, v243\n\t"
    "v_max_f32 v241, v241, v244\n\t"
    "v_sub_f32 v192, 0, v239\n\t"
    "v_sub_f32 v193, 0, v239\n\t"
    "v_sub_f32 v194, 0, v239\n\t"
    "v_sub_f32 v195, 0, v239\n\t"
    "v_sub_f32 v196, 0, v240\n\t"
    "v_sub_f32 v197, 0, v240\n\t"
    "v_sub_f32 v198, 0, v240\n\t"
    "v_sub_f32 v199, 0, v240\n\t"
    "v_sub_f32 v200, 0, v241\n\t"
    "v_sub_f32 v201, 0, v241\n\t"
    "v_sub_f32 v202, 0, v241\n\t"
    "v_sub_f32 v203, 0, v241\n\t"
    "s_waitcnt lgkmcnt(4)\n\t"
    "v_mfma_f32_16x16x32_bf16 v[168:171], v[204:207], v[96:99], v[192:195]\n\t"
    "ds_read_b128 v[224:227], v248 offset:12288\n\t"
    "s_add_u32 s88, s88, 16384\n\t"
    "s_cmp_eq_u32 s88, 98304\n\t"
    "s_cselect_b32 s88, 0, s88\n\t"
    "v_add_u32 v249, s88, v246\n\t"
    "v_xor_b32 v250, 32, v249\n\t"
    "v_sub_f32 v144, v144, v239\n\t"
    "v_sub_f32 v145, v145, v239\n\t"
    "v_mfma_f32_16x16x32_bf16 v[176:179], v[204:207], v[112:115], v[196:199]\n\t"
    "v_sub_f32 v146, v146, v239\n\t"
    "v_sub_f32 v147, v147, v239\n\t"
    "v_sub_f32 v148, v148, v239\n\t"
    "v_sub_f32 v149, v149, v239\n\t"
    "v_sub_f32 v150, v150, v239\n\t"
    "v_mfma_f32_16x16x32_bf16 v[184:187], v[204:207], v[128:131], v[200:203]\n\t"
    "v_sub_f32 v151, v151, v239\n\t"
    "v_mov_b32 v236, 0\n\t"
    "v_sub_f32 v152, v152, v240\n\t"
    "v_sub_f32 v153, v153, v240\n\t"
    "buffer_store_dwordx4 v[56:59], v253, s[56:59], s86 offen offset:2048 sc1\n\t"
    "s_waitcnt lgkmcnt(4)\n\t"
    "v_mfma_f32_16x16x32_bf16 v[168:171], v[208:211], v[100:103], v[168:171]\n\t"
    "ds_read_b128 v[228:231], v247 offset:13312\n\t"
    "v_sub_f32 v154, v154, v240\n\t"
    "v_sub_f32 v155, v155, v240\n\t"
    "v_sub_f32 v156, v156, v240\n\t"
    "v_sub_f32 v157, v157, v240\n\t"
    "v_mfma_f32_16x16x32_bf16 v[176:179], v[208:211], v[116:119], v[176:179]\n\t"
    "v_sub_f32 v158, v158, v240\n\t"
    "v_sub_f32 v159, v159, v240\n\t"
    "v_mov_b32 v237, 0\n\t"
    "v_sub_f32 v160, v160, v241\n\t"
    "v_mfma_f32_16x16x32_bf16 v[184:187], v[208:211], v[132:135], v[184:187]\n\t"
    "v_sub_f32 v161, v161, v241\n\t"
    "v_sub_f32 v162, v162, v241\n\t"
    "v_sub_f32 v163, v163, v241\n\t"
    "s_mov_b32 s86, 36864\n\t"
    "buffer_store_dwordx4 v[72:75], v253, s[56:59], s86 offen offset:0 sc1\n\t"
    "s_waitcnt lgkmcnt(4)\n\t"
    "v_mfma_f32_16x16x32_bf16 v[168:171], v[212:215], v[104:107], v[168:171]\n\t"
    "ds_read_b128 v[232:235], v248 offset:13312\n\t"
    "v_sub_f32 v164, v164, v241\n\t"
    "v_sub_f32 v165, v165, v241\n\t"
    "v_sub_f32 v166, v166, v241\n\t"
    "v_mfma_f32_16x16x32_bf16 v[176:179], v[212:215], v[120:123], v[176:179]\n\t"
    "v_sub_f32 v167, v167, v241\n\t"
    "v_mov_b32 v238, 0\n\t"
    "v_exp_f32 v144, v144\n\t"
    "v_exp_f32 v145, v145\n\t"
    "v_mfma_f32_16x16x32_bf16 v[184:187], v[212:215], v[136:139], v[184:187]\n\t"
    "v_add_f32 v236, v236, v144\n\t"
    "v_exp_f32 v146, v146\n\t"
    "v_add_f32 v236, v236, v145\n\t"
    "v_cvt_pk_bf16_f32 v144, v144, v145\n\t"
    "s_waitcnt lgkmcnt(4)\n\t"
    "v_mfma_f32_16x16x32_bf16 v[168:171], v[216:219], v[108:111], v[168:171]\n\t"
    "v_add_u32 v247, s88, v245\n\t"
    "v_xor_b32 v248, 64, v247\n\t"
    "v_exp_f32 v147, v147\n\t"
    "v_add_f32 v236, v236, v146\n\t"
    "v_exp_f32 v148, v148\n\t"
    "v_mfma_f32_16x16x32_bf16 v[176:179], v[216:219], v[124:127], v[176:179]\n\t"
    "v_add_f32 v236, v236, v147\n\t"
    "v_cvt_pk_bf16_f32 v145, v146, v147\n\t"
    "v_exp_f32 v149, v149\n\t"
    "v_add_f32 v236, v236, v148\n\t"
    "v_exp_f32 v150, v150\n\t"
    "v_mfma_f32_16x16x32_bf16 v[184:187], v[216:219], v[140:143], v[184:187]\n\t"
    "v_add_f32 v236, v236, v149\n\t"
    "v_cvt_pk_bf16_f32 v146, v148, v149\n\t"
    "v_exp_f32 v151, v151\n\t"
    "buffer_store_dwordx4 v[76:79], v253, s[56:59], s86 offen offset:1024 sc1\n\t"
    "s_waitcnt lgkmcnt(3)\n\t"
    "v_mfma_f32_16x16x32_bf16 v[172:175], v[220:223], v[96:99], v[192:195]\n\t"
    "s_waitcnt lgkmcnt(0)\n\t"
    "s_barrier\n\t"
    "v_add_f32 v236, v236, v150\n\t"
    "v_exp_f32 v152, v152\n\t"
    "v_add_f32 v236, v236, v151\n\t"
    "v_mfma_f32_16x16x32_bf16 v[180:183], v[220:223], v[112:115], v[196:199]\n\t"
    "v_cvt_pk_bf16_f32 v147, v150, v151\n\t"
    "v_exp_f32 v153, v153\n\t"
    "v_add_f32 v237, v237, v152\n\t"
    "v_exp_f32 v154, v154\n\t"
    "v_mfma_f32_16x16x32_bf16 v[188:191], v[220:223], v[128:131], v[200:203]\n\t"
    "v_add_f32 v237, v237, v153\n\t"
    "v_cvt_pk_bf16_f32 v152, v152, v153\n\t"
    "v_exp_f32 v155, v155\n\t"
    "v_add_f32 v237, v237, v154\n\t"
    "v_mfma_f32_16x16x32_bf16 v[172:175], v[224:227], v[100:103], v[172:175]\n\t"
    "ds_read_b64_tr_b16 v[204:205], v249 offset:0\n\t"
    "ds_read_b64_tr_b16 v[206:207], v249 offset:4096\n\t"
    "ds_read_b64_tr_b16 v[208:209], v250 offset:0\n\t"
    "ds_read_b64_tr_b16 v[210:211], v250 offset:4096\n\t"
    "v_exp_f32 v156, v156\n\t"
    "v_add_f32 v237, v237, v155\n\t"
    "v_mfma_f32_16x16x32_bf16 v[180:183], v[224:227], v[116:119], v[180:183]\n\t"
    "v_cvt_pk_bf16_f32 v153, v154, v155\n\t"
    "v_exp_f32 v157, v157\n\t"
    "v_add_f32 v237, v237, v156\n\t"
    "v_exp_f32 v158, v158\n\t"
    "v_add_f32 v237, v237, v157\n\t"
    "v_mfma_f32_16x16x32_bf16 v[188:191], v[224:227], v[132:135], v[188:191]\n\t"
    "v_cvt_pk_bf16_f32 v154, v156, v157\n\t"
    "v_exp_f32 v159, v159\n\t"
    "v_add_f32 v237, v237, v158\n\t"
    "v_exp_f32 v160, v160\n\t"
    "buffer_store_dwordx4 v[80:83], v253, s[56:59], s86 offen offset:2048 sc1\n\t"
    "v_mfma_f32_16x16x32_bf16 v[172:175], v[228:231], v[104:107], v[172:175]\n\t"
    "ds_read_b64_tr_b16 v[212:213], v249 offset:512\n\t"
    "ds_read_b64_tr_b16 v[214:215], v249 offset:4608\n\t"
    "ds_read_b64_tr_b16 v[216:217], v250 offset:512\n\t"
    "ds_read_b64_tr_b16 v[218:219], v250 offset:4608\n\t"
    "v_add_f32 v237, v237, v159\n\t"
    "v_mfma_f32_16x16x32_bf16 v[180:183], v[228:231], v[120:123], v[180:183]\n\t"
    "v_cvt_pk_bf16_f32 v155, v158, v159\n\t"
    "v_exp_f32 v161, v161\n\t"
    "v_add_f32 v238, v238, v160\n\t"
    "v_exp_f32 v162, v162\n\t"
    "v_add_f32 v238, v238, v161\n\t"
    "v_mfma_f32_16x16x32_bf16 v[188:191], v[228:231], v[136:139], v[188:191]\n\t"
    "v_cvt_pk_bf16_f32 v160, v160, v161\n\t"
    "v_exp_f32 v163, v163\n\t"
    "v_add_f32 v238, v238, v162\n\t"
    "v_exp_f32 v164, v164\n\t"
    "v_add_f32 v238, v238, v163\n\t"
    "v_mfma_f32_16x16x32_bf16 v[172:175], v[232:235], v[108:111], v[172:175]\n\t"
    "ds_read_b64_tr_b16 v[220:221], v249 offset:1024\n\t"
    "ds_read_b64_tr_b16 v[222:223], v249 offset:5120\n\t"
    "v_cvt_pk_bf16_f32 v161, v162, v163\n\t"
    "v_exp_f32 v165, v165\n\t"
    "v_mfma_f32_16x16x32_bf16 v[180:183], v[232:235], v[124:127], v[180:183]\n\t"
    "v_add_f32 v238, v238, v164\n\t"
    "v_exp_f32 v166, v166\n\t"
    "v_add_f32 v238, v238, v165\n\t"
    "v_cvt_pk_bf16_f32 v162, v164, v165\n\t"
    "v_mfma_f32_16x16x32_bf16 v[188:191], v[232:235], v[140:143], v[188:191]\n\t"
    "v_exp_f32 v167, v167\n\t"
    "v_add_f32 v238, v238, v166\n\t"
    "v_add_f32 v238, v238, v167\n\t"
    "v_cvt_pk_bf16_f32 v163, v166, v167\n\t"
    "s_add_u32 s56, s56, 196608\n\t"
    "s_addc_u32 s57, s57, 0\n\t"
    "L_att_%=:\n\t"
    "s_waitcnt lgkmcnt(8)\n\t"
    "v_mfma_f32_16x16x32_bf16 v[0:3], v[204:207], v[144:147], 0\n\t"
    "ds_read_b64_tr_b16 v[224:225], v250 offset:1024\n\t"
    "ds_read_b64_tr_b16 v[226:227], v250 offset:5120\n\t"
    "v_max3_f32 v239, v168, v169, v170\n\t"
    "v_mfma_f32_16x16x32_bf16 v[4:7], v[204:207], v[152:155], 0\n\t"
    "v_max3_f32 v240, v176, v177, v178\n\t"
    "v_max3_f32 v241, v184, v185, v186\n\t"
    "v_mfma_f32_16x16x32_bf16 v[8:11], v[204:207], v[160:163], 0\n\t"
    "v_max3_f32 v239, v239, v171, v172\n\t"
    "v_max3_f32 v240, v240, v179, v180\n\t"
    "s_waitcnt lgkmcnt(8)\n\t"
    "v_mfma_f32_16x16x32_bf16 v[12:15], v[208:211], v[144:147], 0\n\t"
    "ds_read_b64_tr_b16 v[228:229], v249 offset:1536\n\t"
    "ds_read_b64_tr_b16 v[230:231], v249 offset:5632\n\t"
    "v_mfma_f32_16x16x32_bf16 v[16:19], v[208:211], v[152:155], 0\n\t"
    "v_max3_f32 v241, v241, v187, v188\n\t"
    "v_max3_f32 v239, v239, v173, v174\n\t"
    "v_mfma_f32_16x16x32_bf16 v[20:23], v[208:211], v[160:163], 0\n\t"
    "v_max3_f32 v240, v240, v181, v182\n\t"
    "v_max3_f32 v241, v241, v189, v190\n\t"
    "s_waitcnt lgkmcnt(8)\n\t"
    "v_mfma_f32_16x16x32_bf16 v[24:27], v[212:215], v[144:147], 0\n\t"
    "ds_read_b64_tr_b16 v[232:233], v250 offset:1536\n\t"
    "ds_read_b64_tr_b16 v[234:235], v250 offset:5632\n\t"
    "v_mfma_f32_16x16x32_bf16 v[28:31], v[212:215], v[152:155], 0\n\t"
    "v_max3_f32 v239, v239, v175, v183\n\t"
    "v_max3_f32 v240, v240, v241, v191\n\t"
    "v_mfma_f32_16x16x32_bf16 v[32:35], v[212:215], v[160:163], 0\n\t"
    "v_max_f32 v239, v239, v240\n\t"
    "v_cmp_lt_f32 vcc, s79, v239\n\t"
    "s_cbranch_vccnz L_rap1_%=\n\t"
    "L_rarp1_%=:\n\t"
    "s_waitcnt lgkmcnt(8)\n\t"
    "v_mfma_f32_16x16x32_bf16 v[36:39], v[216:219], v[144:147], 0\n\t"
    "ds_read_b128 v[204:207], v247 offset:8192\n\t"
    "s_add_u32 s88, s88, 16384\n\t"
    "s_cmp_eq_u32 s88, 98304\n\t"
    "s_cselect_b32 s88, 0, s88\n\t"
    "v_add_u32 v249, s88, v246\n\t"
    "v_mfma_f32_16x16x32_bf16 v[40:43], v[216:219], v[152:155], 0\n\t"
    "v_xor_b32 v250, 32, v249\n\t"
    "v_exp_f32 v168, v168\n\t"
    "v_exp_f32 v169, v169\n\t"
    "v_mfma_f32_16x16x32_bf16 v[44:47], v[216:219], v[160:163], 0\n\t"
    "v_add_f32 v236, v236, v168\n\t"
    "v_exp_f32 v170, v170\n\t"
    "v_add_f32 v236, v236, v169\n\t"
    "s_waitcnt lgkmcnt(7)\n\t"
    "v_mfma_f32_16x16x32_bf16 v[48:51], v[220:223], v[144:147], 0\n\t"
    "ds_read_b128 v[208:211], v248 offset:8192\n\t"
    "v_cvt_pk_bf16_f32 v168, v168, v169\n\t"
    "v_mfma_f32_16x16x32_bf16 v[52:55], v[220:223], v[152:155], 0\n\t"
    "v_exp_f32 v171, v171\n\t"
    "v_add_f32 v236, v236, v170\n\t"
    "v_mfma_f32_16x16x32_bf16 v[56:59], v[220:223], v[160:163], 0\n\t"
    "v_exp_f32 v172, v172\n\t"
    "v_add_f32 v236, v236, v171\n\t"
    "s_waitcnt lgkmcnt(6)\n\t"
    "v_mfma_f32_16x16x32_bf16 v[60:63], v[224:227], v[144:147], 0\n\t"
    "ds_read_b128 v[212:215], v247 offset:9216\n\t"
    "v_cvt_pk_bf16_f32 v169, v170, v171\n\t"
    "v_mfma_f32_16x16x32_bf16 v[64:67], v[224:227], v[152:155], 0\n\t"
    "v_exp_f32 v173, v173\n\t"
    "v_add_f32 v236, v236, v172\n\t"
    "v_mfma_f32_16x16x32_bf16 v[68:71], v[224:227], v[160:163], 0\n\t"
    "v_exp_f32 v174, v174\n\t"
    "s_waitcnt lgkmcnt(5)\n\t"
    "v_mfma_f32_16x16x32_bf16 v[72:75], v[228:231], v[144:147], 0\n\t"
    "ds_read_b128 v[216:219], v248 offset:9216\n\t"
    "v_add_f32 v236, v236, v173\n\t"
    "v_mfma_f32_16x16x32_bf16 v[76:79], v[228:231], v[152:155], 0\n\t"
    "v_cvt_pk_bf16_f32 v170, v172, v173\n\t"
    "v_exp_f32 v175, v175\n\t"
    "v_mfma_f32_16x16x32_bf16 v[80:83], v[228:231], v[160:163], 0\n\t"
    "v_add_f32 v236, v236, v174\n\t"
    "v_exp_f32 v176, v176\n\t"
    "s_waitcnt lgkmcnt(4)\n\t"
    "v_mfma_f32_16x16x32_bf16 v[84:87], v[232:235], v[144:147], 0\n\t"
    "ds_read_b128 v[220:223], v247 offset:12288\n\t"
    "v_add_f32 v236, v236, v175\n\t"
    "v_cvt_pk_bf16_f32 v171, v174, v175\n\t"
    "v_mfma_f32_16x16x32_bf16 v[88:91], v[232:235], v[152:155], 0\n\t"
    "v_exp_f32 v177, v177\n\t"
    "v_add_f32 v237, v237, v176\n\t"
    "v_mfma_f32_16x16x32_bf16 v[92:95], v[232:235], v[160:163], 0\n\t"
    "v_exp_f32 v178, v178\n\t"
    "s_cmp_lg_u32 s78, 0\n\t"
    "s_cbranch_scc1 L_rbp1_%=\n\t"
    "L_rbrp1_%=:\n\t"
    "s_waitcnt lgkmcnt(4)\n\t"
    "v_mfma_f32_16x16x32_bf16 v[144:147], v[204:207], v[96:99], v[192:195]\n\t"
    "ds_read_b128 v[224:227], v248 offset:12288\n\t"
    "v_add_f32 v237, v237, v177\n\t"
    "v_mfma_f32_16x16x32_bf16 v[152:155], v[204:207], v[112:115], v[196:199]\n\t"
    "v_cvt_pk_bf16_f32 v176, v176, v177\n\t"
    "v_exp_f32 v179, v179\n\t"
    "v_mfma_f32_16x16x32_bf16 v[160:163], v[204:207], v[128:131], v[200:203]\n\t"
    "v_add_f32 v237, v237, v178\n\t"
    "s_waitcnt lgkmcnt(4)\n\t"
    "v_mfma_f32_16x16x32_bf16 v[144:147], v[208:211], v[100:103], v[144:147]\n\t"
    "ds_read_b128 v[228:231], v247 offset:13312\n\t"
    "v_exp_f32 v180, v180\n\t"
    "v_mfma_f32_16x16x32_bf16 v[152:155], v[208:211], v[116:119], v[152:155]\n\t"
    "v_add_f32 v237, v237, v179\n\t"
    "v_cvt_pk_bf16_f32 v177, v178, v179\n\t"
    "v_mfma_f32_16x16x32_bf16 v[160:163], v[208:211], v[132:135], v[160:163]\n\t"
    "v_exp_f32 v181, v181\n\t"
    "v_add_f32 v237, v237, v180\n\t"
    "s_waitcnt lgkmcnt(4)\n\t"
    "v_mfma_f32_16x16x32_bf16 v[144:147], v[212:215], v[104:107], v[144:147]\n\t"
    "ds_read_b128 v[232:235], v248 offset:13312\n\t"
    "v_exp_f32 v182, v182\n\t"
    "v_mfma_f32_16x16x32_bf16 v[152:155], v[212:215], v[120:123], v[152:155]\n\t"
    "v_add_f32 v237, v237, v181\n\t"
    "v_cvt_pk_bf16_f32 v178, v180, v181\n\t"
    "v_mfma_f32_16x16x32_bf16 v[160:163], v[212:215], v[136:139], v[160:163]\n\t"
    "v_exp_f32 v183, v183\n\t"
    "s_waitcnt lgkmcnt(4)\n\t"
    "v_mfma_f32_16x16x32_bf16 v[144:147], v[216:219], v[108:111], v[144:147]\n\t"
    "v_add_u32 v247, s88, v245\n\t"
    "v_xor_b32 v248, 64, v247\n\t"
    "v_add_f32 v237, v237, v182\n\t"
    "v_mfma_f32_16x16x32_bf16 v[152:155], v[216:219], v[124:127], v[152:155]\n\t"
    "v_exp_f32 v184, v184\n\t"
    "v_add_f32 v237, v237, v183\n\t"
    "v_mfma_f32_16x16x32_bf16 v[160:163], v[216:219], v[140:143], v[160:163]\n\t"
    "v_cvt_pk_bf16_f32 v179, v182, v183\n\t"
    "v_exp_f32 v185, v185\n\t"
    "s_waitcnt lgkmcnt(3)\n\t"
    "v_mfma_f32_16x16x32_bf16 v[148:151], v[220:223], v[96:99], v[192:195]\n\t"
    "s_waitcnt lgkmcnt(0)\n\t"
    "s_barrier\n\t"
    "v_add_f32 v238, v238, v184\n\t"
    "v_mfma_f32_16x16x32_bf16 v[156:159], v[220:223], v[112:115], v[196:199]\n\t"
    "v_exp_f32 v186, v186\n\t"
    "v_add_f32 v238, v238, v185\n\t"
    "v_mfma_f32_16x16x32_bf16 v[164:167], v[220:223], v[128:131], v[200:203]\n\t"
    "v_cvt_pk_bf16_f32 v184, v184, v185\n\t"
    "v_exp_f32 v187, v187\n\t"
    "v_mfma_f32_16x16x32_bf16 v[148:151], v[224:227], v[100:103], v[148:151]\n\t"
    "ds_read_b64_tr_b16 v[204:205], v249 offset:0\n\t"
    "ds_read_b64_tr_b16 v[206:207], v249 offset:4096\n\t"
    "ds_read_b64_tr_b16 v[208:209], v250 offset:0\n\t"
    "v_mfma_f32_16x16x32_bf16 v[156:159], v[224:227], v[116:119], v[156:159]\n\t"
    "ds_read_b64_tr_b16 v[210:211], v250 offset:4096\n\t"
    "v_add_f32 v238, v238, v186\n\t"
    "v_exp_f32 v188, v188\n\t"
    "v_mfma_f32_16x16x32_bf16 v[164:167], v[224:227], v[132:135], v[164:167]\n\t"
    "v_add_f32 v238, v238, v187\n\t"
    "v_cvt_pk_bf16_f32 v185, v186, v187\n\t"
    "v_mfma_f32_16x16x32_bf16 v[148:151], v[228:231], v[104:107], v[148:151]\n\t"
    "ds_read_b64_tr_b16 v[212:213], v249 offset:512\n\t"
    "ds_read_b64_tr_b16 v[214:215], v249 offset:4608\n\t"
    "ds_read_b64_tr_b16 v[216:217], v250 offset:512\n\t"
    "v_mfma_f32_16x16x32_bf16 v[156:159], v[228:231], v[120:123], v[156:159]\n\t"
    "ds_read_b64_tr_b16 v[218:219], v250 offset:4608\n\t"
    "v_exp_f32 v189, v189\n\t"
    "v_add_f32 v238, v238, v188\n\t"
    "v_mfma_f32_16x16x32_bf16 v[164:167], v[228:231], v[136:139], v[164:167]\n\t"
    "v_exp_f32 v190, v190\n\t"
    "v_add_f32 v238, v238, v189\n\t"
    "v_cvt_pk_bf16_f32 v186, v188, v189\n\t"
    "v_mfma_f32_16x16x32_bf16 v[148:151], v[232:235], v[108:111], v[148:151]\n\t"
    "ds_read_b64_tr_b16 v[220:221], v249 offset:1024\n\t"
    "ds_read_b64_tr_b16 v[222:223], v249 offset:5120\n\t"
    "v_mfma_f32_16x16x32_bf16 v[156:159], v[232:235], v[124:127], v[156:159]\n\t"
    "v_exp_f32 v191, v191\n\t"
    "v_add_f32 v238, v238, v190\n\t"
    "v_mfma_f32_16x16x32_bf16 v[164:167], v[232:235], v[140:143], v[164:167]\n\t"
    "v_add_f32 v238, v238, v191\n\t"
    "v_cvt_pk_bf16_f32 v187, v190, v191\n\t"
    "s_waitcnt lgkmcnt(8)\n\t"
    "v_mfma_f32_16x16x32_bf16 v[0:3], v[204:207], v[168:171], v[0:3]\n\t"
    "ds_read_b64_tr_b16 v[224:225], v250 offset:1024\n\t"
    "ds_read_b64_tr_b16 v[226:227], v250 offset:5120\n\t"
    "v_max3_f32 v239, v144, v145, v146\n\t"
    "v_mfma_f32_16x16x32_bf16 v[4:7], v[204:207], v[176:179], v[4:7]\n\t"
    "v_max3_f32 v240, v152, v153, v154\n\t"
    "v_max3_f32 v241, v160, v161, v162\n\t"
    "v_mfma_f32_16x16x32_bf16 v[8:11], v[204:207], v[184:187], v[8:11]\n\t"
    "v_max3_f32 v239, v239, v147, v148\n\t"
    "v_max3_f32 v240, v240, v155, v156\n\t"
    "s_waitcnt lgkmcnt(8)\n\t"
    "v_mfma_f32_16x16x32_bf16 v[12:15], v[208:211], v[168:171], v[12:15]\n\t"
    "ds_read_b64_tr_b16 v[228:229], v249 offset:1536\n\t"
    "ds_read_b64_tr_b16 v[230:231], v249 offset:5632\n\t"
    "v_mfma_f32_16x16x32_bf16 v[16:19], v[208:211], v[176:179], v[16:19]\n\t"
    "v_max3_f32 v241, v241, v163, v164\n\t"
    "v_max3_f32 v239, v239, v149, v150\n\t"
    "v_mfma_f32_16x16x32_bf16 v[20:23], v[208:211], v[184:187], v[20:23]\n\t"
    "v_max3_f32 v240, v240, v157, v158\n\t"
    "v_max3_f32 v241, v241, v165, v166\n\t"
    "s_waitcnt lgkmcnt(8)\n\t"
    "v_mfma_f32_16x16x32_bf16 v[24:27], v[212:215], v[168:171], v[24:27]\n\t"
    "ds_read_b64_tr_b16 v[232:233], v250 offset:1536\n\t"
    "ds_read_b64_tr_b16 v[234:235], v250 offset:5632\n\t"
    "v_mfma_f32_16x16x32_bf16 v[28:31], v[212:215], v[176:179], v[28:31]\n\t"
    "v_max3_f32 v239, v239, v151, v159\n\t"
    "v_max3_f32 v240, v240, v241, v167\n\t"
    "v_mfma_f32_16x16x32_bf16 v[32:35], v[212:215], v[184:187], v[32:35]\n\t"
    "v_max_f32 v239, v239, v240\n\t"
    "v_cmp_lt_f32 vcc, s79, v239\n\t"
    "s_cbranch_vccnz L_rap2_%=\n\t"
    "L_rarp2_%=:\n\t"
    "s_waitcnt lgkmcnt(8)\n\t"
    "v_mfma_f32_16x16x32_bf16 v[36:39], v[216:219], v[168:171], v[36:39]\n\t"
    "ds_read_b128 v[204:207], v247 offset:8192\n\t"
    "s_add_u32 s88, s88, 16384\n\t"
    "s_cmp_eq_u32 s88, 98304\n\t"
    "s_cselect_b32 s88, 0, s88\n\t"
    "v_add_u32 v249, s88, v246\n\t"
    "v_mfma_f32_16x16x32_bf16 v[40:43], v[216:219], v[176:179], v[40:43]\n\t"
    "v_xor_b32 v250, 32, v249\n\t"
    "v_exp_f32 v144, v144\n\t"
    "v_exp_f32 v145, v145\n\t"
    "v_mfma_f32_16x16x32_bf16 v[44:47], v[216:219], v[184:187], v[44:47]\n\t"
    "v_add_f32 v236, v236, v144\n\t"
    "v_exp_f32 v146, v146\n\t"
    "v_add_f32 v236, v236, v145\n\t"
    "s_waitcnt lgkmcnt(7)\n\t"
    "v_mfma_f32_16x16x32_bf16 v[48:51], v[220:223], v[168:171], v[48:51]\n\t"
    "ds_read_b128 v[208:211], v248 offset:8192\n\t"
    "v_cvt_pk_bf16_f32 v144, v144, v145\n\t"
    "v_mfma_f32_16x16x32_bf16 v[52:55], v[220:223], v[176:179], v[52:55]\n\t"
    "v_exp_f32 v147, v147\n\t"
    "v_add_f32 v236, v236, v146\n\t"
    "v_mfma_f32_16x16x32_bf16 v[56:59], v[220:223], v[184:187], v[56:59]\n\t"
    "v_exp_f32 v148, v148\n\t"
    "v_add_f32 v236, v236, v147\n\t"
    "s_waitcnt lgkmcnt(6)\n\t"
    "v_mfma_f32_16x16x32_bf16 v[60:63], v[224:227], v[168:171], v[60:63]\n\t"
    "ds_read_b128 v[212:215], v247 offset:9216\n\t"
    "v_cvt_pk_bf16_f32 v145, v146, v147\n\t"
    "v_mfma_f32_16x16x32_bf16 v[64:67], v[224:227], v[176:179], v[64:67]\n\t"
    "v_exp_f32 v149, v149\n\t"
    "v_add_f32 v236, v236, v148\n\t"
    "v_mfma_f32_16x16x32_bf16 v[68:71], v[224:227], v[184:187], v[68:71]\n\t"
    "v_exp_f32 v150, v150\n\t"
    "s_waitcnt lgkmcnt(5)\n\t"
    "v_mfma_f32_16x16x32_bf16 v[72:75], v[228:231], v[168:171], v[72:75]\n\t"
    "ds_read_b128 v[216:219], v248 offset:9216\n\t"
    "v_add_f32 v236, v236, v149\n\t"
    "v_mfma_f32_16x16x32_bf16 v[76:79], v[228:231], v[176:179], v[76:79]\n\t"
    "v_cvt_pk_bf16_f32 v146, v148, v149\n\t"
    "v_exp_f32 v151, v151\n\t"
    "v_mfma_f32_16x16x32_bf16 v[80:83], v[228:231], v[184:187], v[80:83]\n\t"
    "v_add_f32 v236, v236, v150\n\t"
    "v_exp_f32 v152, v152\n\t"
    "s_waitcnt lgkmcnt(4)\n\t"
    "v_mfma_f32_16x16x32_bf16 v[84:87], v[232:235], v[168:171], v[84:87]\n\t"
    "ds_read_b128 v[220:223], v247 offset:12288\n\t"
    "v_add_f32 v236, v236, v151\n\t"
    "v_cvt_pk_bf16_f32 v147, v150, v151\n\t"
    "v_mfma_f32_16x16x32_bf16 v[88:91], v[232:235], v[176:179], v[88:91]\n\t"
    "v_exp_f32 v153, v153\n\t"
    "v_add_f32 v237, v237, v152\n\t"
    "v_mfma_f32_16x16x32_bf16 v[92:95], v[232:235], v[184:187], v[92:95]\n\t"
    "v_exp_f32 v154, v154\n\t"
    "s_cmp_lg_u32 s78, 0\n\t"
    "s_cbranch_scc1 L_rbp2_%=\n\t"
    "L_rbrp2_%=:\n\t"
    "s_waitcnt lgkmcnt(4)\n\t"
    "v_mfma_f32_16x16x32_bf16 v[168:171], v[204:207], v[96:99], v[192:195]\n\t"
    "ds_read_b128 v[224:227], v248 offset:12288\n\t"
    "v_add_f32 v237, v237, v153\n\t"
    "v_mfma_f32_16x16x32_bf16 v[176:179], v[204:207], v[112:115], v[196:199]\n\t"
    "v_cvt_pk_bf16_f32 v152, v152, v153\n\t"
    "v_exp_f32 v155, v155\n\t"
    "v_mfma_f32_16x16x32_bf16 v[184:187], v[204:207], v[128:131], v[200:203]\n\t"
    "v_add_f32 v237, v237, v154\n\t"
    "s_waitcnt lgkmcnt(4)\n\t"
    "v_mfma_f32_16x16x32_bf16 v[168:171], v[208:211], v[100:103], v[168:171]\n\t"
    "ds_read_b128 v[228:231], v247 offset:13312\n\t"
    "v_exp_f32 v156, v156\n\t"
    "v_mfma_f32_16x16x32_bf16 v[176:179], v[208:211], v[116:119], v[176:179]\n\t"
    "v_add_f32 v237, v237, v155\n\t"
    "v_cvt_pk_bf16_f32 v153, v154, v155\n\t"
    "v_mfma_f32_16x16x32_bf16 v[184:187], v[208:211], v[132:135], v[184:187]\n\t"
    "v_exp_f32 v157, v157\n\t"
    "v_add_f32 v237, v237, v156\n\t"
    "s_waitcnt lgkmcnt(4)\n\t"
    "v_mfma_f32_16x16x32_bf16 v[168:171], v[212:215], v[104:107], v[168:171]\n\t"
    "ds_read_b128 v[232:235], v248 offset:13312\n\t"
    "v_exp_f32 v158, v158\n\t"
    "v_mfma_f32_16x16x32_bf16 v[176:179], v[212:215], v[120:123], v[176:179]\n\t"
    "v_add_f32 v237, v237, v157\n\t"
    "v_cvt_pk_bf16_f32 v154, v156, v157\n\t"
    "v_mfma_f32_16x16x32_bf16 v[184:187], v[212:215], v[136:139], v[184:187]\n\t"
    "v_exp_f32 v159, v159\n\t"
    "s_waitcnt lgkmcnt(4)\n\t"
    "v_mfma_f32_16x16x32_bf16 v[168:171], v[216:219], v[108:111], v[168:171]\n\t"
    "v_add_u32 v247, s88, v245\n\t"
    "v_xor_b32 v248, 64, v247\n\t"
    "v_add_f32 v237, v237, v158\n\t"
    "v_mfma_f32_16x16x32_bf16 v[176:179], v[216:219], v[124:127], v[176:179]\n\t"
    "v_exp_f32 v160, v160\n\t"
    "v_add_f32 v237, v237, v159\n\t"
    "v_mfma_f32_16x16x32_bf16 v[184:187], v[216:219], v[140:143], v[184:187]\n\t"
    "v_cvt_pk_bf16_f32 v155, v158, v159\n\t"
    "v_exp_f32 v161, v161\n\t"
    "s_waitcnt lgkmcnt(3)\n\t"
    "v_mfma_f32_16x16x32_bf16 v[172:175], v[220:223], v[96:99], v[192:195]\n\t"
    "s_waitcnt lgkmcnt(0)\n\t"
    "s_barrier\n\t"
    "v_add_f32 v238, v238, v160\n\t"
    "v_mfma_f32_16x16x32_bf16 v[180:183], v[220:223], v[112:115], v[196:199]\n\t"
    "v_exp_f32 v162, v162\n\t"
    "v_add_f32 v238, v238, v161\n\t"
    "v_mfma_f32_16x16x32_bf16 v[188:191], v[220:223], v[128:131], v[200:203]\n\t"
    "v_cvt_pk_bf16_f32 v160, v160, v161\n\t"
    "v_exp_f32 v163, v163\n\t"
    "v_mfma_f32_16x16x32_bf16 v[172:175], v[224:227], v[100:103], v[172:175]\n\t"
    "ds_read_b64_tr_b16 v[204:205], v249 offset:0\n\t"
    "ds_read_b64_tr_b16 v[206:207], v249 offset:4096\n\t"
    "ds_read_b64_tr_b16 v[208:209], v250 offset:0\n\t"
    "v_mfma_f32_16x16x32_bf16 v[180:183], v[224:227], v[116:119], v[180:183]\n\t"
    "ds_read_b64_tr_b16 v[210:211], v250 offset:4096\n\t"
    "v_add_f32 v238, v238, v162\n\t"
    "v_exp_f32 v164, v164\n\t"
    "v_mfma_f32_16x16x32_bf16 v[188:191], v[224:227], v[132:135], v[188:191]\n\t"
    "v_add_f32 v238, v238, v163\n\t"
    "v_cvt_pk_bf16_f32 v161, v162, v163\n\t"
    "v_mfma_f32_16x16x32_bf16 v[172:175], v[228:231], v[104:107], v[172:175]\n\t"
    "ds_read_b64_tr_b16 v[212:213], v249 offset:512\n\t"
    "ds_read_b64_tr_b16 v[214:215], v249 offset:4608\n\t"
    "ds_read_b64_tr_b16 v[216:217], v250 offset:512\n\t"
    "v_mfma_f32_16x16x32_bf16 v[180:183], v[228:231], v[120:123], v[180:183]\n\t"
    "ds_read_b64_tr_b16 v[218:219], v250 offset:4608\n\t"
    "v_exp_f32 v165, v165\n\t"
    "v_add_f32 v238, v238, v164\n\t"
    "v_mfma_f32_16x16x32_bf16 v[188:191], v[228:231], v[136:139], v[188:191]\n\t"
    "v_exp_f32 v166, v166\n\t"
    "v_add_f32 v238, v238, v165\n\t"
    "v_cvt_pk_bf16_f32 v162, v164, v165\n\t"
    "v_mfma_f32_16x16x32_bf16 v[172:175], v[232:235], v[108:111], v[172:175]\n\t"
    "ds_read_b64_tr_b16 v[220:221], v249 offset:1024\n\t"
    "ds_read_b64_tr_b16 v[222:223], v249 offset:5120\n\t"
    "v_mfma_f32_16x16x32_bf16 v[180:183], v[232:235], v[124:127], v[180:183]\n\t"
    "v_exp_f32 v167, v167\n\t"
    "v_add_f32 v238, v238, v166\n\t"
    "v_mfma_f32_16x16x32_bf16 v[188:191], v[232:235], v[140:143], v[188:191]\n\t"
    "v_add_f32 v238, v238, v167\n\t"
    "v_cvt_pk_bf16_f32 v163, v166, v167\n\t"
    "s_waitcnt vmcnt(0)\n\t"
    "s_mov_b32 s77, 6\n\t"
    "L_at_%=:\n\t"
    "s_waitcnt lgkmcnt(8)\n\t"
    "v_mfma_f32_16x16x32_bf16 v[0:3], v[204:207], v[144:147], v[0:3]\n\t"
    "ds_read_b64_tr_b16 v[224:225], v250 offset:1024\n\t"
    "ds_read_b64_tr_b16 v[226:227], v250 offset:5120\n\t"
    "v_max3_f32 v239, v168, v169, v170\n\t"
    "v_mfma_f32_16x16x32_bf16 v[4:7], v[204:207], v[152:155], v[4:7]\n\t"
    "v_max3_f32 v240, v176, v177, v178\n\t"
    "v_max3_f32 v241, v184, v185, v186\n\t"
    "v_mfma_f32_16x16x32_bf16 v[8:11], v[204:207], v[160:163], v[8:11]\n\t"
    "v_max3_f32 v239, v239, v171, v172\n\t"
    "v_max3_f32 v240, v240, v179, v180\n\t"
    "s_waitcnt lgkmcnt(8)\n\t"
    "v_mfma_f32_16x16x32_bf16 v[12:15], v[208:211], v[144:147], v[12:15]\n\t"
    "ds_read_b64_tr_b16 v[228:229], v249 offset:1536\n\t"
    "ds_read_b64_tr_b16 v[230:231], v249 offset:5632\n\t"
    "v_mfma_f32_16x16x32_bf16 v[16:19], v[208:211], v[152:155], v[16:19]\n\t"
    "v_max3_f32 v241, v241, v187, v188\n\t"
    "v_max3_f32 v239, v239, v173, v174\n\t"
    "v_mfma_f32_16x16x32_bf16 v[20:23], v[208:211], v[160:163], v[20:23]\n\t"
    "v_max3_f32 v240, v240, v181, v182\n\t"
    "v_max3_f32 v241, v241, v189, v190\n\t"
    "s_waitcnt lgkmcnt(8)\n\t"
    "v_mfma_f32_16x16x32_bf16 v[24:27], v[212:215], v[144:147], v[24:27]\n\t"
    "ds_read_b64_tr_b16 v[232:233], v250 offset:1536\n\t"
    "ds_read_b64_tr_b16 v[234:235], v250 offset:5632\n\t"
    "v_mfma_f32_16x16x32_bf16 v[28:31], v[212:215], v[152:155], v[28:31]\n\t"
    "v_max3_f32 v239, v239, v175, v183\n\t"
    "v_max3_f32 v240, v240, v241, v191\n\t"
    "v_mfma_f32_16x16x32_bf16 v[32:35], v[212:215], v[160:163], v[32:35]\n\t"
    "v_max_f32 v239, v239, v240\n\t"
    "v_cmp_lt_f32 vcc, s79, v239\n\t"
    "s_cbranch_vccnz L_ralo_%=\n\t"
    "L_rarlo_%=:\n\t"
    "s_waitcnt lgkmcnt(8)\n\t"
    "v_mfma_f32_16x16x32_bf16 v[36:39], v[216:219], v[144:147], v[36:39]\n\t"
    "ds_read_b128 v[204:207], v247 offset:8192\n\t"
    "s_add_u32 s88, s88, 16384\n\t"
    "s_cmp_eq_u32 s88, 98304\n\t"
    "s_cselect_b32 s88, 0, s88\n\t"
    "v_add_u32 v249, s88, v246\n\t"
    "v_mfma_f32_16x16x32_bf16 v[40:43], v[216:219], v[152:155], v[40:43]\n\t"
    "v_xor_b32 v250, 32, v249\n\t"
    "v_exp_f32 v168, v168\n\t"
    "v_exp_f32 v169, v169\n\t"
    "v_mfma_f32_16x16x32_bf16 v[44:47], v[216:219], v[160:163], v[44:47]\n\t"
    "v_add_f32 v236, v236, v168\n\t"
    "v_exp_f32 v170, v170\n\t"
    "v_add_f32 v236, v236, v169\n\t"
    "s_waitcnt lgkmcnt(7)\n\t"
    "v_mfma_f32_16x16x32_bf16 v[48:51], v[220:223], v[144:147], v[48:51]\n\t"
    "ds_read_b128 v[208:211], v248 offset:8192\n\t"
    "v_cvt_pk_bf16_f32 v168, v168, v169\n\t"
    "v_mfma_f32_16x16x32_bf16 v[52:55], v[220:223], v[152:155], v[52:55]\n\t"
    "v_exp_f32 v171, v171\n\t"
    "v_add_f32 v236, v236, v170\n\t"
    "v_mfma_f32_16x16x32_bf16 v[56:59], v[220:223], v[160:163], v[56:59]\n\t"
    "v_exp_f32 v172, v172\n\t"
    "v_add_f32 v236, v236, v171\n\t"
    "s_waitcnt lgkmcnt(6)\n\t"
    "v_mfma_f32_16x16x32_bf16 v[60:63], v[224:227], v[144:147], v[60:63]\n\t"
    "ds_read_b128 v[212:215], v247 offset:9216\n\t"
    "v_cvt_pk_bf16_f32 v169, v170, v171\n\t"
    "v_mfma_f32_16x16x32_bf16 v[64:67], v[224:227], v[152:155], v[64:67]\n\t"
    "v_exp_f32 v173, v173\n\t"
    "v_add_f32 v236, v236, v172\n\t"
    "v_mfma_f32_16x16x32_bf16 v[68:71], v[224:227], v[160:163], v[68:71]\n\t"
    "v_exp_f32 v174, v174\n\t"
    "s_waitcnt lgkmcnt(5)\n\t"
    "v_mfma_f32_16x16x32_bf16 v[72:75], v[228:231], v[144:147], v[72:75]\n\t"
    "ds_read_b128 v[216:219], v248 offset:9216\n\t"
    "v_add_f32 v236, v236, v173\n\t"
    "v_mfma_f32_16x16x32_bf16 v[76:79], v[228:231], v[152:155], v[76:79]\n\t"
    "v_cvt_pk_bf16_f32 v170, v172, v173\n\t"
    "v_exp_f32 v175, v175\n\t"
    "v_mfma_f32_16x16x32_bf16 v[80:83], v[228:231], v[160:163], v[80:83]\n\t"
    "v_add_f32 v236, v236, v174\n\t"
    "v_exp_f32 v176, v176\n\t"
    "s_waitcnt lgkmcnt(4)\n\t"
    "v_mfma_f32_16x16x32_bf16 v[84:87], v[232:235], v[144:147], v[84:87]\n\t"
    "ds_read_b128 v[220:223], v247 offset:12288\n\t"
    "v_add_f32 v236, v236, v175\n\t"
    "v_cvt_pk_bf16_f32 v171, v174, v175\n\t"
    "v_mfma_f32_16x16x32_bf16 v[88:91], v[232:235], v[152:155], v[88:91]\n\t"
    "v_exp_f32 v177, v177\n\t"
    "v_add_f32 v237, v237, v176\n\t"
    "v_mfma_f32_16x16x32_bf16 v[92:95], v[232:235], v[160:163], v[92:95]\n\t"
    "v_exp_f32 v178, v178\n\t"
    "s_cmp_lg_u32 s78, 0\n\t"
    "s_cbranch_scc1 L_rblo_%=\n\t"
    "L_rbrlo_%=:\n\t"
    "s_waitcnt lgkmcnt(4)\n\t"
    "v_mfma_f32_16x16x32_bf16 v[144:147], v[204:207], v[96:99], v[192:195]\n\t"
    "ds_read_b128 v[224:227], v248 offset:12288\n\t"
    "v_add_f32 v237, v237, v177\n\t"
    "v_mfma_f32_16x16x32_bf16 v[152:155], v[204:207], v[112:115], v[196:199]\n\t"
    "v_cvt_pk_bf16_f32 v176, v176, v177\n\t"
    "v_exp_f32 v179, v179\n\t"
    "v_mfma_f32_16x16x32_bf16 v[160:163], v[204:207], v[128:131], v[200:203]\n\t"
    "v_add_f32 v237, v237, v178\n\t"
    "s_waitcnt lgkmcnt(4)\n\t"
    "v_mfma_f32_16x16x32_bf16 v[144:147], v[208:211], v[100:103], v[144:147]\n\t"
    "ds_read_b128 v[228:231], v247 offset:13312\n\t"
    "v_exp_f32 v180, v180\n\t"
    "v_mfma_f32_16x16x32_bf16 v[152:155], v[208:211], v[116:119], v[152:155]\n\t"
    "v_add_f32 v237, v237, v179\n\t"
    "v_cvt_pk_bf16_f32 v177, v178, v179\n\t"
    "v_mfma_f32_16x16x32_bf16 v[160:163], v[208:211], v[132:135], v[160:163]\n\t"
    "v_exp_f32 v181, v181\n\t"
    "v_add_f32 v237, v237, v180\n\t"
    "s_waitcnt lgkmcnt(4)\n\t"
    "v_mfma_f32_16x16x32_bf16 v[144:147], v[212:215], v[104:107], v[144:147]\n\t"
    "ds_read_b128 v[232:235], v248 offset:13312\n\t"
    "v_exp_f32 v182, v182\n\t"
    "v_mfma_f32_16x16x32_bf16 v[152:155], v[212:215], v[120:123], v[152:155]\n\t"
    "v_add_f32 v237, v237, v181\n\t"
    "v_cvt_pk_bf16_f32 v178, v180, v181\n\t"
    "v_mfma_f32_16x16x32_bf16 v[160:163], v[212:215], v[136:139], v[160:163]\n\t"
    "v_exp_f32 v183, v183\n\t"
    "s_waitcnt lgkmcnt(4)\n\t"
    "v_mfma_f32_16x16x32_bf16 v[144:147], v[216:219], v[108:111], v[144:147]\n\t"
    "v_add_u32 v247, s88, v245\n\t"
    "v_xor_b32 v248, 64, v247\n\t"
    "v_add_f32 v237, v237, v182\n\t"
    "v_mfma_f32_16x16x32_bf16 v[152:155], v[216:219], v[124:127], v[152:155]\n\t"
    "v_exp_f32 v184, v184\n\t"
    "v_add_f32 v237, v237, v183\n\t"
    "v_mfma_f32_16x16x32_bf16 v[160:163], v[216:219], v[140:143], v[160:163]\n\t"
    "v_cvt_pk_bf16_f32 v179, v182, v183\n\t"
    "v_exp_f32 v185, v185\n\t"
    "s_waitcnt lgkmcnt(3)\n\t"
    "v_mfma_f32_16x16x32_bf16 v[148:151], v[220:223], v[96:99], v[192:195]\n\t"
    "s_waitcnt lgkmcnt(0)\n\t"
    "s_barrier\n\t"
    "v_add_f32 v238, v238, v184\n\t"
    "v_mfma_f32_16x16x32_bf16 v[156:159], v[220:223], v[112:115], v[196:199]\n\t"
    "v_exp_f32 v186, v186\n\t"
    "v_add_f32 v238, v238, v185\n\t"
    "v_mfma_f32_16x16x32_bf16 v[164:167], v[220:223], v[128:131], v[200:203]\n\t"
    "v_cvt_pk_bf16_f32 v184, v184, v185\n\t"
    "v_exp_f32 v187, v187\n\t"
    "v_mfma_f32_16x16x32_bf16 v[148:151], v[224:227], v[100:103], v[148:151]\n\t"
    "ds_read_b64_tr_b16 v[204:205], v249 offset:0\n\t"
    "ds_read_b64_tr_b16 v[206:207], v249 offset:4096\n\t"
    "ds_read_b64_tr_b16 v[208:209], v250 offset:0\n\t"
    "v_mfma_f32_16x16x32_bf16 v[156:159], v[224:227], v[116:119], v[156:159]\n\t"
    "ds_read_b64_tr_b16 v[210:211], v250 offset:4096\n\t"
    "v_add_f32 v238, v238, v186\n\t"
    "v_exp_f32 v188, v188\n\t"
    "v_mfma_f32_16x16x32_bf16 v[164:167], v[224:227], v[132:135], v[164:167]\n\t"
    "v_add_f32 v238, v238, v187\n\t"
    "v_cvt_pk_bf16_f32 v185, v186, v187\n\t"
    "v_mfma_f32_16x16x32_bf16 v[148:151], v[228:231], v[104:107], v[148:151]\n\t"
    "ds_read_b64_tr_b16 v[212:213], v249 offset:512\n\t"
    "ds_read_b64_tr_b16 v[214:215], v249 offset:4608\n\t"
    "ds_read_b64_tr_b16 v[216:217], v250 offset:512\n\t"
    "v_mfma_f32_16x16x32_bf16 v[156:159], v[228:231], v[120:123], v[156:159]\n\t"
    "ds_read_b64_tr_b16 v[218:219], v250 offset:4608\n\t"
    "v_exp_f32 v189, v189\n\t"
    "v_add_f32 v238, v238, v188\n\t"
    "v_mfma_f32_16x16x32_bf16 v[164:167], v[228:231], v[136:139], v[164:167]\n\t"
    "v_exp_f32 v190, v190\n\t"
    "v_add_f32 v238, v238, v189\n\t"
    "v_cvt_pk_bf16_f32 v186, v188, v189\n\t"
    "v_mfma_f32_16x16x32_bf16 v[148:151], v[232:235], v[108:111], v[148:151]\n\t"
    "ds_read_b64_tr_b16 v[220:221], v249 offset:1024\n\t"
    "ds_read_b64_tr_b16 v[222:223], v249 offset:5120\n\t"
    "v_mfma_f32_16x16x32_bf16 v[156:159], v[232:235], v[124:127], v[156:159]\n\t"
    "v_exp_f32 v191, v191\n\t"
    "v_add_f32 v238, v238, v190\n\t"
    "v_mfma_f32_16x16x32_bf16 v[164:167], v[232:235], v[140:143], v[164:167]\n\t"
    "v_add_f32 v238, v238, v191\n\t"
    "v_cvt_pk_bf16_f32 v187, v190, v191\n\t"
    "s_waitcnt lgkmcnt(8)\n\t"
    "v_mfma_f32_16x16x32_bf16 v[0:3], v[204:207], v[168:171], v[0:3]\n\t"
    "ds_read_b64_tr_b16 v[224:225], v250 offset:1024\n\t"
    "ds_read_b64_tr_b16 v[226:227], v250 offset:5120\n\t"
    "v_max3_f32 v239, v144, v145, v146\n\t"
    "v_mfma_f32_16x16x32_bf16 v[4:7], v[204:207], v[176:179], v[4:7]\n\t"
    "v_max3_f32 v240, v152, v153, v154\n\t"
    "v_max3_f32 v241, v160, v161, v162\n\t"
    "v_mfma_f32_16x16x32_bf16 v[8:11], v[204:207], v[184:187], v[8:11]\n\t"
    "v_max3_f32 v239, v239, v147, v148\n\t"
    "v_max3_f32 v240, v240, v155, v156\n\t"
    "s_waitcnt lgkmcnt(8)\n\t"
    "v_mfma_f32_16x16x32_bf16 v[12:15], v[208:211], v[168:171], v[12:15]\n\t"
    "ds_read_b64_tr_b16 v[228:229], v249 offset:1536\n\t"
    "ds_read_b64_tr_b16 v[230:231], v249 offset:5632\n\t"
    "v_mfma_f32_16x16x32_bf16 v[16:19], v[208:211], v[176:179], v[16:19]\n\t"
    "v_max3_f32 v241, v241, v163, v164\n\t"
    "v_max3_f32 v239, v239, v149, v150\n\t"
    "v_mfma_f32_16x16x32_bf16 v[20:23], v[208:211], v[184:187], v[20:23]\n\t"
    "v_max3_f32 v240, v240, v157, v158\n\t"
    "v_max3_f32 v241, v241, v165, v166\n\t"
    "s_waitcnt lgkmcnt(8)\n\t"
    "v_mfma_f32_16x16x32_bf16 v[24:27], v[212:215], v[168:171], v[24:27]\n\t"
    "ds_read_b64_tr_b16 v[232:233], v250 offset:1536\n\t"
    "ds_read_b64_tr_b16 v[234:235], v250 offset:5632\n\t"
    "v_mfma_f32_16x16x32_bf16 v[28:31], v[212:215], v[176:179], v[28:31]\n\t"
    "v_max3_f32 v239, v239, v151, v159\n\t"
    "v_max3_f32 v240, v240, v241, v167\n\t"
    "v_mfma_f32_16x16x32_bf16 v[32:35], v[212:215], v[184:187], v[32:35]\n\t"
    "v_max_f32 v239, v239, v240\n\t"
    "v_cmp_lt_f32 vcc, s79, v239\n\t"
    "s_cbranch_vccnz L_rale_%=\n\t"
    "L_rarle_%=:\n\t"
    "s_waitcnt lgkmcnt(8)\n\t"
    "v_mfma_f32_16x16x32_bf16 v[36:39], v[216:219], v[168:171], v[36:39]\n\t"
    "ds_read_b128 v[204:207], v247 offset:8192\n\t"
    "s_add_u32 s88, s88, 16384\n\t"
    "s_cmp_eq_u32 s88, 98304\n\t"
    "s_cselect_b32 s88, 0, s88\n\t"
    "v_add_u32 v249, s88, v246\n\t"
    "v_mfma_f32_16x16x32_bf16 v[40:43], v[216:219], v[176:179], v[40:43]\n\t"
    "v_xor_b32 v250, 32, v249\n\t"
    "v_exp_f32 v144, v144\n\t"
    "v_exp_f32 v145, v145\n\t"
    "v_mfma_f32_16x16x32_bf16 v[44:47], v[216:219], v[184:187], v[44:47]\n\t"
    "v_add_f32 v236, v236, v144\n\t"
    "v_exp_f32 v146, v146\n\t"
    "v_add_f32 v236, v236, v145\n\t"
    "s_waitcnt lgkmcnt(7)\n\t"
    "v_mfma_f32_16x16x32_bf16 v[48:51], v[220:223], v[168:171], v[48:51]\n\t"
    "ds_read_b128 v[208:211], v248 offset:8192\n\t"
    "v_cvt_pk_bf16_f32 v144, v144, v145\n\t"
    "v_mfma_f32_16x16x32_bf16 v[52:55], v[220:223], v[176:179], v[52:55]\n\t"
    "v_exp_f32 v147, v147\n\t"
    "v_add_f32 v236, v236, v146\n\t"
    "v_mfma_f32_16x16x32_bf16 v[56:59], v[220:223], v[184:187], v[56:59]\n\t"
    "v_exp_f32 v148, v148\n\t"
    "v_add_f32 v236, v236, v147\n\t"
    "s_waitcnt lgkmcnt(6)\n\t"
    "v_mfma_f32_16x16x32_bf16 v[60:63], v[224:227], v[168:171], v[60:63]\n\t"
    "ds_read_b128 v[212:215], v247 offset:9216\n\t"
    "v_cvt_pk_bf16_f32 v145, v146, v147\n\t"
    "v_mfma_f32_16x16x32_bf16 v[64:67], v[224:227], v[176:179], v[64:67]\n\t"
    "v_exp_f32 v149, v149\n\t"
    "v_add_f32 v236, v236, v148\n\t"
    "v_mfma_f32_16x16x32_bf16 v[68:71], v[224:227], v[184:187], v[68:71]\n\t"
    "v_exp_f32 v150, v150\n\t"
    "s_waitcnt lgkmcnt(5)\n\t"
    "v_mfma_f32_16x16x32_bf16 v[72:75], v[228:231], v[168:171], v[72:75]\n\t"
    "ds_read_b128 v[216:219], v248 offset:9216\n\t"
    "v_add_f32 v236, v236, v149\n\t"
    "v_mfma_f32_16x16x32_bf16 v[76:79], v[228:231], v[176:179], v[76:79]\n\t"
    "v_cvt_pk_bf16_f32 v146, v148, v149\n\t"
    "v_exp_f32 v151, v151\n\t"
    "v_mfma_f32_16x16x32_bf16 v[80:83], v[228:231], v[184:187], v[80:83]\n\t"
    "v_add_f32 v236, v236, v150\n\t"
    "v_exp_f32 v152, v152\n\t"
    "s_waitcnt lgkmcnt(4)\n\t"
    "v_mfma_f32_16x16x32_bf16 v[84:87], v[232:235], v[168:171], v[84:87]\n\t"
    "ds_read_b128 v[220:223], v247 offset:12288\n\t"
    "v_add_f32 v236, v236, v151\n\t"
    "v_cvt_pk_bf16_f32 v147, v150, v151\n\t"
    "v_mfma_f32_16x16x32_bf16 v[88:91], v[232:235], v[176:179], v[88:91]\n\t"
    "v_exp_f32 v153, v153\n\t"
    "v_add_f32 v237, v237, v152\n\t"
    "v_mfma_f32_16x16x32_bf16 v[92:95], v[232:235], v[184:187], v[92:95]\n\t"
    "v_exp_f32 v154, v154\n\t"
    "s_cmp_lg_u32 s78, 0\n\t"
    "s_cbranch_scc1 L_rble_%=\n\t"
    "L_rbrle_%=:\n\t"
    "s_waitcnt lgkmcnt(4)\n\t"
    "v_mfma_f32_16x16x32_bf16 v[168:171], v[204:207], v[96:99], v[192:195]\n\t"
    "ds_read_b128 v[224:227], v248 offset:12288\n\t"
    "v_add_f32 v237, v237, v153\n\t"
    "v_mfma_f32_16x16x32_bf16 v[176:179], v[204:207], v[112:115], v[196:199]\n\t"
    "v_cvt_pk_bf16_f32 v152, v152, v153\n\t"
    "v_exp_f32 v155, v155\n\t"
    "v_mfma_f32_16x16x32_bf16 v[184:187], v[204:207], v[128:131], v[200:203]\n\t"
    "v_add_f32 v237, v237, v154\n\t"
    "s_waitcnt lgkmcnt(4)\n\t"
    "v_mfma_f32_16x16x32_bf16 v[168:171], v[208:211], v[100:103], v[168:171]\n\t"
    "ds_read_b128 v[228:231], v247 offset:13312\n\t"
    "v_exp_f32 v156, v156\n\t"
    "v_mfma_f32_16x16x32_bf16 v[176:179], v[208:211], v[116:119], v[176:179]\n\t"
    "v_add_f32 v237, v237, v155\n\t"
    "v_cvt_pk_bf16_f32 v153, v154, v155\n\t"
    "v_mfma_f32_16x16x32_bf16 v[184:187], v[208:211], v[132:135], v[184:187]\n\t"
    "v_exp_f32 v157, v157\n\t"
    "v_add_f32 v237, v237, v156\n\t"
    "s_waitcnt lgkmcnt(4)\n\t"
    "v_mfma_f32_16x16x32_bf16 v[168:171], v[212:215], v[104:107], v[168:171]\n\t"
    "ds_read_b128 v[232:235], v248 offset:13312\n\t"
    "v_exp_f32 v158, v158\n\t"
    "v_mfma_f32_16x16x32_bf16 v[176:179], v[212:215], v[120:123], v[176:179]\n\t"
    "v_add_f32 v237, v237, v157\n\t"
    "v_cvt_pk_bf16_f32 v154, v156, v157\n\t"
    "v_mfma_f32_16x16x32_bf16 v[184:187], v[212:215], v[136:139], v[184:187]\n\t"
    "v_exp_f32 v159, v159\n\t"
    "s_waitcnt lgkmcnt(4)\n\t"
    "v_mfma_f32_16x16x32_bf16 v[168:171], v[216:219], v[108:111], v[168:171]\n\t"
    "v_add_u32 v247, s88, v245\n\t"
    "v_xor_b32 v248, 64, v247\n\t"
    "v_add_f32 v237, v237, v158\n\t"
    "v_mfma_f32_16x16x32_bf16 v[176:179], v[216:219], v[124:127], v[176:179]\n\t"
    "v_exp_f32 v160, v160\n\t"
    "v_add_f32 v237, v237, v159\n\t"
    "v_mfma_f32_16x16x32_bf16 v[184:187], v[216:219], v[140:143], v[184:187]\n\t"
    "v_cvt_pk_bf16_f32 v155, v158, v159\n\t"
    "v_exp_f32 v161, v161\n\t"
    "s_waitcnt lgkmcnt(3)\n\t"
    "v_mfma_f32_16x16x32_bf16 v[172:175], v[220:223], v[96:99], v[192:195]\n\t"
    "s_waitcnt lgkmcnt(0)\n\t"
    "s_barrier\n\t"
    "v_add_f32 v238, v238, v160\n\t"
    "v_mfma_f32_16x16x32_bf16 v[180:183], v[220:223], v[112:115], v[196:199]\n\t"
    "v_exp_f32 v162, v162\n\t"
    "v_add_f32 v238, v238, v161\n\t"
    "v_mfma_f32_16x16x32_bf16 v[188:191], v[220:223], v[128:131], v[200:203]\n\t"
    "v_cvt_pk_bf16_f32 v160, v160, v161\n\t"
    "v_exp_f32 v163, v163\n\t"
    "v_mfma_f32_16x16x32_bf16 v[172:175], v[224:227], v[100:103], v[172:175]\n\t"
    "ds_read_b64_tr_b16 v[204:205], v249 offset:0\n\t"
    "ds_read_b64_tr_b16 v[206:207], v249 offset:4096\n\t"
    "ds_read_b64_tr_b16 v[208:209], v250 offset:0\n\t"
    "v_mfma_f32_16x16x32_bf16 v[180:183], v[224:227], v[116:119], v[180:183]\n\t"
    "ds_read_b64_tr_b16 v[210:211], v250 offset:4096\n\t"
    "v_add_f32 v238, v238, v162\n\t"
    "v_exp_f32 v164, v164\n\t"
    "v_mfma_f32_16x16x32_bf16 v[188:191], v[224:227], v[132:135], v[188:191]\n\t"
    "v_add_f32 v238, v238, v163\n\t"
    "v_cvt_pk_bf16_f32 v161, v162, v163\n\t"
    "v_mfma_f32_16x16x32_bf16 v[172:175], v[228:231], v[104:107], v[172:175]\n\t"
    "ds_read_b64_tr_b16 v[212:213], v249 offset:512\n\t"
    "ds_read_b64_tr_b16 v[214:215], v249 offset:4608\n\t"
    "ds_read_b64_tr_b16 v[216:217], v250 offset:512\n\t"
    "v_mfma_f32_16x16x32_bf16 v[180:183], v[228:231], v[120:123], v[180:183]\n\t"
    "ds_read_b64_tr_b16 v[218:219], v250 offset:4608\n\t"
    "v_exp_f32 v165, v165\n\t"
    "v_add_f32 v238, v238, v164\n\t"
    "v_mfma_f32_16x16x32_bf16 v[188:191], v[228:231], v[136:139], v[188:191]\n\t"
    "v_exp_f32 v166, v166\n\t"
    "v_add_f32 v238, v238, v165\n\t"
    "v_cvt_pk_bf16_f32 v162, v164, v165\n\t"
    "v_mfma_f32_16x16x32_bf16 v[172:175], v[232:235], v[108:111], v[172:175]\n\t"
    "ds_read_b64_tr_b16 v[220:221], v249 offset:1024\n\t"
    "ds_read_b64_tr_b16 v[222:223], v249 offset:5120\n\t"
    "v_mfma_f32_16x16x32_bf16 v[180:183], v[232:235], v[124:127], v[180:183]\n\t"
    "v_exp_f32 v167, v167\n\t"
    "v_add_f32 v238, v238, v166\n\t"
    "v_mfma_f32_16x16x32_bf16 v[188:191], v[232:235], v[140:143], v[188:191]\n\t"
    "v_add_f32 v238, v238, v167\n\t"
    "v_cvt_pk_bf16_f32 v163, v166, v167\n\t"
    "s_sub_u32 s77, s77, 1\n\t"
    "s_cmp_lg_u32 s77, 0\n\t"
    "s_cbranch_scc1 L_at_%=\n\t"
    "s_waitcnt lgkmcnt(8)\n\t"
    "v_mfma_f32_16x16x32_bf16 v[0:3], v[204:207], v[144:147], v[0:3]\n\t"
    "ds_read_b64_tr_b16 v[224:225], v250 offset:1024\n\t"
    "ds_read_b64_tr_b16 v[226:227], v250 offset:5120\n\t"
    "v_max3_f32 v239, v168, v169, v170\n\t"
    "v_mfma_f32_16x16x32_bf16 v[4:7], v[204:207], v[152:155], v[4:7]\n\t"
    "v_max3_f32 v240, v176, v177, v178\n\t"
    "v_max3_f32 v241, v184, v185, v186\n\t"
    "v_max3_f32 v239, v239, v171, v172\n\t"
    "v_mfma_f32_16x16x32_bf16 v[8:11], v[204:207], v[160:163], v[8:11]\n\t"
    "v_max3_f32 v240, v240, v179, v180\n\t"
    "v_max3_f32 v241, v241, v187, v188\n\t"
    "v_max3_f32 v239, v239, v173, v174\n\t"
    "s_waitcnt lgkmcnt(8)\n\t"
    "v_mfma_f32_16x16x32_bf16 v[12:15], v[208:211], v[144:147], v[12:15]\n\t"
    "ds_read_b64_tr_b16 v[228:229], v249 offset:1536\n\t"
    "ds_read_b64_tr_b16 v[230:231], v249 offset:5632\n\t"
    "v_max3_f32 v240, v240, v181, v182\n\t"
    "v_mfma_f32_16x16x32_bf16 v[16:19], v[208:211], v[152:155], v[16:19]\n\t"
    "v_max3_f32 v241, v241, v189, v190\n\t"
    "v_max3_f32 v239, v239, v175, v183\n\t"
    "v_max3_f32 v240, v240, v241, v191\n\t"
    "v_mfma_f32_16x16x32_bf16 v[20:23], v[208:211], v[160:163], v[20:23]\n\t"
    "v_max_f32 v239, v239, v240\n\t"
    "v_cmp_lt_f32 vcc, s79, v239\n\t"
    "s_cbranch_vccnz L_rap15_%=\n\t"
    "L_rarp15_%=:\n\t"
    "s_waitcnt lgkmcnt(8)\n\t"
    "v_mfma_f32_16x16x32_bf16 v[24:27], v[212:215], v[144:147], v[24:27]\n\t"
    "ds_read_b64_tr_b16 v[232:233], v250 offset:1536\n\t"
    "ds_read_b64_tr_b16 v[234:235], v250 offset:5632\n\t"
    "v_exp_f32 v168, v168\n\t"
    "v_exp_f32 v169, v169\n\t"
    "v_mfma_f32_16x16x32_bf16 v[28:31], v[212:215], v[152:155], v[28:31]\n\t"
    "v_add_f32 v236, v236, v168\n\t"
    "v_exp_f32 v170, v170\n\t"
    "v_add_f32 v236, v236, v169\n\t"
    "v_cvt_pk_bf16_f32 v168, v168, v169\n\t"
    "v_mfma_f32_16x16x32_bf16 v[32:35], v[212:215], v[160:163], v[32:35]\n\t"
    "v_exp_f32 v171, v171\n\t"
    "v_add_f32 v236, v236, v170\n\t"
    "v_exp_f32 v172, v172\n\t"
    "v_add_f32 v236, v236, v171\n\t"
    "s_waitcnt lgkmcnt(8)\n\t"
    "v_mfma_f32_16x16x32_bf16 v[36:39], v[216:219], v[144:147], v[36:39]\n\t"
    "s_add_u32 s88, s88, 16384\n\t"
    "s_cmp_eq_u32 s88, 98304\n\t"
    "s_cselect_b32 s88, 0, s88\n\t"
    "v_add_u32 v249, s88, v246\n\t"
    "v_xor_b32 v250, 32, v249\n\t"
    "v_cvt_pk_bf16_f32 v169, v170, v171\n\t"
    "v_exp_f32 v173, v173\n\t"
    "v_mfma_f32_16x16x32_bf16 v[40:43], v[216:219], v[152:155], v[40:43]\n\t"
    "v_add_f32 v236, v236, v172\n\t"
    "v_exp_f32 v174, v174\n\t"
    "v_add_f32 v236, v236, v173\n\t"
    "v_cvt_pk_bf16_f32 v170, v172, v173\n\t"
    "v_mfma_f32_16x16x32_bf16 v[44:47], v[216:219], v[160:163], v[44:47]\n\t"
    "v_exp_f32 v175, v175\n\t"
    "v_add_f32 v236, v236, v174\n\t"
    "v_exp_f32 v176, v176\n\t"
    "v_add_f32 v236, v236, v175\n\t"
    "s_waitcnt lgkmcnt(6)\n\t"
    "v_mfma_f32_16x16x32_bf16 v[48:51], v[220:223], v[144:147], v[48:51]\n\t"
    "s_waitcnt lgkmcnt(0)\n\t"
    "s_barrier\n\t"
    "v_cvt_pk_bf16_f32 v171, v174, v175\n\t"
    "v_exp_f32 v177, v177\n\t"
    "v_add_f32 v237, v237, v176\n\t"
    "v_mfma_f32_16x16x32_bf16 v[52:55], v[220:223], v[152:155], v[52:55]\n\t"
    "v_exp_f32 v178, v178\n\t"
    "v_add_f32 v237, v237, v177\n\t"
    "v_cvt_pk_bf16_f32 v176, v176, v177\n\t"
    "v_exp_f32 v179, v179\n\t"
    "v_mfma_f32_16x16x32_bf16 v[56:59], v[220:223], v[160:163], v[56:59]\n\t"
    "v_add_f32 v237, v237, v178\n\t"
    "v_exp_f32 v180, v180\n\t"
    "v_add_f32 v237, v237, v179\n\t"
    "v_mfma_f32_16x16x32_bf16 v[60:63], v[224:227], v[144:147], v[60:63]\n\t"
    "ds_read_b64_tr_b16 v[204:205], v249 offset:0\n\t"
    "ds_read_b64_tr_b16 v[206:207], v249 offset:4096\n\t"
    "ds_read_b64_tr_b16 v[208:209], v250 offset:0\n\t"
    "ds_read_b64_tr_b16 v[210:211], v250 offset:4096\n\t"
    "v_cvt_pk_bf16_f32 v177, v178, v179\n\t"
    "v_mfma_f32_16x16x32_bf16 v[64:67], v[224:227], v[152:155], v[64:67]\n\t"
    "v_exp_f32 v181, v181\n\t"
    "v_add_f32 v237, v237, v180\n\t"
    "v_exp_f32 v182, v182\n\t"
    "v_add_f32 v237, v237, v181\n\t"
    "v_cvt_pk_bf16_f32 v178, v180, v181\n\t"
    "v_mfma_f32_16x16x32_bf16 v[68:71], v[224:227], v[160:163], v[68:71]\n\t"
    "v_exp_f32 v183, v183\n\t"
    "v_add_f32 v237, v237, v182\n\t"
    "v_exp_f32 v184, v184\n\t"
    "v_add_f32 v237, v237, v183\n\t"
    "v_mfma_f32_16x16x32_bf16 v[72:75], v[228:231], v[144:147], v[72:75]\n\t"
    "ds_read_b64_tr_b16 v[212:213], v249 offset:512\n\t"
    "ds_read_b64_tr_b16 v[214:215], v249 offset:4608\n\t"
    "ds_read_b64_tr_b16 v[216:217], v250 offset:512\n\t"
    "ds_read_b64_tr_b16 v[218:219], v250 offset:4608\n\t"
    "v_cvt_pk_bf16_f32 v179, v182, v183\n\t"
    "v_mfma_f32_16x16x32_bf16 v[76:79], v[228:231], v[152:155], v[76:79]\n\t"
    "v_exp_f32 v185, v185\n\t"
    "v_add_f32 v238, v238, v184\n\t"
    "v_exp_f32 v186, v186\n\t"
    "v_add_f32 v238, v238, v185\n\t"
    "v_cvt_pk_bf16_f32 v184, v184, v185\n\t"
    "v_mfma_f32_16x16x32_bf16 v[80:83], v[228:231], v[160:163], v[80:83]\n\t"
    "v_exp_f32 v187, v187\n\t"
    "v_add_f32 v238, v238, v186\n\t"
    "v_exp_f32 v188, v188\n\t"
    "v_add_f32 v238, v238, v187\n\t"
    "v_mfma_f32_16x16x32_bf16 v[84:87], v[232:235], v[144:147], v[84:87]\n\t"
    "ds_read_b64_tr_b16 v[220:221], v249 offset:1024\n\t"
    "ds_read_b64_tr_b16 v[222:223], v249 offset:5120\n\t"
    "v_cvt_pk_bf16_f32 v185, v186, v187\n\t"
    "v_exp_f32 v189, v189\n\t"
    "v_add_f32 v238, v238, v188\n\t"
    "v_mfma_f32_16x16x32_bf16 v[88:91], v[232:235], v[152:155], v[88:91]\n\t"
    "v_exp_f32 v190, v190\n\t"
    "v_add_f32 v238, v238, v189\n\t"
    "v_cvt_pk_bf16_f32 v186, v188, v189\n\t"
    "v_exp_f32 v191, v191\n\t"
    "v_mfma_f32_16x16x32_bf16 v[92:95], v[232:235], v[160:163], v[92:95]\n\t"
    "v_add_f32 v238, v238, v190\n\t"
    "v_add_f32 v238, v238, v191\n\t"
    "v_cvt_pk_bf16_f32 v187, v190, v191\n\t"
    "s_cmp_lg_u32 s78, 0\n\t"
    "s_cbranch_scc1 L_rbp15_%=\n\t"
    "L_rbrp15_%=:\n\t"
    "s_waitcnt lgkmcnt(8)\n\t"
    "v_mfma_f32_16x16x32_bf16 v[0:3], v[204:207], v[168:171], v[0:3]\n\t"
    "ds_read_b64_tr_b16 v[224:225], v250 offset:1024\n\t"
    "ds_read_b64_tr_b16 v[226:227], v250 offset:5120\n\t"
    "v_mov_b32 v239, v236\n\t"
    "v_mfma_f32_16x16x32_bf16 v[4:7], v[204:207], v[176:179], v[4:7]\n\t"
    "v_mov_b32 v240, v237\n\t"
    "v_mov_b32 v241, v238\n\t"
    "v_permlane16_swap_b32 v236, v239\n\t"
    "v_mfma_f32_16x16x32_bf16 v[8:11], v[204:207], v[184:187], v[8:11]\n\t"
    "v_permlane16_swap_b32 v237, v240\n\t"
    "v_permlane16_swap_b32 v238, v241\n\t"
    "v_add_f32 v236, v236, v239\n\t"
    "s_waitcnt lgkmcnt(8)\n\t"
    "v_mfma_f32_16x16x32_bf16 v[12:15], v[208:211], v[168:171], v[12:15]\n\t"
    "ds_read_b64_tr_b16 v[228:229], v249 offset:1536\n\t"
    "ds_read_b64_tr_b16 v[230:231], v249 offset:5632\n\t"
    "v_add_f32 v237, v237, v240\n\t"
    "v_mfma_f32_16x16x32_bf16 v[16:19], v[208:211], v[176:179], v[16:19]\n\t"
    "v_add_f32 v238, v238, v241\n\t"
    "v_mov_b32 v239, v236\n\t"
    "v_mov_b32 v240, v237\n\t"
    "v_mfma_f32_16x16x32_bf16 v[20:23], v[208:211], v[184:187], v[20:23]\n\t"
    "v_mov_b32 v241, v238\n\t"
    "v_permlane32_swap_b32 v236, v239\n\t"
    "v_permlane32_swap_b32 v237, v240\n\t"
    "s_waitcnt lgkmcnt(8)\n\t"
    "v_mfma_f32_16x16x32_bf16 v[24:27], v[212:215], v[168:171], v[24:27]\n\t"
    "ds_read_b64_tr_b16 v[232:233], v250 offset:1536\n\t"
    "ds_read_b64_tr_b16 v[234:235], v250 offset:5632\n\t"
    "v_permlane32_swap_b32 v238, v241\n\t"
    "v_mfma_f32_16x16x32_bf16 v[28:31], v[212:215], v[176:179], v[28:31]\n\t"
    "v_add_f32 v236, v236, v239\n\t"
    "v_add_f32 v237, v237, v240\n\t"
    "v_add_f32 v238, v238, v241\n\t"
    "v_mfma_f32_16x16x32_bf16 v[32:35], v[212:215], v[184:187], v[32:35]\n\t"
    "v_rcp_f32 v236, v236\n\t"
    "v_rcp_f32 v237, v237\n\t"
    "v_rcp_f32 v238, v238\n\t"
    "s_waitcnt lgkmcnt(8)\n\t"
    "v_mfma_f32_16x16x32_bf16 v[36:39], v[216:219], v[168:171], v[36:39]\n\t"
    "s_add_u32 s88, s88, 16384\n\t"
    "s_cmp_eq_u32 s88, 98304\n\t"
    "s_cselect_b32 s88, 0, s88\n\t"
    "v_mul_f32 v0, v0, v236\n\t"
    "v_mul_f32 v1, v1, v236\n\t"
    "v_mul_f32 v2, v2, v236\n\t"
    "v_mul_f32 v3, v3, v236\n\t"
    "v_mfma_f32_16x16x32_bf16 v[40:43], v[216:219], v[176:179], v[40:43]\n\t"
    "v_mul_f32 v12, v12, v236\n\t"
    "v_mul_f32 v13, v13, v236\n\t"
    "v_mul_f32 v14, v14, v236\n\t"
    "v_mul_f32 v15, v15, v236\n\t"
    "v_mfma_f32_16x16x32_bf16 v[44:47], v[216:219], v[184:187], v[44:47]\n\t"
    "v_cvt_pk_bf16_f32 v0, v0, v1\n\t"
    "v_cvt_pk_bf16_f32 v1, v2, v3\n\t"
    "v_cvt_pk_bf16_f32 v2, v12, v13\n\t"
    "v_cvt_pk_bf16_f32 v3, v14, v15\n\t"
    "s_waitcnt lgkmcnt(6)\n\t"
    "v_mfma_f32_16x16x32_bf16 v[48:51], v[220:223], v[168:171], v[48:51]\n\t"
    "v_mul_f32 v4, v4, v237\n\t"
    "v_mul_f32 v5, v5, v237\n\t"
    "v_mul_f32 v6, v6, v237\n\t"
    "v_mul_f32 v7, v7, v237\n\t"
    "v_mfma_f32_16x16x32_bf16 v[52:55], v[220:223], v[176:179], v[52:55]\n\t"
    "v_mul_f32 v16, v16, v237\n\t"
    "v_mul_f32 v17, v17, v237\n\t"
    "v_mul_f32 v18, v18, v237\n\t"
    "v_mul_f32 v19, v19, v237\n\t"
    "v_mfma_f32_16x16x32_bf16 v[56:59], v[220:223], v[184:187], v[56:59]\n\t"
    "v_cvt_pk_bf16_f32 v4, v4, v5\n\t"
    "v_cvt_pk_bf16_f32 v5, v6, v7\n\t"
    "v_cvt_pk_bf16_f32 v6, v16, v17\n\t"
    "v_cvt_pk_bf16_f32 v7, v18, v19\n\t"
    "s_waitcnt lgkmcnt(4)\n\t"
    "v_mfma_f32_16x16x32_bf16 v[60:63], v[224:227], v[168:171], v[60:63]\n\t"
    "v_mul_f32 v8, v8, v238\n\t"
    "v_mul_f32 v9, v9, v238\n\t"
    "v_mul_f32 v10, v10, v238\n\t"
    "v_mul_f32 v11, v11, v238\n\t"
    "v_mfma_f32_16x16x32_bf16 v[64:67], v[224:227], v[176:179], v[64:67]\n\t"
    "v_mul_f32 v20, v20, v238\n\t"
    "v_mul_f32 v21, v21, v238\n\t"
    "v_mul_f32 v22, v22, v238\n\t"
    "v_mul_f32 v23, v23, v238\n\t"
    "v_mfma_f32_16x16x32_bf16 v[68:71], v[224:227], v[184:187], v[68:71]\n\t"
    "v_cvt_pk_bf16_f32 v8, v8, v9\n\t"
    "v_cvt_pk_bf16_f32 v9, v10, v11\n\t"
    "v_cvt_pk_bf16_f32 v10, v20, v21\n\t"
    "v_cvt_pk_bf16_f32 v11, v22, v23\n\t"
    "s_waitcnt lgkmcnt(2)\n\t"
    "v_mfma_f32_16x16x32_bf16 v[72:75], v[228:231], v[168:171], v[72:75]\n\t"
    "v_mul_f32 v24, v24, v236\n\t"
    "v_mul_f32 v25, v25, v236\n\t"
    "v_mul_f32 v26, v26, v236\n\t"
    "v_mul_f32 v27, v27, v236\n\t"
    "v_mul_f32 v36, v36, v236\n\t"
    "v_mul_f32 v37, v37, v236\n\t"
    "v_mfma_f32_16x16x32_bf16 v[76:79], v[228:231], v[176:179], v[76:79]\n\t"
    "v_mul_f32 v38, v38, v236\n\t"
    "v_mul_f32 v39, v39, v236\n\t"
    "v_cvt_pk_bf16_f32 v24, v24, v25\n\t"
    "v_cvt_pk_bf16_f32 v25, v26, v27\n\t"
    "v_cvt_pk_bf16_f32 v26, v36, v37\n\t"
    "v_cvt_pk_bf16_f32 v27, v38, v39\n\t"
    "v_mfma_f32_16x16x32_bf16 v[80:83], v[228:231], v[184:187], v[80:83]\n\t"
    "v_mul_f32 v28, v28, v237\n\t"
    "v_mul_f32 v29, v29, v237\n\t"
    "v_mul_f32 v30, v30, v237\n\t"
    "v_mul_f32 v31, v31, v237\n\t"
    "v_mul_f32 v40, v40, v237\n\t"
    "v_mul_f32 v41, v41, v237\n\t"
    "s_waitcnt lgkmcnt(0)\n\t"
    "v_mfma_f32_16x16x32_bf16 v[84:87], v[232:235], v[168:171], v[84:87]\n\t"
    "v_mul_f32 v42, v42, v237\n\t"
    "v_mul_f32 v43, v43, v237\n\t"
    "v_cvt_pk_bf16_f32 v28, v28, v29\n\t"
    "v_cvt_pk_bf16_f32 v29, v30, v31\n\t"
    "v_cvt_pk_bf16_f32 v30, v40, v41\n\t"
    "v_cvt_pk_bf16_f32 v31, v42, v43\n\t"
    "v_mfma_f32_16x16x32_bf16 v[88:91], v[232:235], v[176:179], v[88:91]\n\t"
    "v_mul_f32 v32, v32, v238\n\t"
    "v_mul_f32 v33, v33, v238\n\t"
    "v_mul_f32 v34, v34, v238\n\t"
    "v_mul_f32 v35, v35, v238\n\t"
    "v_mul_f32 v44, v44, v238\n\t"
    "v_mul_f32 v45, v45, v238\n\t"
    "v_mfma_f32_16x16x32_bf16 v[92:95], v[232:235], v[184:187], v[92:95]\n\t"
    "v_mul_f32 v46, v46, v238\n\t"
    "v_mul_f32 v47, v47, v238\n\t"
    "v_cvt_pk_bf16_f32 v32, v32, v33\n\t"
    "v_cvt_pk_bf16_f32 v33, v34, v35\n\t"
    "v_cvt_pk_bf16_f32 v34, v44, v45\n\t"
    "v_cvt_pk_bf16_f32 v35, v46, v47\n\t"
    "v_mul_f32 v48, v48, v236\n\t"
    "v_mul_f32 v49, v49, v236\n\t"
    "v_mul_f32 v50, v50, v236\n\t"
    "v_mul_f32 v51, v51, v236\n\t"
    "v_mul_f32 v60, v60, v236\n\t"
    "v_mul_f32 v61, v61, v236\n\t"
    "v_mul_f32 v62, v62, v236\n\t"
    "v_mul_f32 v63, v63, v236\n\t"
    "v_cvt_pk_bf16_f32 v48, v48, v49\n\t"
    "v_cvt_pk_bf16_f32 v49, v50, v51\n\t"
    "v_cvt_pk_bf16_f32 v50, v60, v61\n\t"
    "v_cvt_pk_bf16_f32 v51, v62, v63\n\t"
    "v_mul_f32 v52, v52, v237\n\t"
    "v_mul_f32 v53, v53, v237\n\t"
    "v_mul_f32 v54, v54, v237\n\t"
    "v_mul_f32 v55, v55, v237\n\t"
    "v_mul_f32 v64, v64, v237\n\t"
    "v_mul_f32 v65, v65, v237\n\t"
    "v_mul_f32 v66, v66, v237\n\t"
    "v_mul_f32 v67, v67, v237\n\t"
    "v_cvt_pk_bf16_f32 v52, v52, v53\n\t"
    "v_cvt_pk_bf16_f32 v53, v54, v55\n\t"
    "v_cvt_pk_bf16_f32 v54, v64, v65\n\t"
    "v_cvt_pk_bf16_f32 v55, v66, v67\n\t"
    "v_mul_f32 v56, v56, v238\n\t"
    "v_mul_f32 v57, v57, v238\n\t"
    "v_mul_f32 v58, v58, v238\n\t"
    "v_mul_f32 v59, v59, v238\n\t"
    "v_mul_f32 v68, v68, v238\n\t"
    "v_mul_f32 v69, v69, v238\n\t"
    "v_mul_f32 v70, v70, v238\n\t"
    "v_mul_f32 v71, v71, v238\n\t"
    "v_cvt_pk_bf16_f32 v56, v56, v57\n\t"
    "v_cvt_pk_bf16_f32 v57, v58, v59\n\t"
    "v_cvt_pk_bf16_f32 v58, v68, v69\n\t"
    "v_cvt_pk_bf16_f32 v59, v70, v71\n\t"
    "v_mul_f32 v72, v72, v236\n\t"
    "v_mul_f32 v73, v73, v236\n\t"
    "v_mul_f32 v74, v74, v236\n\t"
    "v_mul_f32 v75, v75, v236\n\t"
    "v_mul_f32 v84, v84, v236\n\t"
    "v_mul_f32 v85, v85, v236\n\t"
    "v_mul_f32 v86, v86, v236\n\t"
    "v_mul_f32 v87, v87, v236\n\t"
    "v_cvt_pk_bf16_f32 v72, v72, v73\n\t"
    "v_cvt_pk_bf16_f32 v73, v74, v75\n\t"
    "v_cvt_pk_bf16_f32 v74, v84, v85\n\t"
    "v_cvt_pk_bf16_f32 v75, v86, v87\n\t"
    "v_mul_f32 v76, v76, v237\n\t"
    "v_mul_f32 v77, v77, v237\n\t"
    "v_mul_f32 v78, v78, v237\n\t"
    "v_mul_f32 v79, v79, v237\n\t"
    "v_mul_f32 v88, v88, v237\n\t"
    "v_mul_f32 v89, v89, v237\n\t"
    "v_mul_f32 v90, v90, v237\n\t"
    "v_mul_f32 v91, v91, v237\n\t"
    "v_cvt_pk_bf16_f32 v76, v76, v77\n\t"
    "v_cvt_pk_bf16_f32 v77, v78, v79\n\t"
    "v_cvt_pk_bf16_f32 v78, v88, v89\n\t"
    "v_cvt_pk_bf16_f32 v79, v90, v91\n\t"
    "v_mul_f32 v80, v80, v238\n\t"
    "v_mul_f32 v81, v81, v238\n\t"
    "v_mul_f32 v82, v82, v238\n\t"
    "v_mul_f32 v83, v83, v238\n\t"
    "v_mul_f32 v92, v92, v238\n\t"
    "v_mul_f32 v93, v93, v238\n\t"
    "v_mul_f32 v94, v94, v238\n\t"
    "v_mul_f32 v95, v95, v238\n\t"
    "v_cvt_pk_bf16_f32 v80, v80, v81\n\t"
    "v_cvt_pk_bf16_f32 v81, v82, v83\n\t"
    "v_cvt_pk_bf16_f32 v82, v92, v93\n\t"
    "v_cvt_pk_bf16_f32 v83, v94, v95\n\t"
    "s_add_u32 s76, s76, 1\n\t"
    "s_cmp_lt_u32 s76, 2\n\t"
    "s_nop 1\n\t"
    "s_cbranch_scc1 L_item_%=\n\t"
    "s_mov_b32 s86, 0\n\t"
    "buffer_store_dwordx4 v[0:3], v253, s[56:59], s86 offen offset:0 sc1\n\t"
    "buffer_store_dwordx4 v[4:7], v253, s[56:59], s86 offen offset:1024 sc1\n\t"
    "buffer_store_dwordx4 v[8:11], v253, s[56:59], s86 offen offset:2048 sc1\n\t"
    "s_mov_b32 s86, 12288\n\t"
    "buffer_store_dwordx4 v[24:27], v253, s[56:59], s86 offen offset:0 sc1\n\t"
    "buffer_store_dwordx4 v[28:31], v253, s[56:59], s86 offen offset:1024 sc1\n\t"
    "buffer_store_dwordx4 v[32:35], v253, s[56:59], s86 offen offset:2048 sc1\n\t"
    "s_mov_b32 s86, 24576\n\t"
    "buffer_store_dwordx4 v[48:51], v253, s[56:59], s86 offen offset:0 sc1\n\t"
    "buffer_store_dwordx4 v[52:55], v253, s[56:59], s86 offen offset:1024 sc1\n\t"
    "buffer_store_dwordx4 v[56:59], v253, s[56:59], s86 offen offset:2048 sc1\n\t"
    "s_mov_b32 s86, 36864\n\t"
    "buffer_store_dwordx4 v[72:75], v253, s[56:59], s86 offen offset:0 sc1\n\t"
    "buffer_store_dwordx4 v[76:79], v253, s[56:59], s86 offen offset:1024 sc1\n\t"
    "buffer_store_dwordx4 v[80:83], v253, s[56:59], s86 offen offset:2048 sc1\n\t"
    "v_mov_b32 v252, v253\n\t"
    "s_lshr_b32 s85, s26, 1\n\t"
    "s_and_b32 s86, s26, 1\n\t"
    "s_mul_i32 s84, s85, 6144\n\t"
    "s_add_u32 s84, s84, 65536\n\t"
    "s_add_u32 s84, s84, s27\n\t"
    "v_add_u32 v248, s84, v252\n\t"
    "v_lshrrev_b32 v254, 9, v252\n\t"
    "v_lshl_add_u32 v254, v254, 10, v246\n\t"
    "s_bfe_u32 s84, s35, 0x10003\n\t"
    "s_mul_i32 s85, s84, 2\n\t"
    "s_mul_i32 s87, s86, 5\n\t"
    "s_add_u32 s85, s85, s87\n\t"
    "s_lshr_b32 s84, s85, 1\n\t"
    "s_lshl_b32 s84, s84, 9\n\t"
    "s_add_u32 s84, s84, 77824\n\t"
    "v_add_u32 v254, s84, v254\n\t"
    "s_and_b32 s85, s85, 1\n\t"
    "s_lshl_b32 s84, s85, 5\n\t"
    "s_lshl_b32 s85, s85, 9\n\t"
    "v_xor_b32 v244, s84, v254\n\t"
    "v_xor_b32 v245, 32, v244\n\t"
    "v_add_u32 v245, s85, v245\n\t"
    "v_add_u32 v250, 49152, v248\n\t"
    "v_add_u32 v247, 49152, v244\n\t"
    "v_add_u32 v249, 49152, v245\n\t"
    "s_waitcnt lgkmcnt(0)\n\t"
    "s_barrier\n\t"
    "ds_read_b64_tr_b16 v[144:145], v247 offset:0\n\t"
    "ds_read_b64_tr_b16 v[146:147], v247 offset:6144\n\t"
    "ds_read_b128 v[120:123], v250 offset:0\n\t"
    "ds_read_b128 v[124:127], v250 offset:1024\n\t"
    "ds_read_b128 v[128:131], v250 offset:2048\n\t"
    "ds_read_b128 v[132:135], v250 offset:3072\n\t"
    "ds_read_b128 v[136:139], v250 offset:4096\n\t"
    "ds_read_b128 v[140:143], v250 offset:5120\n\t"
    "ds_read_b64_tr_b16 v[148:149], v249 offset:0\n\t"
    "ds_read_b64_tr_b16 v[150:151], v249 offset:6144\n\t"
    "ds_read_b64_tr_b16 v[152:153], v247 offset:512\n\t"
    "ds_read_b64_tr_b16 v[154:155], v247 offset:6656\n\t"
    "ds_read_b64_tr_b16 v[156:157], v249 offset:512\n\t"
    "ds_read_b64_tr_b16 v[158:159], v249 offset:6656\n\t"
    "ds_read_b64_tr_b16 v[160:161], v247 offset:1024\n\t"
    "ds_read_b64_tr_b16 v[162:163], v247 offset:7168\n\t"
    "s_waitcnt lgkmcnt(13)\n\t"
    "v_mfma_f32_16x16x32_bf16 v[0:3], v[144:147], v[120:123], 0\n\t"
    "s_waitcnt lgkmcnt(12)\n\t"
    "v_mfma_f32_16x16x32_bf16 v[4:7], v[144:147], v[124:127], 0\n\t"
    "s_waitcnt lgkmcnt(11)\n\t"
    "v_mfma_f32_16x16x32_bf16 v[8:11], v[144:147], v[128:131], 0\n\t"
    "s_waitcnt lgkmcnt(10)\n\t"
    "v_mfma_f32_16x16x32_bf16 v[12:15], v[144:147], v[132:135], 0\n\t"
    "s_waitcnt lgkmcnt(9)\n\t"
    "v_mfma_f32_16x16x32_bf16 v[16:19], v[144:147], v[136:139], 0\n\t"
    "s_waitcnt lgkmcnt(8)\n\t"
    "v_mfma_f32_16x16x32_bf16 v[20:23], v[144:147], v[140:143], 0\n\t"
    "s_waitcnt lgkmcnt(6)\n\t"
    "v_mfma_f32_16x16x32_bf16 v[24:27], v[148:151], v[120:123], 0\n\t"
    "s_waitcnt lgkmcnt(0)\n\t"
    "s_barrier\n\t"
    "v_mfma_f32_16x16x32_bf16 v[28:31], v[148:151], v[124:127], 0\n\t"
    "v_mfma_f32_16x16x32_bf16 v[32:35], v[148:151], v[128:131], 0\n\t"
    "ds_read_b64_tr_b16 v[188:189], v247 offset:24576\n\t"
    "v_mfma_f32_16x16x32_bf16 v[36:39], v[148:151], v[132:135], 0\n\t"
    "ds_read_b64_tr_b16 v[190:191], v247 offset:30720\n\t"
    "v_mfma_f32_16x16x32_bf16 v[40:43], v[148:151], v[136:139], 0\n\t"
    "ds_read_b128 v[164:167], v250 offset:24576\n\t"
    "v_mfma_f32_16x16x32_bf16 v[44:47], v[148:151], v[140:143], 0\n\t"
    "v_mfma_f32_16x16x32_bf16 v[48:51], v[152:155], v[120:123], 0\n\t"
    "ds_read_b128 v[168:171], v250 offset:25600\n\t"
    "v_mfma_f32_16x16x32_bf16 v[52:55], v[152:155], v[124:127], 0\n\t"
    "ds_read_b128 v[172:175], v250 offset:26624\n\t"
    "v_mfma_f32_16x16x32_bf16 v[56:59], v[152:155], v[128:131], 0\n\t"
    "ds_read_b128 v[176:179], v250 offset:27648\n\t"
    "v_mfma_f32_16x16x32_bf16 v[60:63], v[152:155], v[132:135], 0\n\t"
    "v_mfma_f32_16x16x32_bf16 v[64:67], v[152:155], v[136:139], 0\n\t"
    "ds_read_b128 v[180:183], v250 offset:28672\n\t"
    "v_mfma_f32_16x16x32_bf16 v[68:71], v[152:155], v[140:143], 0\n\t"
    "ds_read_b128 v[184:187], v250 offset:29696\n\t"
    "v_mfma_f32_16x16x32_bf16 v[72:75], v[156:159], v[120:123], 0\n\t"
    "ds_read_b64_tr_b16 v[192:193], v249 offset:24576\n\t"
    "v_mfma_f32_16x16x32_bf16 v[76:79], v[156:159], v[124:127], 0\n\t"
    "v_mfma_f32_16x16x32_bf16 v[80:83], v[156:159], v[128:131], 0\n\t"
    "ds_read_b64_tr_b16 v[194:195], v249 offset:30720\n\t"
    "v_mfma_f32_16x16x32_bf16 v[84:87], v[156:159], v[132:135], 0\n\t"
    "ds_read_b64_tr_b16 v[196:197], v247 offset:25088\n\t"
    "v_mfma_f32_16x16x32_bf16 v[88:91], v[156:159], v[136:139], 0\n\t"
    "ds_read_b64_tr_b16 v[198:199], v247 offset:31232\n\t"
    "v_mfma_f32_16x16x32_bf16 v[92:95], v[156:159], v[140:143], 0\n\t"
    "v_mfma_f32_16x16x32_bf16 v[96:99], v[160:163], v[120:123], 0\n\t"
    "ds_read_b64_tr_b16 v[200:201], v249 offset:25088\n\t"
    "v_mfma_f32_16x16x32_bf16 v[100:103], v[160:163], v[124:127], 0\n\t"
    "ds_read_b64_tr_b16 v[202:203], v249 offset:31232\n\t"
    "v_mfma_f32_16x16x32_bf16 v[104:107], v[160:163], v[128:131], 0\n\t"
    "ds_read_b64_tr_b16 v[204:205], v247 offset:25600\n\t"
    "v_mfma_f32_16x16x32_bf16 v[108:111], v[160:163], v[132:135], 0\n\t"
    "v_mfma_f32_16x16x32_bf16 v[112:115], v[160:163], v[136:139], 0\n\t"
    "ds_read_b64_tr_b16 v[206:207], v247 offset:31744\n\t"
    "v_mfma_f32_16x16x32_bf16 v[116:119], v[160:163], v[140:143], 0\n\t"
    "s_waitcnt lgkmcnt(13)\n\t"
    "v_mfma_f32_16x16x32_bf16 v[0:3], v[188:191], v[164:167], v[0:3]\n\t"
    "s_waitcnt lgkmcnt(12)\n\t"
    "v_mfma_f32_16x16x32_bf16 v[4:7], v[188:191], v[168:171], v[4:7]\n\t"
    "s_waitcnt lgkmcnt(11)\n\t"
    "v_mfma_f32_16x16x32_bf16 v[8:11], v[188:191], v[172:175], v[8:11]\n\t"
    "s_waitcnt lgkmcnt(10)\n\t"
    "v_mfma_f32_16x16x32_bf16 v[12:15], v[188:191], v[176:179], v[12:15]\n\t"
    "s_waitcnt lgkmcnt(9)\n\t"
    "v_mfma_f32_16x16x32_bf16 v[16:19], v[188:191], v[180:183], v[16:19]\n\t"
    "s_waitcnt lgkmcnt(8)\n\t"
    "v_mfma_f32_16x16x32_bf16 v[20:23], v[188:191], v[184:187], v[20:23]\n\t"
    "s_waitcnt lgkmcnt(6)\n\t"
    "v_mfma_f32_16x16x32_bf16 v[24:27], v[192:195], v[164:167], v[24:27]\n\t"
    "s_waitcnt lgkmcnt(0)\n\t"
    "s_barrier\n\t"
    "v_mfma_f32_16x16x32_bf16 v[28:31], v[192:195], v[168:171], v[28:31]\n\t"
    "v_mfma_f32_16x16x32_bf16 v[32:35], v[192:195], v[172:175], v[32:35]\n\t"
    "ds_read_b64_tr_b16 v[144:145], v244 offset:0\n\t"
    "v_mfma_f32_16x16x32_bf16 v[36:39], v[192:195], v[176:179], v[36:39]\n\t"
    "ds_read_b64_tr_b16 v[146:147], v244 offset:6144\n\t"
    "v_mfma_f32_16x16x32_bf16 v[40:43], v[192:195], v[180:183], v[40:43]\n\t"
    "ds_read_b128 v[120:123], v248 offset:0\n\t"
    "v_mfma_f32_16x16x32_bf16 v[44:47], v[192:195], v[184:187], v[44:47]\n\t"
    "v_mfma_f32_16x16x32_bf16 v[48:51], v[196:199], v[164:167], v[48:51]\n\t"
    "ds_read_b128 v[124:127], v248 offset:1024\n\t"
    "v_mfma_f32_16x16x32_bf16 v[52:55], v[196:199], v[168:171], v[52:55]\n\t"
    "ds_read_b128 v[128:131], v248 offset:2048\n\t"
    "v_mfma_f32_16x16x32_bf16 v[56:59], v[196:199], v[172:175], v[56:59]\n\t"
    "ds_read_b128 v[132:135], v248 offset:3072\n\t"
    "v_mfma_f32_16x16x32_bf16 v[60:63], v[196:199], v[176:179], v[60:63]\n\t"
    "v_mfma_f32_16x16x32_bf16 v[64:67], v[196:199], v[180:183], v[64:67]\n\t"
    "ds_read_b128 v[136:139], v248 offset:4096\n\t"
    "v_mfma_f32_16x16x32_bf16 v[68:71], v[196:199], v[184:187], v[68:71]\n\t"
    "ds_read_b128 v[140:143], v248 offset:5120\n\t"
    "v_mfma_f32_16x16x32_bf16 v[72:75], v[200:203], v[164:167], v[72:75]\n\t"
    "ds_read_b64_tr_b16 v[148:149], v245 offset:0\n\t"
    "v_mfma_f32_16x16x32_bf16 v[76:79], v[200:203], v[168:171], v[76:79]\n\t"
    "v_mfma_f32_16x16x32_bf16 v[80:83], v[200:203], v[172:175], v[80:83]\n\t"
    "ds_read_b64_tr_b16 v[150:151], v245 offset:6144\n\t"
    "v_mfma_f32_16x16x32_bf16 v[84:87], v[200:203], v[176:179], v[84:87]\n\t"
    "ds_read_b64_tr_b16 v[152:153], v244 offset:512\n\t"
    "v_mfma_f32_16x16x32_bf16 v[88:91], v[200:203], v[180:183], v[88:91]\n\t"
    "ds_read_b64_tr_b16 v[154:155], v244 offset:6656\n\t"
    "v_mfma_f32_16x16x32_bf16 v[92:95], v[200:203], v[184:187], v[92:95]\n\t"
    "v_mfma_f32_16x16x32_bf16 v[96:99], v[204:207], v[164:167], v[96:99]\n\t"
    "ds_read_b64_tr_b16 v[156:157], v245 offset:512\n\t"
    "v_mfma_f32_16x16x32_bf16 v[100:103], v[204:207], v[168:171], v[100:103]\n\t"
    "ds_read_b64_tr_b16 v[158:159], v245 offset:6656\n\t"
    "v_mfma_f32_16x16x32_bf16 v[104:107], v[204:207], v[172:175], v[104:107]\n\t"
    "ds_read_b64_tr_b16 v[160:161], v244 offset:1024\n\t"
    "v_mfma_f32_16x16x32_bf16 v[108:111], v[204:207], v[176:179], v[108:111]\n\t"
    "v_mfma_f32_16x16x32_bf16 v[112:115], v[204:207], v[180:183], v[112:115]\n\t"
    "ds_read_b64_tr_b16 v[162:163], v244 offset:7168\n\t"
    "v_mfma_f32_16x16x32_bf16 v[116:119], v[204:207], v[184:187], v[116:119]\n\t"
    "s_waitcnt lgkmcnt(13)\n\t"
    "v_mfma_f32_16x16x32_bf16 v[0:3], v[144:147], v[120:123], v[0:3]\n\t"
    "s_waitcnt lgkmcnt(12)\n\t"
    "v_mfma_f32_16x16x32_bf16 v[4:7], v[144:147], v[124:127], v[4:7]\n\t"
    "s_waitcnt lgkmcnt(11)\n\t"
    "v_mfma_f32_16x16x32_bf16 v[8:11], v[144:147], v[128:131], v[8:11]\n\t"
    "s_waitcnt lgkmcnt(10)\n\t"
    "v_mfma_f32_16x16x32_bf16 v[12:15], v[144:147], v[132:135], v[12:15]\n\t"
    "s_waitcnt lgkmcnt(9)\n\t"
    "v_mfma_f32_16x16x32_bf16 v[16:19], v[144:147], v[136:139], v[16:19]\n\t"
    "s_waitcnt lgkmcnt(8)\n\t"
    "v_mfma_f32_16x16x32_bf16 v[20:23], v[144:147], v[140:143], v[20:23]\n\t"
    "s_waitcnt lgkmcnt(6)\n\t"
    "v_mfma_f32_16x16x32_bf16 v[24:27], v[148:151], v[120:123], v[24:27]\n\t"
    "s_waitcnt vmcnt(0)\n\t"
    "s_waitcnt lgkmcnt(0)\n\t"
    "s_barrier\n\t"
    "v_mfma_f32_16x16x32_bf16 v[28:31], v[148:151], v[124:127], v[28:31]\n\t"
    "v_mfma_f32_16x16x32_bf16 v[32:35], v[148:151], v[128:131], v[32:35]\n\t"
    "ds_read_b64_tr_b16 v[188:189], v244 offset:24576\n\t"
    "v_mfma_f32_16x16x32_bf16 v[36:39], v[148:151], v[132:135], v[36:39]\n\t"
    "ds_read_b64_tr_b16 v[190:191], v244 offset:30720\n\t"
    "v_mfma_f32_16x16x32_bf16 v[40:43], v[148:151], v[136:139], v[40:43]\n\t"
    "ds_read_b128 v[164:167], v248 offset:24576\n\t"
    "v_mfma_f32_16x16x32_bf16 v[44:47], v[148:151], v[140:143], v[44:47]\n\t"
    "v_mfma_f32_16x16x32_bf16 v[48:51], v[152:155], v[120:123], v[48:51]\n\t"
    "ds_read_b128 v[168:171], v248 offset:25600\n\t"
    "v_mfma_f32_16x16x32_bf16 v[52:55], v[152:155], v[124:127], v[52:55]\n\t"
    "ds_read_b128 v[172:175], v248 offset:26624\n\t"
    "v_mfma_f32_16x16x32_bf16 v[56:59], v[152:155], v[128:131], v[56:59]\n\t"
    "ds_read_b128 v[176:179], v248 offset:27648\n\t"
    "v_mfma_f32_16x16x32_bf16 v[60:63], v[152:155], v[132:135], v[60:63]\n\t"
    "v_mfma_f32_16x16x32_bf16 v[64:67], v[152:155], v[136:139], v[64:67]\n\t"
    "ds_read_b128 v[180:183], v248 offset:28672\n\t"
    "v_mfma_f32_16x16x32_bf16 v[68:71], v[152:155], v[140:143], v[68:71]\n\t"
    "ds_read_b128 v[184:187], v248 offset:29696\n\t"
    "v_mfma_f32_16x16x32_bf16 v[72:75], v[156:159], v[120:123], v[72:75]\n\t"
    "ds_read_b64_tr_b16 v[192:193], v245 offset:24576\n\t"
    "v_mfma_f32_16x16x32_bf16 v[76:79], v[156:159], v[124:127], v[76:79]\n\t"
    "v_mfma_f32_16x16x32_bf16 v[80:83], v[156:159], v[128:131], v[80:83]\n\t"
    "ds_read_b64_tr_b16 v[194:195], v245 offset:30720\n\t"
    "v_mfma_f32_16x16x32_bf16 v[84:87], v[156:159], v[132:135], v[84:87]\n\t"
    "ds_read_b64_tr_b16 v[196:197], v244 offset:25088\n\t"
    "v_mfma_f32_16x16x32_bf16 v[88:91], v[156:159], v[136:139], v[88:91]\n\t"
    "ds_read_b64_tr_b16 v[198:199], v244 offset:31232\n\t"
    "v_mfma_f32_16x16x32_bf16 v[92:95], v[156:159], v[140:143], v[92:95]\n\t"
    "v_mfma_f32_16x16x32_bf16 v[96:99], v[160:163], v[120:123], v[96:99]\n\t"
    "ds_read_b64_tr_b16 v[200:201], v245 offset:25088\n\t"
    "v_mfma_f32_16x16x32_bf16 v[100:103], v[160:163], v[124:127], v[100:103]\n\t"
    "ds_read_b64_tr_b16 v[202:203], v245 offset:31232\n\t"
    "v_mfma_f32_16x16x32_bf16 v[104:107], v[160:163], v[128:131], v[104:107]\n\t"
    "ds_read_b64_tr_b16 v[204:205], v244 offset:25600\n\t"
    "v_mfma_f32_16x16x32_bf16 v[108:111], v[160:163], v[132:135], v[108:111]\n\t"
    "v_mfma_f32_16x16x32_bf16 v[112:115], v[160:163], v[136:139], v[112:115]\n\t"
    "ds_read_b64_tr_b16 v[206:207], v244 offset:31744\n\t"
    "v_mfma_f32_16x16x32_bf16 v[116:119], v[160:163], v[140:143], v[116:119]\n\t"
    "s_waitcnt lgkmcnt(13)\n\t"
    "v_mfma_f32_16x16x32_bf16 v[0:3], v[188:191], v[164:167], v[0:3]\n\t"
    "s_waitcnt lgkmcnt(12)\n\t"
    "v_mfma_f32_16x16x32_bf16 v[4:7], v[188:191], v[168:171], v[4:7]\n\t"
    "s_waitcnt lgkmcnt(11)\n\t"
    "v_mfma_f32_16x16x32_bf16 v[8:11], v[188:191], v[172:175], v[8:11]\n\t"
    "s_waitcnt lgkmcnt(10)\n\t"
    "v_mfma_f32_16x16x32_bf16 v[12:15], v[188:191], v[176:179], v[12:15]\n\t"
    "s_waitcnt lgkmcnt(9)\n\t"
    "v_mfma_f32_16x16x32_bf16 v[16:19], v[188:191], v[180:183], v[16:19]\n\t"
    "s_waitcnt lgkmcnt(8)\n\t"
    "v_mfma_f32_16x16x32_bf16 v[20:23], v[188:191], v[184:187], v[20:23]\n\t"
    "s_waitcnt lgkmcnt(6)\n\t"
    "v_mfma_f32_16x16x32_bf16 v[24:27], v[192:195], v[164:167], v[24:27]\n\t"
    "s_waitcnt lgkmcnt(0)\n\t"
    "s_barrier\n\t"
    "v_mfma_f32_16x16x32_bf16 v[28:31], v[192:195], v[168:171], v[28:31]\n\t"
    "v_mfma_f32_16x16x32_bf16 v[32:35], v[192:195], v[172:175], v[32:35]\n\t"
    "ds_read_b64_tr_b16 v[144:145], v247 offset:0\n\t"
    "v_mfma_f32_16x16x32_bf16 v[36:39], v[192:195], v[176:179], v[36:39]\n\t"
    "ds_read_b64_tr_b16 v[146:147], v247 offset:6144\n\t"
    "v_mfma_f32_16x16x32_bf16 v[40:43], v[192:195], v[180:183], v[40:43]\n\t"
    "ds_read_b128 v[120:123], v250 offset:0\n\t"
    "v_mfma_f32_16x16x32_bf16 v[44:47], v[192:195], v[184:187], v[44:47]\n\t"
    "v_mfma_f32_16x16x32_bf16 v[48:51], v[196:199], v[164:167], v[48:51]\n\t"
    "ds_read_b128 v[124:127], v250 offset:1024\n\t"
    "v_mfma_f32_16x16x32_bf16 v[52:55], v[196:199], v[168:171], v[52:55]\n\t"
    "ds_read_b128 v[128:131], v250 offset:2048\n\t"
    "v_mfma_f32_16x16x32_bf16 v[56:59], v[196:199], v[172:175], v[56:59]\n\t"
    "ds_read_b128 v[132:135], v250 offset:3072\n\t"
    "v_mfma_f32_16x16x32_bf16 v[60:63], v[196:199], v[176:179], v[60:63]\n\t"
    "v_mfma_f32_16x16x32_bf16 v[64:67], v[196:199], v[180:183], v[64:67]\n\t"
    "ds_read_b128 v[136:139], v250 offset:4096\n\t"
    "v_mfma_f32_16x16x32_bf16 v[68:71], v[196:199], v[184:187], v[68:71]\n\t"
    "ds_read_b128 v[140:143], v250 offset:5120\n\t"
    "v_mfma_f32_16x16x32_bf16 v[72:75], v[200:203], v[164:167], v[72:75]\n\t"
    "ds_read_b64_tr_b16 v[148:149], v249 offset:0\n\t"
    "v_mfma_f32_16x16x32_bf16 v[76:79], v[200:203], v[168:171], v[76:79]\n\t"
    "v_mfma_f32_16x16x32_bf16 v[80:83], v[200:203], v[172:175], v[80:83]\n\t"
    "ds_read_b64_tr_b16 v[150:151], v249 offset:6144\n\t"
    "v_mfma_f32_16x16x32_bf16 v[84:87], v[200:203], v[176:179], v[84:87]\n\t"
    "ds_read_b64_tr_b16 v[152:153], v247 offset:512\n\t"
    "v_mfma_f32_16x16x32_bf16 v[88:91], v[200:203], v[180:183], v[88:91]\n\t"
    "ds_read_b64_tr_b16 v[154:155], v247 offset:6656\n\t"
    "v_mfma_f32_16x16x32_bf16 v[92:95], v[200:203], v[184:187], v[92:95]\n\t"
    "v_mfma_f32_16x16x32_bf16 v[96:99], v[204:207], v[164:167], v[96:99]\n\t"
    "ds_read_b64_tr_b16 v[156:157], v249 offset:512\n\t"
    "v_mfma_f32_16x16x32_bf16 v[100:103], v[204:207], v[168:171], v[100:103]\n\t"
    "ds_read_b64_tr_b16 v[158:159], v249 offset:6656\n\t"
    "v_mfma_f32_16x16x32_bf16 v[104:107], v[204:207], v[172:175], v[104:107]\n\t"
    "ds_read_b64_tr_b16 v[160:161], v247 offset:1024\n\t"
    "v_mfma_f32_16x16x32_bf16 v[108:111], v[204:207], v[176:179], v[108:111]\n\t"
    "v_mfma_f32_16x16x32_bf16 v[112:115], v[204:207], v[180:183], v[112:115]\n\t"
    "ds_read_b64_tr_b16 v[162:163], v247 offset:7168\n\t"
    "v_mfma_f32_16x16x32_bf16 v[116:119], v[204:207], v[184:187], v[116:119]\n\t"
    "s_lshr_b32 s79, s35, 3\n\t"
    "s_mul_i32 s84, s29, 7864320\n\t"
    "s_mul_i32 s87, s30, 983040\n\t"
    "s_add_u32 s84, s84, s87\n\t"
    "s_mul_i32 s87, s79, 245760\n\t"
    "s_add_u32 s84, s84, s87\n\t"
    "s_mul_i32 s87, s26, 61440\n\t"
    "s_add_u32 s84, s84, s87\n\t"
    "s_mul_i32 s87, s34, 15360\n\t"
    "s_add_u32 s84, s84, s87\n\t"
    "s_add_u32 s84, s84, 100663296\n\t"
    "s_add_u32 s56, s22, s84\n\t"
    "s_addc_u32 s57, s23, 0\n\t"
    "s_and_b32 s57, s57, 0xffff\n\t"
    "s_mov_b32 s58, 15360\n\t"
    "s_mov_b32 s59, 0x00020000\n\t"
    "s_mul_i32 s84, s29, 7864320\n\t"
    "s_mul_i32 s87, s30, 983040\n\t"
    "s_add_u32 s84, s84, s87\n\t"
    "s_mul_i32 s87, s79, 245760\n\t"
    "s_add_u32 s84, s84, s87\n\t"
    "s_mul_i32 s87, s34, 61440\n\t"
    "s_add_u32 s84, s84, s87\n\t"
    "s_add_u32 s84, s84, 100663296\n\t"
    "s_add_u32 s60, s22, s84\n\t"
    "s_addc_u32 s61, s23, 0\n\t"
    "s_and_b32 s61, s61, 0xffff\n\t"
    "s_mov_b32 s62, 61440\n\t"
    "s_mov_b32 s63, 0x00020000\n\t"
    "s_lshr_b32 s85, s34, 1\n\t"
    "s_and_b32 s86, s34, 1\n\t"
    "s_mul_i32 s84, s30, 491520\n\t"
    "s_mul_i32 s87, s85, 245760\n\t"
    "s_add_u32 s84, s84, s87\n\t"
    "s_mul_i32 s87, s29, 1280\n\t"
    "s_add_u32 s84, s84, s87\n\t"
    "s_mul_i32 s87, s79, 320\n\t"
    "s_add_u32 s84, s84, s87\n\t"
    "s_mul_i32 s87, s86, 160\n\t"
    "s_add_u32 s84, s84, s87\n\t"
    "s_add_u32 s44, s24, s84\n\t"
    "s_addc_u32 s45, s25, 0\n\t"
    "s_and_b32 s45, s45, 0xffff\n\t"
    "s_mov_b32 s46, 243360\n\t"
    "s_mov_b32 s47, 0x00020000\n\t"
    "s_mul_i32 s78, s29, 512\n\t"
    "s_mul_i32 s87, s30, 64\n\t"
    "s_add_u32 s78, s78, s87\n\t"
    "s_mul_i32 s87, s79, 16\n\t"
    "s_add_u32 s78, s78, s87\n\t"
    "s_add_u32 s78, s78, 4096\n\t"
    "s_mov_b32 s77, 6\n\t"
    "L_op_%=:\n\t"
    "s_waitcnt lgkmcnt(13)\n\t"
    "v_mfma_f32_16x16x32_bf16 v[0:3], v[144:147], v[120:123], v[0:3]\n\t"
    "s_waitcnt lgkmcnt(12)\n\t"
    "v_mfma_f32_16x16x32_bf16 v[4:7], v[144:147], v[124:127], v[4:7]\n\t"
    "s_waitcnt lgkmcnt(11)\n\t"
    "v_mfma_f32_16x16x32_bf16 v[8:11], v[144:147], v[128:131], v[8:11]\n\t"
    "s_waitcnt lgkmcnt(10)\n\t"
    "v_mfma_f32_16x16x32_bf16 v[12:15], v[144:147], v[132:135], v[12:15]\n\t"
    "s_waitcnt lgkmcnt(9)\n\t"
    "v_mfma_f32_16x16x32_bf16 v[16:19], v[144:147], v[136:139], v[16:19]\n\t"
    "s_waitcnt lgkmcnt(8)\n\t"
    "v_mfma_f32_16x16x32_bf16 v[20:23], v[144:147], v[140:143], v[20:23]\n\t"
    "s_waitcnt lgkmcnt(6)\n\t"
    "v_mfma_f32_16x16x32_bf16 v[24:27], v[148:151], v[120:123], v[24:27]\n\t"
    "s_waitcnt lgkmcnt(0)\n\t"
    "s_barrier\n\t"
    "v_mfma_f32_16x16x32_bf16 v[28:31], v[148:151], v[124:127], v[28:31]\n\t"
    "v_mfma_f32_16x16x32_bf16 v[32:35], v[148:151], v[128:131], v[32:35]\n\t"
    "ds_read_b64_tr_b16 v[188:189], v247 offset:24576\n\t"
    "v_mfma_f32_16x16x32_bf16 v[36:39], v[148:151], v[132:135], v[36:39]\n\t"
    "ds_read_b64_tr_b16 v[190:191], v247 offset:30720\n\t"
    "v_mfma_f32_16x16x32_bf16 v[40:43], v[148:151], v[136:139], v[40:43]\n\t"
    "ds_read_b128 v[164:167], v250 offset:24576\n\t"
    "v_mfma_f32_16x16x32_bf16 v[44:47], v[148:151], v[140:143], v[44:47]\n\t"
    "v_mfma_f32_16x16x32_bf16 v[48:51], v[152:155], v[120:123], v[48:51]\n\t"
    "ds_read_b128 v[168:171], v250 offset:25600\n\t"
    "v_mfma_f32_16x16x32_bf16 v[52:55], v[152:155], v[124:127], v[52:55]\n\t"
    "ds_read_b128 v[172:175], v250 offset:26624\n\t"
    "v_mfma_f32_16x16x32_bf16 v[56:59], v[152:155], v[128:131], v[56:59]\n\t"
    "ds_read_b128 v[176:179], v250 offset:27648\n\t"
    "v_mfma_f32_16x16x32_bf16 v[60:63], v[152:155], v[132:135], v[60:63]\n\t"
    "v_mfma_f32_16x16x32_bf16 v[64:67], v[152:155], v[136:139], v[64:67]\n\t"
    "ds_read_b128 v[180:183], v250 offset:28672\n\t"
    "v_mfma_f32_16x16x32_bf16 v[68:71], v[152:155], v[140:143], v[68:71]\n\t"
    "ds_read_b128 v[184:187], v250 offset:29696\n\t"
    "v_mfma_f32_16x16x32_bf16 v[72:75], v[156:159], v[120:123], v[72:75]\n\t"
    "ds_read_b64_tr_b16 v[192:193], v249 offset:24576\n\t"
    "v_mfma_f32_16x16x32_bf16 v[76:79], v[156:159], v[124:127], v[76:79]\n\t"
    "v_mfma_f32_16x16x32_bf16 v[80:83], v[156:159], v[128:131], v[80:83]\n\t"
    "ds_read_b64_tr_b16 v[194:195], v249 offset:30720\n\t"
    "v_mfma_f32_16x16x32_bf16 v[84:87], v[156:159], v[132:135], v[84:87]\n\t"
    "ds_read_b64_tr_b16 v[196:197], v247 offset:25088\n\t"
    "v_mfma_f32_16x16x32_bf16 v[88:91], v[156:159], v[136:139], v[88:91]\n\t"
    "ds_read_b64_tr_b16 v[198:199], v247 offset:31232\n\t"
    "v_mfma_f32_16x16x32_bf16 v[92:95], v[156:159], v[140:143], v[92:95]\n\t"
    "v_mfma_f32_16x16x32_bf16 v[96:99], v[160:163], v[120:123], v[96:99]\n\t"
    "ds_read_b64_tr_b16 v[200:201], v249 offset:25088\n\t"
    "v_mfma_f32_16x16x32_bf16 v[100:103], v[160:163], v[124:127], v[100:103]\n\t"
    "ds_read_b64_tr_b16 v[202:203], v249 offset:31232\n\t"
    "v_mfma_f32_16x16x32_bf16 v[104:107], v[160:163], v[128:131], v[104:107]\n\t"
    "ds_read_b64_tr_b16 v[204:205], v247 offset:25600\n\t"
    "v_mfma_f32_16x16x32_bf16 v[108:111], v[160:163], v[132:135], v[108:111]\n\t"
    "v_mfma_f32_16x16x32_bf16 v[112:115], v[160:163], v[136:139], v[112:115]\n\t"
    "ds_read_b64_tr_b16 v[206:207], v247 offset:31744\n\t"
    "v_mfma_f32_16x16x32_bf16 v[116:119], v[160:163], v[140:143], v[116:119]\n\t"
    "s_waitcnt lgkmcnt(13)\n\t"
    "v_mfma_f32_16x16x32_bf16 v[0:3], v[188:191], v[164:167], v[0:3]\n\t"
    "s_waitcnt lgkmcnt(12)\n\t"
    "v_mfma_f32_16x16x32_bf16 v[4:7], v[188:191], v[168:171], v[4:7]\n\t"
    "s_waitcnt lgkmcnt(11)\n\t"
    "v_mfma_f32_16x16x32_bf16 v[8:11], v[188:191], v[172:175], v[8:11]\n\t"
    "s_waitcnt lgkmcnt(10)\n\t"
    "v_mfma_f32_16x16x32_bf16 v[12:15], v[188:191], v[176:179], v[12:15]\n\t"
    "s_waitcnt lgkmcnt(9)\n\t"
    "v_mfma_f32_16x16x32_bf16 v[16:19], v[188:191], v[180:183], v[16:19]\n\t"
    "s_waitcnt lgkmcnt(8)\n\t"
    "v_mfma_f32_16x16x32_bf16 v[20:23], v[188:191], v[184:187], v[20:23]\n\t"
    "s_waitcnt lgkmcnt(6)\n\t"
    "v_mfma_f32_16x16x32_bf16 v[24:27], v[192:195], v[164:167], v[24:27]\n\t"
    "s_waitcnt lgkmcnt(0)\n\t"
    "s_barrier\n\t"
    "v_mfma_f32_16x16x32_bf16 v[28:31], v[192:195], v[168:171], v[28:31]\n\t"
    "v_mfma_f32_16x16x32_bf16 v[32:35], v[192:195], v[172:175], v[32:35]\n\t"
    "ds_read_b64_tr_b16 v[144:145], v244 offset:0\n\t"
    "v_mfma_f32_16x16x32_bf16 v[36:39], v[192:195], v[176:179], v[36:39]\n\t"
    "ds_read_b64_tr_b16 v[146:147], v244 offset:6144\n\t"
    "v_mfma_f32_16x16x32_bf16 v[40:43], v[192:195], v[180:183], v[40:43]\n\t"
    "ds_read_b128 v[120:123], v248 offset:0\n\t"
    "v_mfma_f32_16x16x32_bf16 v[44:47], v[192:195], v[184:187], v[44:47]\n\t"
    "v_mfma_f32_16x16x32_bf16 v[48:51], v[196:199], v[164:167], v[48:51]\n\t"
    "ds_read_b128 v[124:127], v248 offset:1024\n\t"
    "v_mfma_f32_16x16x32_bf16 v[52:55], v[196:199], v[168:171], v[52:55]\n\t"
    "ds_read_b128 v[128:131], v248 offset:2048\n\t"
    "v_mfma_f32_16x16x32_bf16 v[56:59], v[196:199], v[172:175], v[56:59]\n\t"
    "ds_read_b128 v[132:135], v248 offset:3072\n\t"
    "v_mfma_f32_16x16x32_bf16 v[60:63], v[196:199], v[176:179], v[60:63]\n\t"
    "v_mfma_f32_16x16x32_bf16 v[64:67], v[196:199], v[180:183], v[64:67]\n\t"
    "ds_read_b128 v[136:139], v248 offset:4096\n\t"
    "v_mfma_f32_16x16x32_bf16 v[68:71], v[196:199], v[184:187], v[68:71]\n\t"
    "ds_read_b128 v[140:143], v248 offset:5120\n\t"
    "v_mfma_f32_16x16x32_bf16 v[72:75], v[200:203], v[164:167], v[72:75]\n\t"
    "ds_read_b64_tr_b16 v[148:149], v245 offset:0\n\t"
    "v_mfma_f32_16x16x32_bf16 v[76:79], v[200:203], v[168:171], v[76:79]\n\t"
    "v_mfma_f32_16x16x32_bf16 v[80:83], v[200:203], v[172:175], v[80:83]\n\t"
    "ds_read_b64_tr_b16 v[150:151], v245 offset:6144\n\t"
    "v_mfma_f32_16x16x32_bf16 v[84:87], v[200:203], v[176:179], v[84:87]\n\t"
    "ds_read_b64_tr_b16 v[152:153], v244 offset:512\n\t"
    "v_mfma_f32_16x16x32_bf16 v[88:91], v[200:203], v[180:183], v[88:91]\n\t"
    "ds_read_b64_tr_b16 v[154:155], v244 offset:6656\n\t"
    "v_mfma_f32_16x16x32_bf16 v[92:95], v[200:203], v[184:187], v[92:95]\n\t"
    "v_mfma_f32_16x16x32_bf16 v[96:99], v[204:207], v[164:167], v[96:99]\n\t"
    "ds_read_b64_tr_b16 v[156:157], v245 offset:512\n\t"
    "v_mfma_f32_16x16x32_bf16 v[100:103], v[204:207], v[168:171], v[100:103]\n\t"
    "ds_read_b64_tr_b16 v[158:159], v245 offset:6656\n\t"
    "v_mfma_f32_16x16x32_bf16 v[104:107], v[204:207], v[172:175], v[104:107]\n\t"
    "ds_read_b64_tr_b16 v[160:161], v244 offset:1024\n\t"
    "v_mfma_f32_16x16x32_bf16 v[108:111], v[204:207], v[176:179], v[108:111]\n\t"
    "v_mfma_f32_16x16x32_bf16 v[112:115], v[204:207], v[180:183], v[112:115]\n\t"
    "ds_read_b64_tr_b16 v[162:163], v244 offset:7168\n\t"
    "v_mfma_f32_16x16x32_bf16 v[116:119], v[204:207], v[184:187], v[116:119]\n\t"
    "s_waitcnt lgkmcnt(13)\n\t"
    "v_mfma_f32_16x16x32_bf16 v[0:3], v[144:147], v[120:123], v[0:3]\n\t"
    "s_waitcnt lgkmcnt(12)\n\t"
    "v_mfma_f32_16x16x32_bf16 v[4:7], v[144:147], v[124:127], v[4:7]\n\t"
    "s_waitcnt lgkmcnt(11)\n\t"
    "v_mfma_f32_16x16x32_bf16 v[8:11], v[144:147], v[128:131], v[8:11]\n\t"
    "s_waitcnt lgkmcnt(10)\n\t"
    "v_mfma_f32_16x16x32_bf16 v[12:15], v[144:147], v[132:135], v[12:15]\n\t"
    "s_waitcnt lgkmcnt(9)\n\t"
    "v_mfma_f32_16x16x32_bf16 v[16:19], v[144:147], v[136:139], v[16:19]\n\t"
    "s_waitcnt lgkmcnt(8)\n\t"
    "v_mfma_f32_16x16x32_bf16 v[20:23], v[144:147], v[140:143], v[20:23]\n\t"
    "s_waitcnt lgkmcnt(6)\n\t"
    "v_mfma_f32_16x16x32_bf16 v[24:27], v[148:151], v[120:123], v[24:27]\n\t"
    "s_waitcnt lgkmcnt(0)\n\t"
    "s_barrier\n\t"
    "v_mfma_f32_16x16x32_bf16 v[28:31], v[148:151], v[124:127], v[28:31]\n\t"
    "v_mfma_f32_16x16x32_bf16 v[32:35], v[148:151], v[128:131], v[32:35]\n\t"
    "ds_read_b64_tr_b16 v[188:189], v244 offset:24576\n\t"
    "v_mfma_f32_16x16x32_bf16 v[36:39], v[148:151], v[132:135], v[36:39]\n\t"
    "ds_read_b64_tr_b16 v[190:191], v244 offset:30720\n\t"
    "v_mfma_f32_16x16x32_bf16 v[40:43], v[148:151], v[136:139], v[40:43]\n\t"
    "ds_read_b128 v[164:167], v248 offset:24576\n\t"
    "v_mfma_f32_16x16x32_bf16 v[44:47], v[148:151], v[140:143], v[44:47]\n\t"
    "v_mfma_f32_16x16x32_bf16 v[48:51], v[152:155], v[120:123], v[48:51]\n\t"
    "ds_read_b128 v[168:171], v248 offset:25600\n\t"
    "v_mfma_f32_16x16x32_bf16 v[52:55], v[152:155], v[124:127], v[52:55]\n\t"
    "ds_read_b128 v[172:175], v248 offset:26624\n\t"
    "v_mfma_f32_16x16x32_bf16 v[56:59], v[152:155], v[128:131], v[56:59]\n\t"
    "ds_read_b128 v[176:179], v248 offset:27648\n\t"
    "v_mfma_f32_16x16x32_bf16 v[60:63], v[152:155], v[132:135], v[60:63]\n\t"
    "v_mfma_f32_16x16x32_bf16 v[64:67], v[152:155], v[136:139], v[64:67]\n\t"
    "ds_read_b128 v[180:183], v248 offset:28672\n\t"
    "v_mfma_f32_16x16x32_bf16 v[68:71], v[152:155], v[140:143], v[68:71]\n\t"
    "ds_read_b128 v[184:187], v248 offset:29696\n\t"
    "v_mfma_f32_16x16x32_bf16 v[72:75], v[156:159], v[120:123], v[72:75]\n\t"
    "ds_read_b64_tr_b16 v[192:193], v245 offset:24576\n\t"
    "v_mfma_f32_16x16x32_bf16 v[76:79], v[156:159], v[124:127], v[76:79]\n\t"
    "v_mfma_f32_16x16x32_bf16 v[80:83], v[156:159], v[128:131], v[80:83]\n\t"
    "ds_read_b64_tr_b16 v[194:195], v245 offset:30720\n\t"
    "v_mfma_f32_16x16x32_bf16 v[84:87], v[156:159], v[132:135], v[84:87]\n\t"
    "ds_read_b64_tr_b16 v[196:197], v244 offset:25088\n\t"
    "v_mfma_f32_16x16x32_bf16 v[88:91], v[156:159], v[136:139], v[88:91]\n\t"
    "ds_read_b64_tr_b16 v[198:199], v244 offset:31232\n\t"
    "v_mfma_f32_16x16x32_bf16 v[92:95], v[156:159], v[140:143], v[92:95]\n\t"
    "v_mfma_f32_16x16x32_bf16 v[96:99], v[160:163], v[120:123], v[96:99]\n\t"
    "ds_read_b64_tr_b16 v[200:201], v245 offset:25088\n\t"
    "v_mfma_f32_16x16x32_bf16 v[100:103], v[160:163], v[124:127], v[100:103]\n\t"
    "ds_read_b64_tr_b16 v[202:203], v245 offset:31232\n\t"
    "v_mfma_f32_16x16x32_bf16 v[104:107], v[160:163], v[128:131], v[104:107]\n\t"
    "ds_read_b64_tr_b16 v[204:205], v244 offset:25600\n\t"
    "v_mfma_f32_16x16x32_bf16 v[108:111], v[160:163], v[132:135], v[108:111]\n\t"
    "v_mfma_f32_16x16x32_bf16 v[112:115], v[160:163], v[136:139], v[112:115]\n\t"
    "ds_read_b64_tr_b16 v[206:207], v244 offset:31744\n\t"
    "v_mfma_f32_16x16x32_bf16 v[116:119], v[160:163], v[140:143], v[116:119]\n\t"
    "s_waitcnt lgkmcnt(13)\n\t"
    "v_mfma_f32_16x16x32_bf16 v[0:3], v[188:191], v[164:167], v[0:3]\n\t"
    "s_waitcnt lgkmcnt(12)\n\t"
    "v_mfma_f32_16x16x32_bf16 v[4:7], v[188:191], v[168:171], v[4:7]\n\t"
    "s_waitcnt lgkmcnt(11)\n\t"
    "v_mfma_f32_16x16x32_bf16 v[8:11], v[188:191], v[172:175], v[8:11]\n\t"
    "s_waitcnt lgkmcnt(10)\n\t"
    "v_mfma_f32_16x16x32_bf16 v[12:15], v[188:191], v[176:179], v[12:15]\n\t"
    "s_waitcnt lgkmcnt(9)\n\t"
    "v_mfma_f32_16x16x32_bf16 v[16:19], v[188:191], v[180:183], v[16:19]\n\t"
    "s_waitcnt lgkmcnt(8)\n\t"
    "v_mfma_f32_16x16x32_bf16 v[20:23], v[188:191], v[184:187], v[20:23]\n\t"
    "s_waitcnt lgkmcnt(6)\n\t"
    "v_mfma_f32_16x16x32_bf16 v[24:27], v[192:195], v[164:167], v[24:27]\n\t"
    "s_waitcnt lgkmcnt(0)\n\t"
    "s_barrier\n\t"
    "v_mfma_f32_16x16x32_bf16 v[28:31], v[192:195], v[168:171], v[28:31]\n\t"
    "v_mfma_f32_16x16x32_bf16 v[32:35], v[192:195], v[172:175], v[32:35]\n\t"
    "ds_read_b64_tr_b16 v[144:145], v247 offset:0\n\t"
    "v_mfma_f32_16x16x32_bf16 v[36:39], v[192:195], v[176:179], v[36:39]\n\t"
    "ds_read_b64_tr_b16 v[146:147], v247 offset:6144\n\t"
    "v_mfma_f32_16x16x32_bf16 v[40:43], v[192:195], v[180:183], v[40:43]\n\t"
    "ds_read_b128 v[120:123], v250 offset:0\n\t"
    "v_mfma_f32_16x16x32_bf16 v[44:47], v[192:195], v[184:187], v[44:47]\n\t"
    "v_mfma_f32_16x16x32_bf16 v[48:51], v[196:199], v[164:167], v[48:51]\n\t"
    "ds_read_b128 v[124:127], v250 offset:1024\n\t"
    "v_mfma_f32_16x16x32_bf16 v[52:55], v[196:199], v[168:171], v[52:55]\n\t"
    "ds_read_b128 v[128:131], v250 offset:2048\n\t"
    "v_mfma_f32_16x16x32_bf16 v[56:59], v[196:199], v[172:175], v[56:59]\n\t"
    "ds_read_b128 v[132:135], v250 offset:3072\n\t"
    "v_mfma_f32_16x16x32_bf16 v[60:63], v[196:199], v[176:179], v[60:63]\n\t"
    "v_mfma_f32_16x16x32_bf16 v[64:67], v[196:199], v[180:183], v[64:67]\n\t"
    "ds_read_b128 v[136:139], v250 offset:4096\n\t"
    "v_mfma_f32_16x16x32_bf16 v[68:71], v[196:199], v[184:187], v[68:71]\n\t"
    "ds_read_b128 v[140:143], v250 offset:5120\n\t"
    "v_mfma_f32_16x16x32_bf16 v[72:75], v[200:203], v[164:167], v[72:75]\n\t"
    "ds_read_b64_tr_b16 v[148:149], v249 offset:0\n\t"
    "v_mfma_f32_16x16x32_bf16 v[76:79], v[200:203], v[168:171], v[76:79]\n\t"
    "v_mfma_f32_16x16x32_bf16 v[80:83], v[200:203], v[172:175], v[80:83]\n\t"
    "ds_read_b64_tr_b16 v[150:151], v249 offset:6144\n\t"
    "v_mfma_f32_16x16x32_bf16 v[84:87], v[200:203], v[176:179], v[84:87]\n\t"
    "ds_read_b64_tr_b16 v[152:153], v247 offset:512\n\t"
    "v_mfma_f32_16x16x32_bf16 v[88:91], v[200:203], v[180:183], v[88:91]\n\t"
    "ds_read_b64_tr_b16 v[154:155], v247 offset:6656\n\t"
    "v_mfma_f32_16x16x32_bf16 v[92:95], v[200:203], v[184:187], v[92:95]\n\t"
    "v_mfma_f32_16x16x32_bf16 v[96:99], v[204:207], v[164:167], v[96:99]\n\t"
    "ds_read_b64_tr_b16 v[156:157], v249 offset:512\n\t"
    "v_mfma_f32_16x16x32_bf16 v[100:103], v[204:207], v[168:171], v[100:103]\n\t"
    "ds_read_b64_tr_b16 v[158:159], v249 offset:6656\n\t"
    "v_mfma_f32_16x16x32_bf16 v[104:107], v[204:207], v[172:175], v[104:107]\n\t"
    "ds_read_b64_tr_b16 v[160:161], v247 offset:1024\n\t"
    "v_mfma_f32_16x16x32_bf16 v[108:111], v[204:207], v[176:179], v[108:111]\n\t"
    "v_mfma_f32_16x16x32_bf16 v[112:115], v[204:207], v[180:183], v[112:115]\n\t"
    "ds_read_b64_tr_b16 v[162:163], v247 offset:7168\n\t"
    "v_mfma_f32_16x16x32_bf16 v[116:119], v[204:207], v[184:187], v[116:119]\n\t"
    "s_sub_u32 s77, s77, 1\n\t"
    "s_cmp_lg_u32 s77, 0\n\t"
    "s_cbranch_scc1 L_op_%=\n\t"
    "s_waitcnt lgkmcnt(13)\n\t"
    "v_mfma_f32_16x16x32_bf16 v[0:3], v[144:147], v[120:123], v[0:3]\n\t"
    "s_waitcnt lgkmcnt(12)\n\t"
    "v_mfma_f32_16x16x32_bf16 v[4:7], v[144:147], v[124:127], v[4:7]\n\t"
    "s_waitcnt lgkmcnt(11)\n\t"
    "v_mfma_f32_16x16x32_bf16 v[8:11], v[144:147], v[128:131], v[8:11]\n\t"
    "s_waitcnt lgkmcnt(10)\n\t"
    "v_mfma_f32_16x16x32_bf16 v[12:15], v[144:147], v[132:135], v[12:15]\n\t"
    "s_waitcnt lgkmcnt(9)\n\t"
    "v_mfma_f32_16x16x32_bf16 v[16:19], v[144:147], v[136:139], v[16:19]\n\t"
    "s_waitcnt lgkmcnt(8)\n\t"
    "v_mfma_f32_16x16x32_bf16 v[20:23], v[144:147], v[140:143], v[20:23]\n\t"
    "s_waitcnt lgkmcnt(6)\n\t"
    "v_mfma_f32_16x16x32_bf16 v[24:27], v[148:151], v[120:123], v[24:27]\n\t"
    "s_waitcnt lgkmcnt(0)\n\t"
    "s_barrier\n\t"
    "v_mfma_f32_16x16x32_bf16 v[28:31], v[148:151], v[124:127], v[28:31]\n\t"
    "v_mfma_f32_16x16x32_bf16 v[32:35], v[148:151], v[128:131], v[32:35]\n\t"
    "ds_read_b64_tr_b16 v[188:189], v247 offset:24576\n\t"
    "v_mfma_f32_16x16x32_bf16 v[36:39], v[148:151], v[132:135], v[36:39]\n\t"
    "ds_read_b64_tr_b16 v[190:191], v247 offset:30720\n\t"
    "v_mfma_f32_16x16x32_bf16 v[40:43], v[148:151], v[136:139], v[40:43]\n\t"
    "ds_read_b128 v[164:167], v250 offset:24576\n\t"
    "v_mfma_f32_16x16x32_bf16 v[44:47], v[148:151], v[140:143], v[44:47]\n\t"
    "v_mfma_f32_16x16x32_bf16 v[48:51], v[152:155], v[120:123], v[48:51]\n\t"
    "ds_read_b128 v[168:171], v250 offset:25600\n\t"
    "v_mfma_f32_16x16x32_bf16 v[52:55], v[152:155], v[124:127], v[52:55]\n\t"
    "ds_read_b128 v[172:175], v250 offset:26624\n\t"
    "v_mfma_f32_16x16x32_bf16 v[56:59], v[152:155], v[128:131], v[56:59]\n\t"
    "ds_read_b128 v[176:179], v250 offset:27648\n\t"
    "v_mfma_f32_16x16x32_bf16 v[60:63], v[152:155], v[132:135], v[60:63]\n\t"
    "v_mfma_f32_16x16x32_bf16 v[64:67], v[152:155], v[136:139], v[64:67]\n\t"
    "ds_read_b128 v[180:183], v250 offset:28672\n\t"
    "v_mfma_f32_16x16x32_bf16 v[68:71], v[152:155], v[140:143], v[68:71]\n\t"
    "ds_read_b128 v[184:187], v250 offset:29696\n\t"
    "v_mfma_f32_16x16x32_bf16 v[72:75], v[156:159], v[120:123], v[72:75]\n\t"
    "ds_read_b64_tr_b16 v[192:193], v249 offset:24576\n\t"
    "v_mfma_f32_16x16x32_bf16 v[76:79], v[156:159], v[124:127], v[76:79]\n\t"
    "v_mfma_f32_16x16x32_bf16 v[80:83], v[156:159], v[128:131], v[80:83]\n\t"
    "ds_read_b64_tr_b16 v[194:195], v249 offset:30720\n\t"
    "v_mfma_f32_16x16x32_bf16 v[84:87], v[156:159], v[132:135], v[84:87]\n\t"
    "ds_read_b64_tr_b16 v[196:197], v247 offset:25088\n\t"
    "v_mfma_f32_16x16x32_bf16 v[88:91], v[156:159], v[136:139], v[88:91]\n\t"
    "ds_read_b64_tr_b16 v[198:199], v247 offset:31232\n\t"
    "v_mfma_f32_16x16x32_bf16 v[92:95], v[156:159], v[140:143], v[92:95]\n\t"
    "v_mfma_f32_16x16x32_bf16 v[96:99], v[160:163], v[120:123], v[96:99]\n\t"
    "ds_read_b64_tr_b16 v[200:201], v249 offset:25088\n\t"
    "v_mfma_f32_16x16x32_bf16 v[100:103], v[160:163], v[124:127], v[100:103]\n\t"
    "ds_read_b64_tr_b16 v[202:203], v249 offset:31232\n\t"
    "v_mfma_f32_16x16x32_bf16 v[104:107], v[160:163], v[128:131], v[104:107]\n\t"
    "ds_read_b64_tr_b16 v[204:205], v247 offset:25600\n\t"
    "v_mfma_f32_16x16x32_bf16 v[108:111], v[160:163], v[132:135], v[108:111]\n\t"
    "v_mfma_f32_16x16x32_bf16 v[112:115], v[160:163], v[136:139], v[112:115]\n\t"
    "ds_read_b64_tr_b16 v[206:207], v247 offset:31744\n\t"
    "v_mfma_f32_16x16x32_bf16 v[116:119], v[160:163], v[140:143], v[116:119]\n\t"
    "s_waitcnt lgkmcnt(13)\n\t"
    "v_mfma_f32_16x16x32_bf16 v[0:3], v[188:191], v[164:167], v[0:3]\n\t"
    "s_waitcnt lgkmcnt(12)\n\t"
    "v_mfma_f32_16x16x32_bf16 v[4:7], v[188:191], v[168:171], v[4:7]\n\t"
    "s_waitcnt lgkmcnt(11)\n\t"
    "v_mfma_f32_16x16x32_bf16 v[8:11], v[188:191], v[172:175], v[8:11]\n\t"
    "s_waitcnt lgkmcnt(10)\n\t"
    "v_mfma_f32_16x16x32_bf16 v[12:15], v[188:191], v[176:179], v[12:15]\n\t"
    "s_waitcnt lgkmcnt(9)\n\t"
    "v_mfma_f32_16x16x32_bf16 v[16:19], v[188:191], v[180:183], v[16:19]\n\t"
    "s_waitcnt lgkmcnt(8)\n\t"
    "v_mfma_f32_16x16x32_bf16 v[20:23], v[188:191], v[184:187], v[20:23]\n\t"
    "s_waitcnt lgkmcnt(6)\n\t"
    "v_mfma_f32_16x16x32_bf16 v[24:27], v[192:195], v[164:167], v[24:27]\n\t"
    "s_waitcnt lgkmcnt(0)\n\t"
    "s_barrier\n\t"
    "v_mfma_f32_16x16x32_bf16 v[28:31], v[192:195], v[168:171], v[28:31]\n\t"
    "v_mfma_f32_16x16x32_bf16 v[32:35], v[192:195], v[172:175], v[32:35]\n\t"
    "ds_read_b64_tr_b16 v[144:145], v244 offset:0\n\t"
    "v_mfma_f32_16x16x32_bf16 v[36:39], v[192:195], v[176:179], v[36:39]\n\t"
    "ds_read_b64_tr_b16 v[146:147], v244 offset:6144\n\t"
    "v_mfma_f32_16x16x32_bf16 v[40:43], v[192:195], v[180:183], v[40:43]\n\t"
    "ds_read_b128 v[120:123], v248 offset:0\n\t"
    "v_mfma_f32_16x16x32_bf16 v[44:47], v[192:195], v[184:187], v[44:47]\n\t"
    "v_mfma_f32_16x16x32_bf16 v[48:51], v[196:199], v[164:167], v[48:51]\n\t"
    "ds_read_b128 v[124:127], v248 offset:1024\n\t"
    "v_mfma_f32_16x16x32_bf16 v[52:55], v[196:199], v[168:171], v[52:55]\n\t"
    "ds_read_b128 v[128:131], v248 offset:2048\n\t"
    "v_mfma_f32_16x16x32_bf16 v[56:59], v[196:199], v[172:175], v[56:59]\n\t"
    "ds_read_b128 v[132:135], v248 offset:3072\n\t"
    "v_mfma_f32_16x16x32_bf16 v[60:63], v[196:199], v[176:179], v[60:63]\n\t"
    "v_mfma_f32_16x16x32_bf16 v[64:67], v[196:199], v[180:183], v[64:67]\n\t"
    "ds_read_b128 v[136:139], v248 offset:4096\n\t"
    "v_mfma_f32_16x16x32_bf16 v[68:71], v[196:199], v[184:187], v[68:71]\n\t"
    "ds_read_b128 v[140:143], v248 offset:5120\n\t"
    "v_mfma_f32_16x16x32_bf16 v[72:75], v[200:203], v[164:167], v[72:75]\n\t"
    "ds_read_b64_tr_b16 v[148:149], v245 offset:0\n\t"
    "v_mfma_f32_16x16x32_bf16 v[76:79], v[200:203], v[168:171], v[76:79]\n\t"
    "v_mfma_f32_16x16x32_bf16 v[80:83], v[200:203], v[172:175], v[80:83]\n\t"
    "ds_read_b64_tr_b16 v[150:151], v245 offset:6144\n\t"
    "v_mfma_f32_16x16x32_bf16 v[84:87], v[200:203], v[176:179], v[84:87]\n\t"
    "ds_read_b64_tr_b16 v[152:153], v244 offset:512\n\t"
    "v_mfma_f32_16x16x32_bf16 v[88:91], v[200:203], v[180:183], v[88:91]\n\t"
    "ds_read_b64_tr_b16 v[154:155], v244 offset:6656\n\t"
    "v_mfma_f32_16x16x32_bf16 v[92:95], v[200:203], v[184:187], v[92:95]\n\t"
    "v_mfma_f32_16x16x32_bf16 v[96:99], v[204:207], v[164:167], v[96:99]\n\t"
    "ds_read_b64_tr_b16 v[156:157], v245 offset:512\n\t"
    "v_mfma_f32_16x16x32_bf16 v[100:103], v[204:207], v[168:171], v[100:103]\n\t"
    "ds_read_b64_tr_b16 v[158:159], v245 offset:6656\n\t"
    "v_mfma_f32_16x16x32_bf16 v[104:107], v[204:207], v[172:175], v[104:107]\n\t"
    "ds_read_b64_tr_b16 v[160:161], v244 offset:1024\n\t"
    "v_mfma_f32_16x16x32_bf16 v[108:111], v[204:207], v[176:179], v[108:111]\n\t"
    "v_mfma_f32_16x16x32_bf16 v[112:115], v[204:207], v[180:183], v[112:115]\n\t"
    "ds_read_b64_tr_b16 v[162:163], v244 offset:7168\n\t"
    "v_mfma_f32_16x16x32_bf16 v[116:119], v[204:207], v[184:187], v[116:119]\n\t"
    "s_waitcnt lgkmcnt(13)\n\t"
    "v_mfma_f32_16x16x32_bf16 v[0:3], v[144:147], v[120:123], v[0:3]\n\t"
    "s_waitcnt lgkmcnt(12)\n\t"
    "v_mfma_f32_16x16x32_bf16 v[4:7], v[144:147], v[124:127], v[4:7]\n\t"
    "s_waitcnt lgkmcnt(11)\n\t"
    "v_mfma_f32_16x16x32_bf16 v[8:11], v[144:147], v[128:131], v[8:11]\n\t"
    "s_waitcnt lgkmcnt(10)\n\t"
    "v_mfma_f32_16x16x32_bf16 v[12:15], v[144:147], v[132:135], v[12:15]\n\t"
    "s_waitcnt lgkmcnt(9)\n\t"
    "v_mfma_f32_16x16x32_bf16 v[16:19], v[144:147], v[136:139], v[16:19]\n\t"
    "s_waitcnt lgkmcnt(8)\n\t"
    "v_mfma_f32_16x16x32_bf16 v[20:23], v[144:147], v[140:143], v[20:23]\n\t"
    "s_waitcnt lgkmcnt(6)\n\t"
    "v_mfma_f32_16x16x32_bf16 v[24:27], v[148:151], v[120:123], v[24:27]\n\t"
    "s_waitcnt lgkmcnt(0)\n\t"
    "s_barrier\n\t"
    "v_mfma_f32_16x16x32_bf16 v[28:31], v[148:151], v[124:127], v[28:31]\n\t"
    "v_mfma_f32_16x16x32_bf16 v[32:35], v[148:151], v[128:131], v[32:35]\n\t"
    "ds_read_b64_tr_b16 v[188:189], v244 offset:24576\n\t"
    "v_mfma_f32_16x16x32_bf16 v[36:39], v[148:151], v[132:135], v[36:39]\n\t"
    "ds_read_b64_tr_b16 v[190:191], v244 offset:30720\n\t"
    "v_mfma_f32_16x16x32_bf16 v[40:43], v[148:151], v[136:139], v[40:43]\n\t"
    "ds_read_b128 v[164:167], v248 offset:24576\n\t"
    "v_mfma_f32_16x16x32_bf16 v[44:47], v[148:151], v[140:143], v[44:47]\n\t"
    "v_mfma_f32_16x16x32_bf16 v[48:51], v[152:155], v[120:123], v[48:51]\n\t"
    "ds_read_b128 v[168:171], v248 offset:25600\n\t"
    "v_mfma_f32_16x16x32_bf16 v[52:55], v[152:155], v[124:127], v[52:55]\n\t"
    "ds_read_b128 v[172:175], v248 offset:26624\n\t"
    "v_mfma_f32_16x16x32_bf16 v[56:59], v[152:155], v[128:131], v[56:59]\n\t"
    "ds_read_b128 v[176:179], v248 offset:27648\n\t"
    "v_mfma_f32_16x16x32_bf16 v[60:63], v[152:155], v[132:135], v[60:63]\n\t"
    "v_mfma_f32_16x16x32_bf16 v[64:67], v[152:155], v[136:139], v[64:67]\n\t"
    "ds_read_b128 v[180:183], v248 offset:28672\n\t"
    "v_mfma_f32_16x16x32_bf16 v[68:71], v[152:155], v[140:143], v[68:71]\n\t"
    "ds_read_b128 v[184:187], v248 offset:29696\n\t"
    "v_mfma_f32_16x16x32_bf16 v[72:75], v[156:159], v[120:123], v[72:75]\n\t"
    "ds_read_b64_tr_b16 v[192:193], v245 offset:24576\n\t"
    "v_mfma_f32_16x16x32_bf16 v[76:79], v[156:159], v[124:127], v[76:79]\n\t"
    "v_mfma_f32_16x16x32_bf16 v[80:83], v[156:159], v[128:131], v[80:83]\n\t"
    "ds_read_b64_tr_b16 v[194:195], v245 offset:30720\n\t"
    "v_mfma_f32_16x16x32_bf16 v[84:87], v[156:159], v[132:135], v[84:87]\n\t"
    "ds_read_b64_tr_b16 v[196:197], v244 offset:25088\n\t"
    "v_mfma_f32_16x16x32_bf16 v[88:91], v[156:159], v[136:139], v[88:91]\n\t"
    "ds_read_b64_tr_b16 v[198:199], v244 offset:31232\n\t"
    "v_mfma_f32_16x16x32_bf16 v[92:95], v[156:159], v[140:143], v[92:95]\n\t"
    "v_mfma_f32_16x16x32_bf16 v[96:99], v[160:163], v[120:123], v[96:99]\n\t"
    "ds_read_b64_tr_b16 v[200:201], v245 offset:25088\n\t"
    "v_mfma_f32_16x16x32_bf16 v[100:103], v[160:163], v[124:127], v[100:103]\n\t"
    "ds_read_b64_tr_b16 v[202:203], v245 offset:31232\n\t"
    "v_mfma_f32_16x16x32_bf16 v[104:107], v[160:163], v[128:131], v[104:107]\n\t"
    "ds_read_b64_tr_b16 v[204:205], v244 offset:25600\n\t"
    "v_mfma_f32_16x16x32_bf16 v[108:111], v[160:163], v[132:135], v[108:111]\n\t"
    "v_mfma_f32_16x16x32_bf16 v[112:115], v[160:163], v[136:139], v[112:115]\n\t"
    "ds_read_b64_tr_b16 v[206:207], v244 offset:31744\n\t"
    "v_mfma_f32_16x16x32_bf16 v[116:119], v[160:163], v[140:143], v[116:119]\n\t"
    "s_cmp_eq_u32 s26, s34\n\t"
    "s_cbranch_scc1 L_own_%=\n\t"
    "s_waitcnt lgkmcnt(13)\n\t"
    "v_mfma_f32_16x16x32_bf16 v[0:3], v[188:191], v[164:167], v[0:3]\n\t"
    "s_waitcnt lgkmcnt(12)\n\t"
    "v_mfma_f32_16x16x32_bf16 v[4:7], v[188:191], v[168:171], v[4:7]\n\t"
    "s_waitcnt lgkmcnt(11)\n\t"
    "v_mfma_f32_16x16x32_bf16 v[8:11], v[188:191], v[172:175], v[8:11]\n\t"
    "s_waitcnt lgkmcnt(10)\n\t"
    "v_mfma_f32_16x16x32_bf16 v[12:15], v[188:191], v[176:179], v[12:15]\n\t"
    "s_waitcnt lgkmcnt(9)\n\t"
    "v_mfma_f32_16x16x32_bf16 v[16:19], v[188:191], v[180:183], v[16:19]\n\t"
    "s_waitcnt lgkmcnt(8)\n\t"
    "v_mfma_f32_16x16x32_bf16 v[20:23], v[188:191], v[184:187], v[20:23]\n\t"
    "v_cvt_pk_bf16_f32 v120, v0, v1\n\t"
    "v_cvt_pk_bf16_f32 v121, v2, v3\n\t"
    "v_cvt_pk_bf16_f32 v122, v4, v5\n\t"
    "s_waitcnt lgkmcnt(6)\n\t"
    "v_mfma_f32_16x16x32_bf16 v[24:27], v[192:195], v[164:167], v[24:27]\n\t"
    "v_cvt_pk_bf16_f32 v123, v6, v7\n\t"
    "s_mov_b32 s84, 0\n\t"
    "buffer_store_dwordx4 v[120:123], v252, s[56:59], s84 offen offset:0 sc1\n\t"
    "v_mfma_f32_16x16x32_bf16 v[28:31], v[192:195], v[168:171], v[28:31]\n\t"
    "v_cvt_pk_bf16_f32 v124, v8, v9\n\t"
    "v_cvt_pk_bf16_f32 v125, v10, v11\n\t"
    "v_cvt_pk_bf16_f32 v126, v12, v13\n\t"
    "v_mfma_f32_16x16x32_bf16 v[32:35], v[192:195], v[172:175], v[32:35]\n\t"
    "v_cvt_pk_bf16_f32 v127, v14, v15\n\t"
    "s_mov_b32 s84, 1024\n\t"
    "buffer_store_dwordx4 v[124:127], v252, s[56:59], s84 offen offset:0 sc1\n\t"
    "v_mfma_f32_16x16x32_bf16 v[36:39], v[192:195], v[176:179], v[36:39]\n\t"
    "v_cvt_pk_bf16_f32 v128, v16, v17\n\t"
    "v_cvt_pk_bf16_f32 v129, v18, v19\n\t"
    "v_cvt_pk_bf16_f32 v130, v20, v21\n\t"
    "v_mfma_f32_16x16x32_bf16 v[40:43], v[192:195], v[180:183], v[40:43]\n\t"
    "v_cvt_pk_bf16_f32 v131, v22, v23\n\t"
    "s_mov_b32 s84, 2048\n\t"
    "buffer_store_dwordx4 v[128:131], v252, s[56:59], s84 offen offset:0 sc1\n\t"
    "v_mfma_f32_16x16x32_bf16 v[44:47], v[192:195], v[184:187], v[44:47]\n\t"
    "v_cvt_pk_bf16_f32 v132, v24, v25\n\t"
    "v_cvt_pk_bf16_f32 v133, v26, v27\n\t"
    "v_cvt_pk_bf16_f32 v134, v28, v29\n\t"
    "s_waitcnt lgkmcnt(4)\n\t"
    "v_mfma_f32_16x16x32_bf16 v[48:51], v[196:199], v[164:167], v[48:51]\n\t"
    "v_cvt_pk_bf16_f32 v135, v30, v31\n\t"
    "s_mov_b32 s84, 3072\n\t"
    "buffer_store_dwordx4 v[132:135], v252, s[56:59], s84 offen offset:0 sc1\n\t"
    "v_mfma_f32_16x16x32_bf16 v[52:55], v[196:199], v[168:171], v[52:55]\n\t"
    "v_cvt_pk_bf16_f32 v136, v32, v33\n\t"
    "v_cvt_pk_bf16_f32 v137, v34, v35\n\t"
    "v_cvt_pk_bf16_f32 v138, v36, v37\n\t"
    "v_mfma_f32_16x16x32_bf16 v[56:59], v[196:199], v[172:175], v[56:59]\n\t"
    "v_cvt_pk_bf16_f32 v139, v38, v39\n\t"
    "s_mov_b32 s84, 4096\n\t"
    "buffer_store_dwordx4 v[136:139], v252, s[56:59], s84 offen offset:0 sc1\n\t"
    "v_mfma_f32_16x16x32_bf16 v[60:63], v[196:199], v[176:179], v[60:63]\n\t"
    "v_cvt_pk_bf16_f32 v140, v40, v41\n\t"
    "v_cvt_pk_bf16_f32 v141, v42, v43\n\t"
    "v_cvt_pk_bf16_f32 v142, v44, v45\n\t"
    "v_mfma_f32_16x16x32_bf16 v[64:67], v[196:199], v[180:183], v[64:67]\n\t"
    "v_cvt_pk_bf16_f32 v143, v46, v47\n\t"
    "s_mov_b32 s84, 5120\n\t"
    "buffer_store_dwordx4 v[140:143], v252, s[56:59], s84 offen offset:0 sc1\n\t"
    "v_mfma_f32_16x16x32_bf16 v[68:71], v[196:199], v[184:187], v[68:71]\n\t"
    "v_cvt_pk_bf16_f32 v144, v48, v49\n\t"
    "v_cvt_pk_bf16_f32 v145, v50, v51\n\t"
    "v_cvt_pk_bf16_f32 v146, v52, v53\n\t"
    "s_waitcnt lgkmcnt(2)\n\t"
    "v_mfma_f32_16x16x32_bf16 v[72:75], v[200:203], v[164:167], v[72:75]\n\t"
    "v_cvt_pk_bf16_f32 v147, v54, v55\n\t"
    "s_mov_b32 s84, 6144\n\t"
    "buffer_store_dwordx4 v[144:147], v252, s[56:59], s84 offen offset:0 sc1\n\t"
    "v_mfma_f32_16x16x32_bf16 v[76:79], v[200:203], v[168:171], v[76:79]\n\t"
    "v_cvt_pk_bf16_f32 v148, v56, v57\n\t"
    "v_cvt_pk_bf16_f32 v149, v58, v59\n\t"
    "v_cvt_pk_bf16_f32 v150, v60, v61\n\t"
    "v_mfma_f32_16x16x32_bf16 v[80:83], v[200:203], v[172:175], v[80:83]\n\t"
    "v_cvt_pk_bf16_f32 v151, v62, v63\n\t"
    "s_mov_b32 s84, 7168\n\t"
    "buffer_store_dwordx4 v[148:151], v252, s[56:59], s84 offen offset:0 sc1\n\t"
    "v_mfma_f32_16x16x32_bf16 v[84:87], v[200:203], v[176:179], v[84:87]\n\t"
    "v_cvt_pk_bf16_f32 v152, v64, v65\n\t"
    "v_cvt_pk_bf16_f32 v153, v66, v67\n\t"
    "v_cvt_pk_bf16_f32 v154, v68, v69\n\t"
    "v_mfma_f32_16x16x32_bf16 v[88:91], v[200:203], v[180:183], v[88:91]\n\t"
    "v_cvt_pk_bf16_f32 v155, v70, v71\n\t"
    "s_mov_b32 s84, 8192\n\t"
    "buffer_store_dwordx4 v[152:155], v252, s[56:59], s84 offen offset:0 sc1\n\t"
    "v_mfma_f32_16x16x32_bf16 v[92:95], v[200:203], v[184:187], v[92:95]\n\t"
    "v_cvt_pk_bf16_f32 v156, v72, v73\n\t"
    "v_cvt_pk_bf16_f32 v157, v74, v75\n\t"
    "v_cvt_pk_bf16_f32 v158, v76, v77\n\t"
    "s_waitcnt lgkmcnt(0)\n\t"
    "v_mfma_f32_16x16x32_bf16 v[96:99], v[204:207], v[164:167], v[96:99]\n\t"
    "v_cvt_pk_bf16_f32 v159, v78, v79\n\t"
    "s_mov_b32 s84, 9216\n\t"
    "buffer_store_dwordx4 v[156:159], v252, s[56:59], s84 offen offset:0 sc1\n\t"
    "v_mfma_f32_16x16x32_bf16 v[100:103], v[204:207], v[168:171], v[100:103]\n\t"
    "v_cvt_pk_bf16_f32 v160, v80, v81\n\t"
    "v_cvt_pk_bf16_f32 v161, v82, v83\n\t"
    "v_cvt_pk_bf16_f32 v162, v84, v85\n\t"
    "v_mfma_f32_16x16x32_bf16 v[104:107], v[204:207], v[172:175], v[104:107]\n\t"
    "v_cvt_pk_bf16_f32 v163, v86, v87\n\t"
    "s_mov_b32 s84, 10240\n\t"
    "buffer_store_dwordx4 v[160:163], v252, s[56:59], s84 offen offset:0 sc1\n\t"
    "v_mfma_f32_16x16x32_bf16 v[108:111], v[204:207], v[176:179], v[108:111]\n\t"
    "v_cvt_pk_bf16_f32 v120, v88, v89\n\t"
    "v_cvt_pk_bf16_f32 v121, v90, v91\n\t"
    "v_cvt_pk_bf16_f32 v122, v92, v93\n\t"
    "v_mfma_f32_16x16x32_bf16 v[112:115], v[204:207], v[180:183], v[112:115]\n\t"
    "v_cvt_pk_bf16_f32 v123, v94, v95\n\t"
    "s_mov_b32 s84, 11264\n\t"
    "buffer_store_dwordx4 v[120:123], v252, s[56:59], s84 offen offset:0 sc1\n\t"
    "v_mfma_f32_16x16x32_bf16 v[116:119], v[204:207], v[184:187], v[116:119]\n\t"
    "v_cvt_pk_bf16_f32 v124, v96, v97\n\t"
    "v_cvt_pk_bf16_f32 v125, v98, v99\n\t"
    "v_cvt_pk_bf16_f32 v126, v100, v101\n\t"
    "v_cvt_pk_bf16_f32 v127, v102, v103\n\t"
    "s_mov_b32 s84, 12288\n\t"
    "buffer_store_dwordx4 v[124:127], v252, s[56:59], s84 offen offset:0 sc1\n\t"
    "v_cvt_pk_bf16_f32 v128, v104, v105\n\t"
    "v_cvt_pk_bf16_f32 v129, v106, v107\n\t"
    "v_cvt_pk_bf16_f32 v130, v108, v109\n\t"
    "v_cvt_pk_bf16_f32 v131, v110, v111\n\t"
    "s_mov_b32 s84, 13312\n\t"
    "buffer_store_dwordx4 v[128:131], v252, s[56:59], s84 offen offset:0 sc1\n\t"
    "v_cvt_pk_bf16_f32 v132, v112, v113\n\t"
    "v_cvt_pk_bf16_f32 v133, v114, v115\n\t"
    "v_cvt_pk_bf16_f32 v134, v116, v117\n\t"
    "v_cvt_pk_bf16_f32 v135, v118, v119\n\t"
    "s_mov_b32 s84, 14336\n\t"
    "buffer_store_dwordx4 v[132:135], v252, s[56:59], s84 offen offset:0 sc1\n\t"
    "s_branch L_ownd_%=\n\t"
    "L_own_%=:\n\t"
    "s_waitcnt lgkmcnt(0)\n\t"
    "v_mfma_f32_16x16x32_bf16 v[0:3], v[188:191], v[164:167], v[0:3]\n\t"
    "v_mfma_f32_16x16x32_bf16 v[4:7], v[188:191], v[168:171], v[4:7]\n\t"
    "v_mfma_f32_16x16x32_bf16 v[8:11], v[188:191], v[172:175], v[8:11]\n\t"
    "v_mfma_f32_16x16x32_bf16 v[12:15], v[188:191], v[176:179], v[12:15]\n\t"
    "v_mfma_f32_16x16x32_bf16 v[16:19], v[188:191], v[180:183], v[16:19]\n\t"
    "v_mfma_f32_16x16x32_bf16 v[20:23], v[188:191], v[184:187], v[20:23]\n\t"
    "v_mfma_f32_16x16x32_bf16 v[24:27], v[192:195], v[164:167], v[24:27]\n\t"
    "v_mfma_f32_16x16x32_bf16 v[28:31], v[192:195], v[168:171], v[28:31]\n\t"
    "v_mfma_f32_16x16x32_bf16 v[32:35], v[192:195], v[172:175], v[32:35]\n\t"
    "v_mfma_f32_16x16x32_bf16 v[36:39], v[192:195], v[176:179], v[36:39]\n\t"
    "v_mfma_f32_16x16x32_bf16 v[40:43], v[192:195], v[180:183], v[40:43]\n\t"
    "v_mfma_f32_16x16x32_bf16 v[44:47], v[192:195], v[184:187], v[44:47]\n\t"
    "v_mfma_f32_16x16x32_bf16 v[48:51], v[196:199], v[164:167], v[48:51]\n\t"
    "v_mfma_f32_16x16x32_bf16 v[52:55], v[196:199], v[168:171], v[52:55]\n\t"
    "v_mfma_f32_16x16x32_bf16 v[56:59], v[196:199], v[172:175], v[56:59]\n\t"
    "v_mfma_f32_16x16x32_bf16 v[60:63], v[196:199], v[176:179], v[60:63]\n\t"
    "v_mfma_f32_16x16x32_bf16 v[64:67], v[196:199], v[180:183], v[64:67]\n\t"
    "v_mfma_f32_16x16x32_bf16 v[68:71], v[196:199], v[184:187], v[68:71]\n\t"
    "v_mfma_f32_16x16x32_bf16 v[72:75], v[200:203], v[164:167], v[72:75]\n\t"
    "v_mfma_f32_16x16x32_bf16 v[76:79], v[200:203], v[168:171], v[76:79]\n\t"
    "v_mfma_f32_16x16x32_bf16 v[80:83], v[200:203], v[172:175], v[80:83]\n\t"
    "v_mfma_f32_16x16x32_bf16 v[84:87], v[200:203], v[176:179], v[84:87]\n\t"
    "v_mfma_f32_16x16x32_bf16 v[88:91], v[200:203], v[180:183], v[88:91]\n\t"
    "v_mfma_f32_16x16x32_bf16 v[92:95], v[200:203], v[184:187], v[92:95]\n\t"
    "v_mfma_f32_16x16x32_bf16 v[96:99], v[204:207], v[164:167], v[96:99]\n\t"
    "v_mfma_f32_16x16x32_bf16 v[100:103], v[204:207], v[168:171], v[100:103]\n\t"
    "v_mfma_f32_16x16x32_bf16 v[104:107], v[204:207], v[172:175], v[104:107]\n\t"
    "v_mfma_f32_16x16x32_bf16 v[108:111], v[204:207], v[176:179], v[108:111]\n\t"
    "v_mfma_f32_16x16x32_bf16 v[112:115], v[204:207], v[180:183], v[112:115]\n\t"
    "v_mfma_f32_16x16x32_bf16 v[116:119], v[204:207], v[184:187], v[116:119]\n\t"
    "s_nop 7\n\t"
    "v_add_u32 v253, s27, v252\n\t"
    "ds_write_b128 v253, v[0:3] offset:0\n\t"
    "ds_write_b128 v253, v[4:7] offset:1024\n\t"
    "ds_write_b128 v253, v[8:11] offset:2048\n\t"
    "ds_write_b128 v253, v[12:15] offset:3072\n\t"
    "ds_write_b128 v253, v[16:19] offset:4096\n\t"
    "ds_write_b128 v253, v[20:23] offset:5120\n\t"
    "ds_write_b128 v253, v[24:27] offset:6144\n\t"
    "ds_write_b128 v253, v[28:31] offset:7168\n\t"
    "ds_write_b128 v253, v[32:35] offset:8192\n\t"
    "ds_write_b128 v253, v[36:39] offset:9216\n\t"
    "ds_write_b128 v253, v[40:43] offset:10240\n\t"
    "ds_write_b128 v253, v[44:47] offset:11264\n\t"
    "ds_write_b128 v253, v[48:51] offset:12288\n\t"
    "ds_write_b128 v253, v[52:55] offset:13312\n\t"
    "ds_write_b128 v253, v[56:59] offset:14336\n\t"
    "ds_write_b128 v253, v[60:63] offset:15360\n\t"
    "ds_write_b128 v253, v[64:67] offset:16384\n\t"
    "ds_write_b128 v253, v[68:71] offset:17408\n\t"
    "ds_write_b128 v253, v[72:75] offset:18432\n\t"
    "ds_write_b128 v253, v[76:79] offset:19456\n\t"
    "ds_write_b128 v253, v[80:83] offset:20480\n\t"
    "ds_write_b128 v253, v[84:87] offset:21504\n\t"
    "ds_write_b128 v253, v[88:91] offset:22528\n\t"
    "ds_write_b128 v253, v[92:95] offset:23552\n\t"
    "ds_write_b128 v253, v[96:99] offset:24576\n\t"
    "ds_write_b128 v253, v[100:103] offset:25600\n\t"
    "ds_write_b128 v253, v[104:107] offset:26624\n\t"
    "ds_write_b128 v253, v[108:111] offset:27648\n\t"
    "ds_write_b128 v253, v[112:115] offset:28672\n\t"
    "ds_write_b128 v253, v[116:119] offset:29696\n\t"
    "L_ownd_%=:\n\t"
    "s_waitcnt vmcnt(0)\n\t"
    "s_waitcnt lgkmcnt(0)\n\t"
    "s_barrier\n\t"
    "s_branch L_xch_%=\n\t"
    "L_xch_%=:\n\t"
    "s_setprio 0\n\t"
    "v_bfe_u32 v251, v252, 4, 4\n\t"
    "v_mul_u32_u24 v251, 2560, v251\n\t"
    "v_lshrrev_b32 v250, 8, v252\n\t"
    "v_lshl_add_u32 v251, v250, 3, v251\n\t"
    "v_bfe_u32 v253, v252, 4, 2\n\t"
    "v_lshlrev_b32 v253, 2, v253\n\t"
    "v_add_u32 v253, s78, v253\n\t"
    "s_lshl_b32 s84, s99, 11\n\t"
    "s_add_u32 s84, s84, s27\n\t"
    "v_add_u32 v250, s84, v252\n\t"
    "ds_read_b128 v[0:3], v250 offset:0\n\t"
    "ds_read_b128 v[4:7], v250 offset:1024\n\t"
    "ds_read_b128 v[32:35], v250 offset:16384\n\t"
    "ds_read_b128 v[36:39], v250 offset:17408\n\t"
    "s_or_b32 s84, s28, s99\n\t"
    "s_cmp_lg_u32 s84, 0\n\t"
    "s_cbranch_scc1 L_x2_%=\n\t"
    "buffer_load_dwordx4 v[64:67], v252, s[64:67], 0 offen sc1\n\t"
    "buffer_load_dwordx4 v[68:71], v252, s[64:67], 0 offen offset:1024 sc1\n\t"
    "L_x2_%=:\n\t"
    "s_mov_b32 s90, 0x40000\n\t"
    "L_x3_%=:\n\t"
    "buffer_load_dword v254, v253, s[64:67], 0 offen sc1\n\t"
    "s_waitcnt vmcnt(0)\n\t"
    "v_cmp_ne_u32 vcc, s89, v254\n\t"
    "s_cbranch_vccz L_x4_%=\n\t"
    "s_sleep 1\n\t"
    "s_sub_u32 s90, s90, 1\n\t"
    "s_cmp_lg_u32 s90, 0\n\t"
    "s_cbranch_scc1 L_x3_%=\n\t"
    "L_x4_%=:\n\t"
    "s_add_u32 s84, s34, 1\n\t"
    "s_and_b32 s84, s84, 3\n\t"
    "s_mul_i32 s85, s84, 15360\n\t"
    "s_mul_i32 s87, s99, 1024\n\t"
    "s_add_u32 s85, s85, s87\n\t"
    "buffer_load_dwordx4 v[8:11], v252, s[60:63], s85 offen offset:0 sc1\n\t"
    "s_add_u32 s84, s34, 2\n\t"
    "s_and_b32 s84, s84, 3\n\t"
    "s_mul_i32 s85, s84, 15360\n\t"
    "s_mul_i32 s87, s99, 1024\n\t"
    "s_add_u32 s85, s85, s87\n\t"
    "buffer_load_dwordx4 v[12:15], v252, s[60:63], s85 offen offset:0 sc1\n\t"
    "s_add_u32 s84, s34, 3\n\t"
    "s_and_b32 s84, s84, 3\n\t"
    "s_mul_i32 s85, s84, 15360\n\t"
    "s_mul_i32 s87, s99, 1024\n\t"
    "s_add_u32 s85, s85, s87\n\t"
    "buffer_load_dwordx4 v[16:19], v252, s[60:63], s85 offen offset:0 sc1\n\t"
    "s_add_u32 s84, s34, 1\n\t"
    "s_and_b32 s84, s84, 3\n\t"
    "s_mul_i32 s85, s84, 15360\n\t"
    "s_mul_i32 s87, s99, 1024\n\t"
    "s_add_u32 s85, s85, s87\n\t"
    "s_add_u32 s85, s85, 8192\n\t"
    "buffer_load_dwordx4 v[40:43], v252, s[60:63], s85 offen offset:0 sc1\n\t"
    "s_add_u32 s84, s34, 2\n\t"
    "s_and_b32 s84, s84, 3\n\t"
    "s_mul_i32 s85, s84, 15360\n\t"
    "s_mul_i32 s87, s99, 1024\n\t"
    "s_add_u32 s85, s85, s87\n\t"
    "s_add_u32 s85, s85, 8192\n\t"
    "buffer_load_dwordx4 v[44:47], v252, s[60:63], s85 offen offset:0 sc1\n\t"
    "s_add_u32 s84, s34, 3\n\t"
    "s_and_b32 s84, s84, 3\n\t"
    "s_mul_i32 s85, s84, 15360\n\t"
    "s_mul_i32 s87, s99, 1024\n\t"
    "s_add_u32 s85, s85, s87\n\t"
    "s_add_u32 s85, s85, 8192\n\t"
    "buffer_load_dwordx4 v[48:51], v252, s[60:63], s85 offen offset:0 sc1\n\t"
    "s_mov_b32 s97, 0xffff0000\n\t"
    "s_waitcnt vmcnt(5)\n\t"
    "v_lshlrev_b32 v20, 16, v8\n\t"
    "v_and_b32 v8, s97, v8\n\t"
    "s_waitcnt lgkmcnt(3)\n\t"
    "v_add_f32 v0, v0, v20\n\t"
    "v_add_f32 v1, v1, v8\n\t"
    "v_lshlrev_b32 v21, 16, v9\n\t"
    "v_and_b32 v9, s97, v9\n\t"
    "v_add_f32 v2, v2, v21\n\t"
    "v_add_f32 v3, v3, v9\n\t"
    "v_lshlrev_b32 v20, 16, v10\n\t"
    "v_and_b32 v10, s97, v10\n\t"
    "s_waitcnt lgkmcnt(2)\n\t"
    "v_add_f32 v4, v4, v20\n\t"
    "v_add_f32 v5, v5, v10\n\t"
    "v_lshlrev_b32 v21, 16, v11\n\t"
    "v_and_b32 v11, s97, v11\n\t"
    "v_add_f32 v6, v6, v21\n\t"
    "v_add_f32 v7, v7, v11\n\t"
    "s_waitcnt vmcnt(4)\n\t"
    "v_lshlrev_b32 v20, 16, v12\n\t"
    "v_and_b32 v12, s97, v12\n\t"
    "v_add_f32 v0, v0, v20\n\t"
    "v_add_f32 v1, v1, v12\n\t"
    "v_lshlrev_b32 v21, 16, v13\n\t"
    "v_and_b32 v13, s97, v13\n\t"
    "v_add_f32 v2, v2, v21\n\t"
    "v_add_f32 v3, v3, v13\n\t"
    "v_lshlrev_b32 v20, 16, v14\n\t"
    "v_and_b32 v14, s97, v14\n\t"
    "v_add_f32 v4, v4, v20\n\t"
    "v_add_f32 v5, v5, v14\n\t"
    "v_lshlrev_b32 v21, 16, v15\n\t"
    "v_and_b32 v15, s97, v15\n\t"
    "v_add_f32 v6, v6, v21\n\t"
    "v_add_f32 v7, v7, v15\n\t"
    "s_waitcnt vmcnt(3)\n\t"
    "v_lshlrev_b32 v20, 16, v16\n\t"
    "v_and_b32 v16, s97, v16\n\t"
    "v_add_f32 v0, v0, v20\n\t"
    "v_add_f32 v1, v1, v16\n\t"
    "v_lshlrev_b32 v21, 16, v17\n\t"
    "v_and_b32 v17, s97, v17\n\t"
    "v_add_f32 v2, v2, v21\n\t"
    "v_add_f32 v3, v3, v17\n\t"
    "v_lshlrev_b32 v20, 16, v18\n\t"
    "v_and_b32 v18, s97, v18\n\t"
    "v_add_f32 v4, v4, v20\n\t"
    "v_add_f32 v5, v5, v18\n\t"
    "v_lshlrev_b32 v21, 16, v19\n\t"
    "v_and_b32 v19, s97, v19\n\t"
    "v_add_f32 v6, v6, v21\n\t"
    "v_add_f32 v7, v7, v19\n\t"
    "s_lshl_b32 s84, s99, 1\n\t"
    "s_add_u32 s84, s84, 0\n\t"
    "s_mul_i32 s85, s84, 43\n\t"
    "s_lshr_b32 s85, s85, 8\n\t"
    "s_mul_i32 s86, s85, 6\n\t"
    "s_sub_u32 s86, s84, s86\n\t"
    "s_mul_i32 s86, s86, 40960\n\t"
    "s_lshl_b32 s85, s85, 5\n\t"
    "s_add_u32 s86, s86, s85\n\t"
    "v_cvt_pk_bf16_f32 v22, v0, v1\n\t"
    "v_cvt_pk_bf16_f32 v23, v2, v3\n\t"
    "buffer_store_dwordx2 v[22:23], v251, s[44:47], s86 offen offset:0\n\t"
    "s_lshl_b32 s84, s99, 1\n\t"
    "s_add_u32 s84, s84, 1\n\t"
    "s_mul_i32 s85, s84, 43\n\t"
    "s_lshr_b32 s85, s85, 8\n\t"
    "s_mul_i32 s86, s85, 6\n\t"
    "s_sub_u32 s86, s84, s86\n\t"
    "s_mul_i32 s86, s86, 40960\n\t"
    "s_lshl_b32 s85, s85, 5\n\t"
    "s_add_u32 s86, s86, s85\n\t"
    "v_cvt_pk_bf16_f32 v24, v4, v5\n\t"
    "v_cvt_pk_bf16_f32 v25, v6, v7\n\t"
    "buffer_store_dwordx2 v[24:25], v251, s[44:47], s86 offen offset:0\n\t"
    "s_waitcnt vmcnt(4)\n\t"
    "v_lshlrev_b32 v52, 16, v40\n\t"
    "v_and_b32 v40, s97, v40\n\t"
    "s_waitcnt lgkmcnt(1)\n\t"
    "v_add_f32 v32, v32, v52\n\t"
    "v_add_f32 v33, v33, v40\n\t"
    "v_lshlrev_b32 v53, 16, v41\n\t"
    "v_and_b32 v41, s97, v41\n\t"
    "v_add_f32 v34, v34, v53\n\t"
    "v_add_f32 v35, v35, v41\n\t"
    "v_lshlrev_b32 v52, 16, v42\n\t"
    "v_and_b32 v42, s97, v42\n\t"
    "s_waitcnt lgkmcnt(0)\n\t"
    "v_add_f32 v36, v36, v52\n\t"
    "v_add_f32 v37, v37, v42\n\t"
    "v_lshlrev_b32 v53, 16, v43\n\t"
    "v_and_b32 v43, s97, v43\n\t"
    "v_add_f32 v38, v38, v53\n\t"
    "v_add_f32 v39, v39, v43\n\t"
    "s_waitcnt vmcnt(3)\n\t"
    "v_lshlrev_b32 v52, 16, v44\n\t"
    "v_and_b32 v44, s97, v44\n\t"
    "v_add_f32 v32, v32, v52\n\t"
    "v_add_f32 v33, v33, v44\n\t"
    "v_lshlrev_b32 v53, 16, v45\n\t"
    "v_and_b32 v45, s97, v45\n\t"
    "v_add_f32 v34, v34, v53\n\t"
    "v_add_f32 v35, v35, v45\n\t"
    "v_lshlrev_b32 v52, 16, v46\n\t"
    "v_and_b32 v46, s97, v46\n\t"
    "v_add_f32 v36, v36, v52\n\t"
    "v_add_f32 v37, v37, v46\n\t"
    "v_lshlrev_b32 v53, 16, v47\n\t"
    "v_and_b32 v47, s97, v47\n\t"
    "v_add_f32 v38, v38, v53\n\t"
    "v_add_f32 v39, v39, v47\n\t"
    "s_waitcnt vmcnt(2)\n\t"
    "v_lshlrev_b32 v52, 16, v48\n\t"
    "v_and_b32 v48, s97, v48\n\t"
    "v_add_f32 v32, v32, v52\n\t"
    "v_add_f32 v33, v33, v48\n\t"
    "v_lshlrev_b32 v53, 16, v49\n\t"
    "v_and_b32 v49, s97, v49\n\t"
    "v_add_f32 v34, v34, v53\n\t"
    "v_add_f32 v35, v35, v49\n\t"
    "v_lshlrev_b32 v52, 16, v50\n\t"
    "v_and_b32 v50, s97, v50\n\t"
    "v_add_f32 v36, v36, v52\n\t"
    "v_add_f32 v37, v37, v50\n\t"
    "v_lshlrev_b32 v53, 16, v51\n\t"
    "v_and_b32 v51, s97, v51\n\t"
    "v_add_f32 v38, v38, v53\n\t"
    "v_add_f32 v39, v39, v51\n\t"
    "s_lshl_b32 s84, s99, 1\n\t"
    "s_add_u32 s84, s84, 16\n\t"
    "s_mul_i32 s85, s84, 43\n\t"
    "s_lshr_b32 s85, s85, 8\n\t"
    "s_mul_i32 s86, s85, 6\n\t"
    "s_sub_u32 s86, s84, s86\n\t"
    "s_mul_i32 s86, s86, 40960\n\t"
    "s_lshl_b32 s85, s85, 5\n\t"
    "s_add_u32 s86, s86, s85\n\t"
    "s_cmp_eq_u32 s99, 7\n\t"
    "s_cselect_b32 s86, 0x7f000000, s86\n\t"
    "v_cvt_pk_bf16_f32 v54, v32, v33\n\t"
    "v_cvt_pk_bf16_f32 v55, v34, v35\n\t"
    "buffer_store_dwordx2 v[54:55], v251, s[44:47], s86 offen offset:0\n\t"
    "s_lshl_b32 s84, s99, 1\n\t"
    "s_add_u32 s84, s84, 17\n\t"
    "s_mul_i32 s85, s84, 43\n\t"
    "s_lshr_b32 s85, s85, 8\n\t"
    "s_mul_i32 s86, s85, 6\n\t"
    "s_sub_u32 s86, s84, s86\n\t"
    "s_mul_i32 s86, s86, 40960\n\t"
    "s_lshl_b32 s85, s85, 5\n\t"
    "s_add_u32 s86, s86, s85\n\t"
    "s_cmp_eq_u32 s99, 7\n\t"
    "s_cselect_b32 s86, 0x7f000000, s86\n\t"
    "v_cvt_pk_bf16_f32 v56, v36, v37\n\t"
    "v_cvt_pk_bf16_f32 v57, v38, v39\n\t"
    "buffer_store_dwordx2 v[56:57], v251, s[44:47], s86 offen offset:0\n\t"
    "s_or_b32 s84, s28, s99\n\t"
    "s_cmp_lg_u32 s84, 0\n\t"
    "s_cbranch_scc1 L_end_%=\n\t"
    "s_mov_b32 s90, 0x20000\n\t"
    "L_eb_%=:\n\t"
    "v_xor_b32 v64, s89, v64\n\t"
    "v_xor_b32 v65, s89, v65\n\t"
    "v_xor_b32 v66, s89, v66\n\t"
    "v_xor_b32 v67, s89, v67\n\t"
    "v_xor_b32 v68, s89, v68\n\t"
    "v_xor_b32 v69, s89, v69\n\t"
    "v_xor_b32 v70, s89, v70\n\t"
    "v_xor_b32 v71, s89, v71\n\t"
    "v_or3_b32 v64, v64, v65, v66\n\t"
    "v_or3_b32 v67, v67, v68, v69\n\t"
    "v_or3_b32 v64, v64, v70, v71\n\t"
    "v_or_b32 v64, v64, v67\n\t"
    "v_cmp_ne_u32 vcc, 0, v64\n\t"
    "s_cbranch_vccz L_ed_%=\n\t"
    "s_sleep 4\n\t"
    "buffer_load_dwordx4 v[64:67], v252, s[64:67], 0 offen sc1\n\t"
    "buffer_load_dwordx4 v[68:71], v252, s[64:67], 0 offen offset:1024 sc1\n\t"
    "s_waitcnt vmcnt(0)\n\t"
    "s_sub_u32 s90, s90, 1\n\t"
    "s_cmp_lg_u32 s90, 0\n\t"
    "s_cbranch_scc1 L_eb_%=\n\t"
    "L_ed_%=:\n\t"
    "v_mov_b32 v72, s89\n\t"
    "s_mov_b32 s84, 8192\n\t"
    "buffer_store_dword v72, v252, s[64:67], s84 offen sc1\n\t"
    "s_branch L_end_%=\n\t"
    "L_hot_end_%=:\n\t"
    "L_pw0_%=:\n\t"
    "s_mov_b32 s90, 0x20000\n\t"
    "L_pl0_%=:\n\t"
    "buffer_load_dword v6, v7, s[64:67], 0 offen sc1\n\t"
    "s_waitcnt vmcnt(0)\n\t"
    "v_cmp_eq_u32 vcc, s89, v6\n\t"
    "s_mov_b32 s97, vcc_lo\n\t"
    "s_bitcmp1_b32 s97, 0\n\t"
    "s_cbranch_scc1 L_pr0_%=\n\t"
    "s_sleep 8\n\t"
    "s_sub_u32 s90, s90, 1\n\t"
    "s_cmp_lg_u32 s90, 0\n\t"
    "s_cbranch_scc1 L_pl0_%=\n\t"
    "s_mov_b32 s97, -1\n\t"
    "s_branch L_pr0_%=\n\t"
    "L_pw1_%=:\n\t"
    "s_mov_b32 s90, 0x20000\n\t"
    "L_pl1_%=:\n\t"
    "buffer_load_dword v6, v7, s[64:67], 0 offen sc1\n\t"
    "s_waitcnt vmcnt(0)\n\t"
    "v_cmp_eq_u32 vcc, s89, v6\n\t"
    "s_mov_b32 s97, vcc_lo\n\t"
    "s_bitcmp1_b32 s97, 1\n\t"
    "s_cbranch_scc1 L_pr1_%=\n\t"
    "s_sleep 8\n\t"
    "s_sub_u32 s90, s90, 1\n\t"
    "s_cmp_lg_u32 s90, 0\n\t"
    "s_cbranch_scc1 L_pl1_%=\n\t"
    "s_mov_b32 s97, -1\n\t"
    "s_branch L_pr1_%=\n\t"
    "L_pw2_%=:\n\t"
    "s_mov_b32 s90, 0x20000\n\t"
    "L_pl2_%=:\n\t"
    "buffer_load_dword v6, v7, s[64:67], 0 offen sc1\n\t"
    "s_waitcnt vmcnt(0)\n\t"
    "v_cmp_eq_u32 vcc, s89, v6\n\t"
    "s_mov_b32 s97, vcc_lo\n\t"
    "s_bitcmp1_b32 s97, 2\n\t"
    "s_cbranch_scc1 L_pr2_%=\n\t"
    "s_sleep 8\n\t"
    "s_sub_u32 s90, s90, 1\n\t"
    "s_cmp_lg_u32 s90, 0\n\t"
    "s_cbranch_scc1 L_pl2_%=\n\t"
    "s_mov_b32 s97, -1\n\t"
    "s_branch L_pr2_%=\n\t"
    "L_pw3_%=:\n\t"
    "s_mov_b32 s90, 0x20000\n\t"
    "L_pl3_%=:\n\t"
    "buffer_load_dword v6, v7, s[64:67], 0 offen sc1\n\t"
    "s_waitcnt vmcnt(0)\n\t"
    "v_cmp_eq_u32 vcc, s89, v6\n\t"
    "s_mov_b32 s97, vcc_lo\n\t"
    "s_bitcmp1_b32 s97, 3\n\t"
    "s_cbranch_scc1 L_pr3_%=\n\t"
    "s_sleep 8\n\t"
    "s_sub_u32 s90, s90, 1\n\t"
    "s_cmp_lg_u32 s90, 0\n\t"
    "s_cbranch_scc1 L_pl3_%=\n\t"
    "s_mov_b32 s97, -1\n\t"
    "s_branch L_pr3_%=\n\t"
    "L_pw4_%=:\n\t"
    "s_mov_b32 s90, 0x20000\n\t"
    "L_pl4_%=:\n\t"
    "buffer_load_dword v6, v7, s[64:67], 0 offen sc1\n\t"
    "s_waitcnt vmcnt(0)\n\t"
    "v_cmp_eq_u32 vcc, s89, v6\n\t"
    "s_mov_b32 s97, vcc_lo\n\t"
    "s_bitcmp1_b32 s97, 4\n\t"
    "s_cbranch_scc1 L_pr4_%=\n\t"
    "s_sleep 8\n\t"
    "s_sub_u32 s90, s90, 1\n\t"
    "s_cmp_lg_u32 s90, 0\n\t"
    "s_cbranch_scc1 L_pl4_%=\n\t"
    "s_mov_b32 s97, -1\n\t"
    "s_branch L_pr4_%=\n\t"
    "L_pw5_%=:\n\t"
    "s_mov_b32 s90, 0x20000\n\t"
    "L_pl5_%=:\n\t"
    "buffer_load_dword v6, v7, s[64:67], 0 offen sc1\n\t"
    "s_waitcnt vmcnt(0)\n\t"
    "v_cmp_eq_u32 vcc, s89, v6\n\t"
    "s_mov_b32 s97, vcc_lo\n\t"
    "s_bitcmp1_b32 s97, 5\n\t"
    "s_cbranch_scc1 L_pr5_%=\n\t"
    "s_sleep 8\n\t"
    "s_sub_u32 s90, s90, 1\n\t"
    "s_cmp_lg_u32 s90, 0\n\t"
    "s_cbranch_scc1 L_pl5_%=\n\t"
    "s_mov_b32 s97, -1\n\t"
    "s_branch L_pr5_%=\n\t"
    "L_pw6_%=:\n\t"
    "s_mov_b32 s90, 0x20000\n\t"
    "L_pl6_%=:\n\t"
    "buffer_load_dword v6, v7, s[64:67], 0 offen sc1\n\t"
    "s_waitcnt vmcnt(0)\n\t"
    "v_cmp_eq_u32 vcc, s89, v6\n\t"
    "s_mov_b32 s97, vcc_lo\n\t"
    "s_bitcmp1_b32 s97, 6\n\t"
    "s_cbranch_scc1 L_pr6_%=\n\t"
    "s_sleep 8\n\t"
    "s_sub_u32 s90, s90, 1\n\t"
    "s_cmp_lg_u32 s90, 0\n\t"
    "s_cbranch_scc1 L_pl6_%=\n\t"
    "s_mov_b32 s97, -1\n\t"
    "s_branch L_pr6_%=\n\t"
    "L_pw7_%=:\n\t"
    "s_mov_b32 s90, 0x20000\n\t"
    "L_pl7_%=:\n\t"
    "buffer_load_dword v6, v7, s[64:67], 0 offen sc1\n\t"
    "s_waitcnt vmcnt(0)\n\t"
    "v_cmp_eq_u32 vcc, s89, v6\n\t"
    "s_mov_b32 s97, vcc_lo\n\t"
    "s_bitcmp1_b32 s97, 7\n\t"
    "s_cbranch_scc1 L_pr7_%=\n\t"
    "s_sleep 8\n\t"
    "s_sub_u32 s90, s90, 1\n\t"
    "s_cmp_lg_u32 s90, 0\n\t"
    "s_cbranch_scc1 L_pl7_%=\n\t"
    "s_mov_b32 s97, -1\n\t"
    "s_branch L_pr7_%=\n\t"
    "L_rap1_%=:\n\t"
    "s_nop 7\n\t"
    "v_max3_f32 v239, v168, v169, v170\n\t"
    "v_max3_f32 v239, v239, v171, v172\n\t"
    "v_max3_f32 v239, v239, v173, v174\n\t"
    "v_max_f32 v239, v239, v175\n\t"
    "v_mov_b32 v240, v239\n\t"
    "s_nop 1\n\t"
    "v_permlane16_swap_b32 v239, v240\n\t"
    "v_max_f32 v239, v239, v240\n\t"
    "v_mov_b32 v240, v239\n\t"
    "s_nop 1\n\t"
    "v_permlane32_swap_b32 v239, v240\n\t"
    "v_max_f32 v239, v239, v240\n\t"
    "v_max_f32 v239, 0, v239\n\t"
    "v_exp_f32 v242, -v239\n\t"
    "v_sub_f32 v168, v168, v239\n\t"
    "v_sub_f32 v169, v169, v239\n\t"
    "v_sub_f32 v170, v170, v239\n\t"
    "v_sub_f32 v171, v171, v239\n\t"
    "v_sub_f32 v172, v172, v239\n\t"
    "v_sub_f32 v173, v173, v239\n\t"
    "v_sub_f32 v174, v174, v239\n\t"
    "v_sub_f32 v175, v175, v239\n\t"
    "v_sub_f32 v192, v192, v239\n\t"
    "v_sub_f32 v193, v193, v239\n\t"
    "v_sub_f32 v194, v194, v239\n\t"
    "v_sub_f32 v195, v195, v239\n\t"
    "v_mul_f32 v236, v236, v242\n\t"
    "v_max3_f32 v239, v176, v177, v178\n\t"
    "v_max3_f32 v239, v239, v179, v180\n\t"
    "v_max3_f32 v239, v239, v181, v182\n\t"
    "v_max_f32 v239, v239, v183\n\t"
    "v_mov_b32 v240, v239\n\t"
    "s_nop 1\n\t"
    "v_permlane16_swap_b32 v239, v240\n\t"
    "v_max_f32 v239, v239, v240\n\t"
    "v_mov_b32 v240, v239\n\t"
    "s_nop 1\n\t"
    "v_permlane32_swap_b32 v239, v240\n\t"
    "v_max_f32 v239, v239, v240\n\t"
    "v_max_f32 v239, 0, v239\n\t"
    "v_exp_f32 v243, -v239\n\t"
    "v_sub_f32 v176, v176, v239\n\t"
    "v_sub_f32 v177, v177, v239\n\t"
    "v_sub_f32 v178, v178, v239\n\t"
    "v_sub_f32 v179, v179, v239\n\t"
    "v_sub_f32 v180, v180, v239\n\t"
    "v_sub_f32 v181, v181, v239\n\t"
    "v_sub_f32 v182, v182, v239\n\t"
    "v_sub_f32 v183, v183, v239\n\t"
    "v_sub_f32 v196, v196, v239\n\t"
    "v_sub_f32 v197, v197, v239\n\t"
    "v_sub_f32 v198, v198, v239\n\t"
    "v_sub_f32 v199, v199, v239\n\t"
    "v_mul_f32 v237, v237, v243\n\t"
    "v_max3_f32 v239, v184, v185, v186\n\t"
    "v_max3_f32 v239, v239, v187, v188\n\t"
    "v_max3_f32 v239, v239, v189, v190\n\t"
    "v_max_f32 v239, v239, v191\n\t"
    "v_mov_b32 v240, v239\n\t"
    "s_nop 1\n\t"
    "v_permlane16_swap_b32 v239, v240\n\t"
    "v_max_f32 v239, v239, v240\n\t"
    "v_mov_b32 v240, v239\n\t"
    "s_nop 1\n\t"
    "v_permlane32_swap_b32 v239, v240\n\t"
    "v_max_f32 v239, v239, v240\n\t"
    "v_max_f32 v239, 0, v239\n\t"
    "v_exp_f32 v244, -v239\n\t"
    "v_sub_f32 v184, v184, v239\n\t"
    "v_sub_f32 v185, v185, v239\n\t"
    "v_sub_f32 v186, v186, v239\n\t"
    "v_sub_f32 v187, v187, v239\n\t"
    "v_sub_f32 v188, v188, v239\n\t"
    "v_sub_f32 v189, v189, v239\n\t"
    "v_sub_f32 v190, v190, v239\n\t"
    "v_sub_f32 v191, v191, v239\n\t"
    "v_sub_f32 v200, v200, v239\n\t"
    "v_sub_f32 v201, v201, v239\n\t"
    "v_sub_f32 v202, v202, v239\n\t"
    "v_sub_f32 v203, v203, v239\n\t"
    "v_mul_f32 v238, v238, v244\n\t"
    "s_mov_b32 s78, 1\n\t"
    "s_nop 1\n\t"
    "s_branch L_rarp1_%=\n\t"
    "L_rbp1_%=:\n\t"
    "s_nop 8\n\t"
    "v_mul_f32 v0, v0, v242\n\t"
    "v_mul_f32 v1, v1, v242\n\t"
    "v_mul_f32 v2, v2, v242\n\t"
    "v_mul_f32 v3, v3, v242\n\t"
    "v_mul_f32 v4, v4, v243\n\t"
    "v_mul_f32 v5, v5, v243\n\t"
    "v_mul_f32 v6, v6, v243\n\t"
    "v_mul_f32 v7, v7, v243\n\t"
    "v_mul_f32 v8, v8, v244\n\t"
    "v_mul_f32 v9, v9, v244\n\t"
    "v_mul_f32 v10, v10, v244\n\t"
    "v_mul_f32 v11, v11, v244\n\t"
    "v_mul_f32 v12, v12, v242\n\t"
    "v_mul_f32 v13, v13, v242\n\t"
    "v_mul_f32 v14, v14, v242\n\t"
    "v_mul_f32 v15, v15, v242\n\t"
    "v_mul_f32 v16, v16, v243\n\t"
    "v_mul_f32 v17, v17, v243\n\t"
    "v_mul_f32 v18, v18, v243\n\t"
    "v_mul_f32 v19, v19, v243\n\t"
    "v_mul_f32 v20, v20, v244\n\t"
    "v_mul_f32 v21, v21, v244\n\t"
    "v_mul_f32 v22, v22, v244\n\t"
    "v_mul_f32 v23, v23, v244\n\t"
    "v_mul_f32 v24, v24, v242\n\t"
    "v_mul_f32 v25, v25, v242\n\t"
    "v_mul_f32 v26, v26, v242\n\t"
    "v_mul_f32 v27, v27, v242\n\t"
    "v_mul_f32 v28, v28, v243\n\t"
    "v_mul_f32 v29, v29, v243\n\t"
    "v_mul_f32 v30, v30, v243\n\t"
    "v_mul_f32 v31, v31, v243\n\t"
    "v_mul_f32 v32, v32, v244\n\t"
    "v_mul_f32 v33, v33, v244\n\t"
    "v_mul_f32 v34, v34, v244\n\t"
    "v_mul_f32 v35, v35, v244\n\t"
    "v_mul_f32 v36, v36, v242\n\t"
    "v_mul_f32 v37, v37, v242\n\t"
    "v_mul_f32 v38, v38, v242\n\t"
    "v_mul_f32 v39, v39, v242\n\t"
    "v_mul_f32 v40, v40, v243\n\t"
    "v_mul_f32 v41, v41, v243\n\t"
    "v_mul_f32 v42, v42, v243\n\t"
    "v_mul_f32 v43, v43, v243\n\t"
    "v_mul_f32 v44, v44, v244\n\t"
    "v_mul_f32 v45, v45, v244\n\t"
    "v_mul_f32 v46, v46, v244\n\t"
    "v_mul_f32 v47, v47, v244\n\t"
    "v_mul_f32 v48, v48, v242\n\t"
    "v_mul_f32 v49, v49, v242\n\t"
    "v_mul_f32 v50, v50, v242\n\t"
    "v_mul_f32 v51, v51, v242\n\t"
    "v_mul_f32 v52, v52, v243\n\t"
    "v_mul_f32 v53, v53, v243\n\t"
    "v_mul_f32 v54, v54, v243\n\t"
    "v_mul_f32 v55, v55, v243\n\t"
    "v_mul_f32 v56, v56, v244\n\t"
    "v_mul_f32 v57, v57, v244\n\t"
    "v_mul_f32 v58, v58, v244\n\t"
    "v_mul_f32 v59, v59, v244\n\t"
    "v_mul_f32 v60, v60, v242\n\t"
    "v_mul_f32 v61, v61, v242\n\t"
    "v_mul_f32 v62, v62, v242\n\t"
    "v_mul_f32 v63, v63, v242\n\t"
    "v_mul_f32 v64, v64, v243\n\t"
    "v_mul_f32 v65, v65, v243\n\t"
    "v_mul_f32 v66, v66, v243\n\t"
    "v_mul_f32 v67, v67, v243\n\t"
    "v_mul_f32 v68, v68, v244\n\t"
    "v_mul_f32 v69, v69, v244\n\t"
    "v_mul_f32 v70, v70, v244\n\t"
    "v_mul_f32 v71, v71, v244\n\t"
    "v_mul_f32 v72, v72, v242\n\t"
    "v_mul_f32 v73, v73, v242\n\t"
    "v_mul_f32 v74, v74, v242\n\t"
    "v_mul_f32 v75, v75, v242\n\t"
    "v_mul_f32 v76, v76, v243\n\t"
    "v_mul_f32 v77, v77, v243\n\t"
    "v_mul_f32 v78, v78, v243\n\t"
    "v_mul_f32 v79, v79, v243\n\t"
    "v_mul_f32 v80, v80, v244\n\t"
    "v_mul_f32 v81, v81, v244\n\t"
    "v_mul_f32 v82, v82, v244\n\t"
    "v_mul_f32 v83, v83, v244\n\t"
    "v_mul_f32 v84, v84, v242\n\t"
    "v_mul_f32 v85, v85, v242\n\t"
    "v_mul_f32 v86, v86, v242\n\t"
    "v_mul_f32 v87, v87, v242\n\t"
    "v_mul_f32 v88, v88, v243\n\t"
    "v_mul_f32 v89, v89, v243\n\t"
    "v_mul_f32 v90, v90, v243\n\t"
    "v_mul_f32 v91, v91, v243\n\t"
    "v_mul_f32 v92, v92, v244\n\t"
    "v_mul_f32 v93, v93, v244\n\t"
    "v_mul_f32 v94, v94, v244\n\t"
    "v_mul_f32 v95, v95, v244\n\t"
    "s_mov_b32 s78, 0\n\t"
    "s_nop 1\n\t"
    "s_branch L_rbrp1_%=\n\t"
    "L_rap2_%=:\n\t"
    "s_nop 7\n\t"
    "v_max3_f32 v239, v144, v145, v146\n\t"
    "v_max3_f32 v239, v239, v147, v148\n\t"
    "v_max3_f32 v239, v239, v149, v150\n\t"
    "v_max_f32 v239, v239, v151\n\t"
    "v_mov_b32 v240, v239\n\t"
    "s_nop 1\n\t"
    "v_permlane16_swap_b32 v239, v240\n\t"
    "v_max_f32 v239, v239, v240\n\t"
    "v_mov_b32 v240, v239\n\t"
    "s_nop 1\n\t"
    "v_permlane32_swap_b32 v239, v240\n\t"
    "v_max_f32 v239, v239, v240\n\t"
    "v_max_f32 v239, 0, v239\n\t"
    "v_exp_f32 v242, -v239\n\t"
    "v_sub_f32 v144, v144, v239\n\t"
    "v_sub_f32 v145, v145, v239\n\t"
    "v_sub_f32 v146, v146, v239\n\t"
    "v_sub_f32 v147, v147, v239\n\t"
    "v_sub_f32 v148, v148, v239\n\t"
    "v_sub_f32 v149, v149, v239\n\t"
    "v_sub_f32 v150, v150, v239\n\t"
    "v_sub_f32 v151, v151, v239\n\t"
    "v_sub_f32 v192, v192, v239\n\t"
    "v_sub_f32 v193, v193, v239\n\t"
    "v_sub_f32 v194, v194, v239\n\t"
    "v_sub_f32 v195, v195, v239\n\t"
    "v_mul_f32 v236, v236, v242\n\t"
    "v_max3_f32 v239, v152, v153, v154\n\t"
    "v_max3_f32 v239, v239, v155, v156\n\t"
    "v_max3_f32 v239, v239, v157, v158\n\t"
    "v_max_f32 v239, v239, v159\n\t"
    "v_mov_b32 v240, v239\n\t"
    "s_nop 1\n\t"
    "v_permlane16_swap_b32 v239, v240\n\t"
    "v_max_f32 v239, v239, v240\n\t"
    "v_mov_b32 v240, v239\n\t"
    "s_nop 1\n\t"
    "v_permlane32_swap_b32 v239, v240\n\t"
    "v_max_f32 v239, v239, v240\n\t"
    "v_max_f32 v239, 0, v239\n\t"
    "v_exp_f32 v243, -v239\n\t"
    "v_sub_f32 v152, v152, v239\n\t"
    "v_sub_f32 v153, v153, v239\n\t"
    "v_sub_f32 v154, v154, v239\n\t"
    "v_sub_f32 v155, v155, v239\n\t"
    "v_sub_f32 v156, v156, v239\n\t"
    "v_sub_f32 v157, v157, v239\n\t"
    "v_sub_f32 v158, v158, v239\n\t"
    "v_sub_f32 v159, v159, v239\n\t"
    "v_sub_f32 v196, v196, v239\n\t"
    "v_sub_f32 v197, v197, v239\n\t"
    "v_sub_f32 v198, v198, v239\n\t"
    "v_sub_f32 v199, v199, v239\n\t"
    "v_mul_f32 v237, v237, v243\n\t"
    "v_max3_f32 v239, v160, v161, v162\n\t"
    "v_max3_f32 v239, v239, v163, v164\n\t"
    "v_max3_f32 v239, v239, v165, v166\n\t"
    "v_max_f32 v239, v239, v167\n\t"
    "v_mov_b32 v240, v239\n\t"
    "s_nop 1\n\t"
    "v_permlane16_swap_b32 v239, v240\n\t"
    "v_max_f32 v239, v239, v240\n\t"
    "v_mov_b32 v240, v239\n\t"
    "s_nop 1\n\t"
    "v_permlane32_swap_b32 v239, v240\n\t"
    "v_max_f32 v239, v239, v240\n\t"
    "v_max_f32 v239, 0, v239\n\t"
    "v_exp_f32 v244, -v239\n\t"
    "v_sub_f32 v160, v160, v239\n\t"
    "v_sub_f32 v161, v161, v239\n\t"
    "v_sub_f32 v162, v162, v239\n\t"
    "v_sub_f32 v163, v163, v239\n\t"
    "v_sub_f32 v164, v164, v239\n\t"
    "v_sub_f32 v165, v165, v239\n\t"
    "v_sub_f32 v166, v166, v239\n\t"
    "v_sub_f32 v167, v167, v239\n\t"
    "v_sub_f32 v200, v200, v239\n\t"
    "v_sub_f32 v201, v201, v239\n\t"
    "v_sub_f32 v202, v202, v239\n\t"
    "v_sub_f32 v203, v203, v239\n\t"
    "v_mul_f32 v238, v238, v244\n\t"
    "s_mov_b32 s78, 1\n\t"
    "s_nop 1\n\t"
    "s_branch L_rarp2_%=\n\t"
    "L_rbp2_%=:\n\t"
    "s_nop 8\n\t"
    "v_mul_f32 v0, v0, v242\n\t"
    "v_mul_f32 v1, v1, v242\n\t"
    "v_mul_f32 v2, v2, v242\n\t"
    "v_mul_f32 v3, v3, v242\n\t"
    "v_mul_f32 v4, v4, v243\n\t"
    "v_mul_f32 v5, v5, v243\n\t"
    "v_mul_f32 v6, v6, v243\n\t"
    "v_mul_f32 v7, v7, v243\n\t"
    "v_mul_f32 v8, v8, v244\n\t"
    "v_mul_f32 v9, v9, v244\n\t"
    "v_mul_f32 v10, v10, v244\n\t"
    "v_mul_f32 v11, v11, v244\n\t"
    "v_mul_f32 v12, v12, v242\n\t"
    "v_mul_f32 v13, v13, v242\n\t"
    "v_mul_f32 v14, v14, v242\n\t"
    "v_mul_f32 v15, v15, v242\n\t"
    "v_mul_f32 v16, v16, v243\n\t"
    "v_mul_f32 v17, v17, v243\n\t"
    "v_mul_f32 v18, v18, v243\n\t"
    "v_mul_f32 v19, v19, v243\n\t"
    "v_mul_f32 v20, v20, v244\n\t"
    "v_mul_f32 v21, v21, v244\n\t"
    "v_mul_f32 v22, v22, v244\n\t"
    "v_mul_f32 v23, v23, v244\n\t"
    "v_mul_f32 v24, v24, v242\n\t"
    "v_mul_f32 v25, v25, v242\n\t"
    "v_mul_f32 v26, v26, v242\n\t"
    "v_mul_f32 v27, v27, v242\n\t"
    "v_mul_f32 v28, v28, v243\n\t"
    "v_mul_f32 v29, v29, v243\n\t"
    "v_mul_f32 v30, v30, v243\n\t"
    "v_mul_f32 v31, v31, v243\n\t"
    "v_mul_f32 v32, v32, v244\n\t"
    "v_mul_f32 v33, v33, v244\n\t"
    "v_mul_f32 v34, v34, v244\n\t"
    "v_mul_f32 v35, v35, v244\n\t"
    "v_mul_f32 v36, v36, v242\n\t"
    "v_mul_f32 v37, v37, v242\n\t"
    "v_mul_f32 v38, v38, v242\n\t"
    "v_mul_f32 v39, v39, v242\n\t"
    "v_mul_f32 v40, v40, v243\n\t"
    "v_mul_f32 v41, v41, v243\n\t"
    "v_mul_f32 v42, v42, v243\n\t"
    "v_mul_f32 v43, v43, v243\n\t"
    "v_mul_f32 v44, v44, v244\n\t"
    "v_mul_f32 v45, v45, v244\n\t"
    "v_mul_f32 v46, v46, v244\n\t"
    "v_mul_f32 v47, v47, v244\n\t"
    "v_mul_f32 v48, v48, v242\n\t"
    "v_mul_f32 v49, v49, v242\n\t"
    "v_mul_f32 v50, v50, v242\n\t"
    "v_mul_f32 v51, v51, v242\n\t"
    "v_mul_f32 v52, v52, v243\n\t"
    "v_mul_f32 v53, v53, v243\n\t"
    "v_mul_f32 v54, v54, v243\n\t"
    "v_mul_f32 v55, v55, v243\n\t"
    "v_mul_f32 v56, v56, v244\n\t"
    "v_mul_f32 v57, v57, v244\n\t"
    "v_mul_f32 v58, v58, v244\n\t"
    "v_mul_f32 v59, v59, v244\n\t"
    "v_mul_f32 v60, v60, v242\n\t"
    "v_mul_f32 v61, v61, v242\n\t"
    "v_mul_f32 v62, v62, v242\n\t"
    "v_mul_f32 v63, v63, v242\n\t"
    "v_mul_f32 v64, v64, v243\n\t"
    "v_mul_f32 v65, v65, v243\n\t"
    "v_mul_f32 v66, v66, v243\n\t"
    "v_mul_f32 v67, v67, v243\n\t"
    "v_mul_f32 v68, v68, v244\n\t"
    "v_mul_f32 v69, v69, v244\n\t"
    "v_mul_f32 v70, v70, v244\n\t"
    "v_mul_f32 v71, v71, v244\n\t"
    "v_mul_f32 v72, v72, v242\n\t"
    "v_mul_f32 v73, v73, v242\n\t"
    "v_mul_f32 v74, v74, v242\n\t"
    "v_mul_f32 v75, v75, v242\n\t"
    "v_mul_f32 v76, v76, v243\n\t"
    "v_mul_f32 v77, v77, v243\n\t"
    "v_mul_f32 v78, v78, v243\n\t"
    "v_mul_f32 v79, v79, v243\n\t"
    "v_mul_f32 v80, v80, v244\n\t"
    "v_mul_f32 v81, v81, v244\n\t"
    "v_mul_f32 v82, v82, v244\n\t"
    "v_mul_f32 v83, v83, v244\n\t"
    "v_mul_f32 v84, v84, v242\n\t"
    "v_mul_f32 v85, v85, v242\n\t"
    "v_mul_f32 v86, v86, v242\n\t"
    "v_mul_f32 v87, v87, v242\n\t"
    "v_mul_f32 v88, v88, v243\n\t"
    "v_mul_f32 v89, v89, v243\n\t"
    "v_mul_f32 v90, v90, v243\n\t"
    "v_mul_f32 v91, v91, v243\n\t"
    "v_mul_f32 v92, v92, v244\n\t"
    "v_mul_f32 v93, v93, v244\n\t"
    "v_mul_f32 v94, v94, v244\n\t"
    "v_mul_f32 v95, v95, v244\n\t"
    "s_mov_b32 s78, 0\n\t"
    "s_nop 1\n\t"
    "s_branch L_rbrp2_%=\n\t"
    "L_ralo_%=:\n\t"
    "s_nop 7\n\t"
    "v_max3_f32 v239, v168, v169, v170\n\t"
    "v_max3_f32 v239, v239, v171, v172\n\t"
    "v_max3_f32 v239, v239, v173, v174\n\t"
    "v_max_f32 v239, v239, v175\n\t"
    "v_mov_b32 v240, v239\n\t"
    "s_nop 1\n\t"
    "v_permlane16_swap_b32 v239, v240\n\t"
    "v_max_f32 v239, v239, v240\n\t"
    "v_mov_b32 v240, v239\n\t"
    "s_nop 1\n\t"
    "v_permlane32_swap_b32 v239, v240\n\t"
    "v_max_f32 v239, v239, v240\n\t"
    "v_max_f32 v239, 0, v239\n\t"
    "v_exp_f32 v242, -v239\n\t"
    "v_sub_f32 v168, v168, v239\n\t"
    "v_sub_f32 v169, v169, v239\n\t"
    "v_sub_f32 v170, v170, v239\n\t"
    "v_sub_f32 v171, v171, v239\n\t"
    "v_sub_f32 v172, v172, v239\n\t"
    "v_sub_f32 v173, v173, v239\n\t"
    "v_sub_f32 v174, v174, v239\n\t"
    "v_sub_f32 v175, v175, v239\n\t"
    "v_sub_f32 v192, v192, v239\n\t"
    "v_sub_f32 v193, v193, v239\n\t"
    "v_sub_f32 v194, v194, v239\n\t"
    "v_sub_f32 v195, v195, v239\n\t"
    "v_mul_f32 v236, v236, v242\n\t"
    "v_max3_f32 v239, v176, v177, v178\n\t"
    "v_max3_f32 v239, v239, v179, v180\n\t"
    "v_max3_f32 v239, v239, v181, v182\n\t"
    "v_max_f32 v239, v239, v183\n\t"
    "v_mov_b32 v240, v239\n\t"
    "s_nop 1\n\t"
    "v_permlane16_swap_b32 v239, v240\n\t"
    "v_max_f32 v239, v239, v240\n\t"
    "v_mov_b32 v240, v239\n\t"
    "s_nop 1\n\t"
    "v_permlane32_swap_b32 v239, v240\n\t"
    "v_max_f32 v239, v239, v240\n\t"
    "v_max_f32 v239, 0, v239\n\t"
    "v_exp_f32 v243, -v239\n\t"
    "v_sub_f32 v176, v176, v239\n\t"
    "v_sub_f32 v177, v177, v239\n\t"
    "v_sub_f32 v178, v178, v239\n\t"
    "v_sub_f32 v179, v179, v239\n\t"
    "v_sub_f32 v180, v180, v239\n\t"
    "v_sub_f32 v181, v181, v239\n\t"
    "v_sub_f32 v182, v182, v239\n\t"
    "v_sub_f32 v183, v183, v239\n\t"
    "v_sub_f32 v196, v196, v239\n\t"
    "v_sub_f32 v197, v197, v239\n\t"
    "v_sub_f32 v198, v198, v239\n\t"
    "v_sub_f32 v199, v199, v239\n\t"
    "v_mul_f32 v237, v237, v243\n\t"
    "v_max3_f32 v239, v184, v185, v186\n\t"
    "v_max3_f32 v239, v239, v187, v188\n\t"
    "v_max3_f32 v239, v239, v189, v190\n\t"
    "v_max_f32 v239, v239, v191\n\t"
    "v_mov_b32 v240, v239\n\t"
    "s_nop 1\n\t"
    "v_permlane16_swap_b32 v239, v240\n\t"
    "v_max_f32 v239, v239, v240\n\t"
    "v_mov_b32 v240, v239\n\t"
    "s_nop 1\n\t"
    "v_permlane32_swap_b32 v239, v240\n\t"
    "v_max_f32 v239, v239, v240\n\t"
    "v_max_f32 v239, 0, v239\n\t"
    "v_exp_f32 v244, -v239\n\t"
    "v_sub_f32 v184, v184, v239\n\t"
    "v_sub_f32 v185, v185, v239\n\t"
    "v_sub_f32 v186, v186, v239\n\t"
    "v_sub_f32 v187, v187, v239\n\t"
    "v_sub_f32 v188, v188, v239\n\t"
    "v_sub_f32 v189, v189, v239\n\t"
    "v_sub_f32 v190, v190, v239\n\t"
    "v_sub_f32 v191, v191, v239\n\t"
    "v_sub_f32 v200, v200, v239\n\t"
    "v_sub_f32 v201, v201, v239\n\t"
    "v_sub_f32 v202, v202, v239\n\t"
    "v_sub_f32 v203, v203, v239\n\t"
    "v_mul_f32 v238, v238, v244\n\t"
    "s_mov_b32 s78, 1\n\t"
    "s_nop 1\n\t"
    "s_branch L_rarlo_%=\n\t"
    "L_rblo_%=:\n\t"
    "s_nop 8\n\t"
    "v_mul_f32 v0, v0, v242\n\t"
    "v_mul_f32 v1, v1, v242\n\t"
    "v_mul_f32 v2, v2, v242\n\t"
    "v_mul_f32 v3, v3, v242\n\t"
    "v_mul_f32 v4, v4, v243\n\t"
    "v_mul_f32 v5, v5, v243\n\t"
    "v_mul_f32 v6, v6, v243\n\t"
    "v_mul_f32 v7, v7, v243\n\t"
    "v_mul_f32 v8, v8, v244\n\t"
    "v_mul_f32 v9, v9, v244\n\t"
    "v_mul_f32 v10, v10, v244\n\t"
    "v_mul_f32 v11, v11, v244\n\t"
    "v_mul_f32 v12, v12, v242\n\t"
    "v_mul_f32 v13, v13, v242\n\t"
    "v_mul_f32 v14, v14, v242\n\t"
    "v_mul_f32 v15, v15, v242\n\t"
    "v_mul_f32 v16, v16, v243\n\t"
    "v_mul_f32 v17, v17, v243\n\t"
    "v_mul_f32 v18, v18, v243\n\t"
    "v_mul_f32 v19, v19, v243\n\t"
    "v_mul_f32 v20, v20, v244\n\t"
    "v_mul_f32 v21, v21, v244\n\t"
    "v_mul_f32 v22, v22, v244\n\t"
    "v_mul_f32 v23, v23, v244\n\t"
    "v_mul_f32 v24, v24, v242\n\t"
    "v_mul_f32 v25, v25, v242\n\t"
    "v_mul_f32 v26, v26, v242\n\t"
    "v_mul_f32 v27, v27, v242\n\t"
    "v_mul_f32 v28, v28, v243\n\t"
    "v_mul_f32 v29, v29, v243\n\t"
    "v_mul_f32 v30, v30, v243\n\t"
    "v_mul_f32 v31, v31, v243\n\t"
    "v_mul_f32 v32, v32, v244\n\t"
    "v_mul_f32 v33, v33, v244\n\t"
    "v_mul_f32 v34, v34, v244\n\t"
    "v_mul_f32 v35, v35, v244\n\t"
    "v_mul_f32 v36, v36, v242\n\t"
    "v_mul_f32 v37, v37, v242\n\t"
    "v_mul_f32 v38, v38, v242\n\t"
    "v_mul_f32 v39, v39, v242\n\t"
    "v_mul_f32 v40, v40, v243\n\t"
    "v_mul_f32 v41, v41, v243\n\t"
    "v_mul_f32 v42, v42, v243\n\t"
    "v_mul_f32 v43, v43, v243\n\t"
    "v_mul_f32 v44, v44, v244\n\t"
    "v_mul_f32 v45, v45, v244\n\t"
    "v_mul_f32 v46, v46, v244\n\t"
    "v_mul_f32 v47, v47, v244\n\t"
    "v_mul_f32 v48, v48, v242\n\t"
    "v_mul_f32 v49, v49, v242\n\t"
    "v_mul_f32 v50, v50, v242\n\t"
    "v_mul_f32 v51, v51, v242\n\t"
    "v_mul_f32 v52, v52, v243\n\t"
    "v_mul_f32 v53, v53, v243\n\t"
    "v_mul_f32 v54, v54, v243\n\t"
    "v_mul_f32 v55, v55, v243\n\t"
    "v_mul_f32 v56, v56, v244\n\t"
    "v_mul_f32 v57, v57, v244\n\t"
    "v_mul_f32 v58, v58, v244\n\t"
    "v_mul_f32 v59, v59, v244\n\t"
    "v_mul_f32 v60, v60, v242\n\t"
    "v_mul_f32 v61, v61, v242\n\t"
    "v_mul_f32 v62, v62, v242\n\t"
    "v_mul_f32 v63, v63, v242\n\t"
    "v_mul_f32 v64, v64, v243\n\t"
    "v_mul_f32 v65, v65, v243\n\t"
    "v_mul_f32 v66, v66, v243\n\t"
    "v_mul_f32 v67, v67, v243\n\t"
    "v_mul_f32 v68, v68, v244\n\t"
    "v_mul_f32 v69, v69, v244\n\t"
    "v_mul_f32 v70, v70, v244\n\t"
    "v_mul_f32 v71, v71, v244\n\t"
    "v_mul_f32 v72, v72, v242\n\t"
    "v_mul_f32 v73, v73, v242\n\t"
    "v_mul_f32 v74, v74, v242\n\t"
    "v_mul_f32 v75, v75, v242\n\t"
    "v_mul_f32 v76, v76, v243\n\t"
    "v_mul_f32 v77, v77, v243\n\t"
    "v_mul_f32 v78, v78, v243\n\t"
    "v_mul_f32 v79, v79, v243\n\t"
    "v_mul_f32 v80, v80, v244\n\t"
    "v_mul_f32 v81, v81, v244\n\t"
    "v_mul_f32 v82, v82, v244\n\t"
    "v_mul_f32 v83, v83, v244\n\t"
    "v_mul_f32 v84, v84, v242\n\t"
    "v_mul_f32 v85, v85, v242\n\t"
    "v_mul_f32 v86, v86, v242\n\t"
    "v_mul_f32 v87, v87, v242\n\t"
    "v_mul_f32 v88, v88, v243\n\t"
    "v_mul_f32 v89, v89, v243\n\t"
    "v_mul_f32 v90, v90, v243\n\t"
    "v_mul_f32 v91, v91, v243\n\t"
    "v_mul_f32 v92, v92, v244\n\t"
    "v_mul_f32 v93, v93, v244\n\t"
    "v_mul_f32 v94, v94, v244\n\t"
    "v_mul_f32 v95, v95, v244\n\t"
    "s_mov_b32 s78, 0\n\t"
    "s_nop 1\n\t"
    "s_branch L_rbrlo_%=\n\t"
    "L_rale_%=:\n\t"
    "s_nop 7\n\t"
    "v_max3_f32 v239, v144, v145, v146\n\t"
    "v_max3_f32 v239, v239, v147, v148\n\t"
    "v_max3_f32 v239, v239, v149, v150\n\t"
    "v_max_f32 v239, v239, v151\n\t"
    "v_mov_b32 v240, v239\n\t"
    "s_nop 1\n\t"
    "v_permlane16_swap_b32 v239, v240\n\t"
    "v_max_f32 v239, v239, v240\n\t"
    "v_mov_b32 v240, v239\n\t"
    "s_nop 1\n\t"
    "v_permlane32_swap_b32 v239, v240\n\t"
    "v_max_f32 v239, v239, v240\n\t"
    "v_max_f32 v239, 0, v239\n\t"
    "v_exp_f32 v242, -v239\n\t"
    "v_sub_f32 v144, v144, v239\n\t"
    "v_sub_f32 v145, v145, v239\n\t"
    "v_sub_f32 v146, v146, v239\n\t"
    "v_sub_f32 v147, v147, v239\n\t"
    "v_sub_f32 v148, v148, v239\n\t"
    "v_sub_f32 v149, v149, v239\n\t"
    "v_sub_f32 v150, v150, v239\n\t"
    "v_sub_f32 v151, v151, v239\n\t"
    "v_sub_f32 v192, v192, v239\n\t"
    "v_sub_f32 v193, v193, v239\n\t"
    "v_sub_f32 v194, v194, v239\n\t"
    "v_sub_f32 v195, v195, v239\n\t"
    "v_mul_f32 v236, v236, v242\n\t"
    "v_max3_f32 v239, v152, v153, v154\n\t"
    "v_max3_f32 v239, v239, v155, v156\n\t"
    "v_max3_f32 v239, v239, v157, v158\n\t"
    "v_max_f32 v239, v239, v159\n\t"
    "v_mov_b32 v240, v239\n\t"
    "s_nop 1\n\t"
    "v_permlane16_swap_b32 v239, v240\n\t"
    "v_max_f32 v239, v239, v240\n\t"
    "v_mov_b32 v240, v239\n\t"
    "s_nop 1\n\t"
    "v_permlane32_swap_b32 v239, v240\n\t"
    "v_max_f32 v239, v239, v240\n\t"
    "v_max_f32 v239, 0, v239\n\t"
    "v_exp_f32 v243, -v239\n\t"
    "v_sub_f32 v152, v152, v239\n\t"
    "v_sub_f32 v153, v153, v239\n\t"
    "v_sub_f32 v154, v154, v239\n\t"
    "v_sub_f32 v155, v155, v239\n\t"
    "v_sub_f32 v156, v156, v239\n\t"
    "v_sub_f32 v157, v157, v239\n\t"
    "v_sub_f32 v158, v158, v239\n\t"
    "v_sub_f32 v159, v159, v239\n\t"
    "v_sub_f32 v196, v196, v239\n\t"
    "v_sub_f32 v197, v197, v239\n\t"
    "v_sub_f32 v198, v198, v239\n\t"
    "v_sub_f32 v199, v199, v239\n\t"
    "v_mul_f32 v237, v237, v243\n\t"
    "v_max3_f32 v239, v160, v161, v162\n\t"
    "v_max3_f32 v239, v239, v163, v164\n\t"
    "v_max3_f32 v239, v239, v165, v166\n\t"
    "v_max_f32 v239, v239, v167\n\t"
    "v_mov_b32 v240, v239\n\t"
    "s_nop 1\n\t"
    "v_permlane16_swap_b32 v239, v240\n\t"
    "v_max_f32 v239, v239, v240\n\t"
    "v_mov_b32 v240, v239\n\t"
    "s_nop 1\n\t"
    "v_permlane32_swap_b32 v239, v240\n\t"
    "v_max_f32 v239, v239, v240\n\t"
    "v_max_f32 v239, 0, v239\n\t"
    "v_exp_f32 v244, -v239\n\t"
    "v_sub_f32 v160, v160, v239\n\t"
    "v_sub_f32 v161, v161, v239\n\t"
    "v_sub_f32 v162, v162, v239\n\t"
    "v_sub_f32 v163, v163, v239\n\t"
    "v_sub_f32 v164, v164, v239\n\t"
    "v_sub_f32 v165, v165, v239\n\t"
    "v_sub_f32 v166, v166, v239\n\t"
    "v_sub_f32 v167, v167, v239\n\t"
    "v_sub_f32 v200, v200, v239\n\t"
    "v_sub_f32 v201, v201, v239\n\t"
    "v_sub_f32 v202, v202, v239\n\t"
    "v_sub_f32 v203, v203, v239\n\t"
    "v_mul_f32 v238, v238, v244\n\t"
    "s_mov_b32 s78, 1\n\t"
    "s_nop 1\n\t"
    "s_branch L_rarle_%=\n\t"
    "L_rble_%=:\n\t"
    "s_nop 8\n\t"
    "v_mul_f32 v0, v0, v242\n\t"
    "v_mul_f32 v1, v1, v242\n\t"
    "v_mul_f32 v2, v2, v242\n\t"
    "v_mul_f32 v3, v3, v242\n\t"
    "v_mul_f32 v4, v4, v243\n\t"
    "v_mul_f32 v5, v5, v243\n\t"
    "v_mul_f32 v6, v6, v243\n\t"
    "v_mul_f32 v7, v7, v243\n\t"
    "v_mul_f32 v8, v8, v244\n\t"
    "v_mul_f32 v9, v9, v244\n\t"
    "v_mul_f32 v10, v10, v244\n\t"
    "v_mul_f32 v11, v11, v244\n\t"
    "v_mul_f32 v12, v12, v242\n\t"
    "v_mul_f32 v13, v13, v242\n\t"
    "v_mul_f32 v14, v14, v242\n\t"
    "v_mul_f32 v15, v15, v242\n\t"
    "v_mul_f32 v16, v16, v243\n\t"
    "v_mul_f32 v17, v17, v243\n\t"
    "v_mul_f32 v18, v18, v243\n\t"
    "v_mul_f32 v19, v19, v243\n\t"
    "v_mul_f32 v20, v20, v244\n\t"
    "v_mul_f32 v21, v21, v244\n\t"
    "v_mul_f32 v22, v22, v244\n\t"
    "v_mul_f32 v23, v23, v244\n\t"
    "v_mul_f32 v24, v24, v242\n\t"
    "v_mul_f32 v25, v25, v242\n\t"
    "v_mul_f32 v26, v26, v242\n\t"
    "v_mul_f32 v27, v27, v242\n\t"
    "v_mul_f32 v28, v28, v243\n\t"
    "v_mul_f32 v29, v29, v243\n\t"
    "v_mul_f32 v30, v30, v243\n\t"
    "v_mul_f32 v31, v31, v243\n\t"
    "v_mul_f32 v32, v32, v244\n\t"
    "v_mul_f32 v33, v33, v244\n\t"
    "v_mul_f32 v34, v34, v244\n\t"
    "v_mul_f32 v35, v35, v244\n\t"
    "v_mul_f32 v36, v36, v242\n\t"
    "v_mul_f32 v37, v37, v242\n\t"
    "v_mul_f32 v38, v38, v242\n\t"
    "v_mul_f32 v39, v39, v242\n\t"
    "v_mul_f32 v40, v40, v243\n\t"
    "v_mul_f32 v41, v41, v243\n\t"
    "v_mul_f32 v42, v42, v243\n\t"
    "v_mul_f32 v43, v43, v243\n\t"
    "v_mul_f32 v44, v44, v244\n\t"
    "v_mul_f32 v45, v45, v244\n\t"
    "v_mul_f32 v46, v46, v244\n\t"
    "v_mul_f32 v47, v47, v244\n\t"
    "v_mul_f32 v48, v48, v242\n\t"
    "v_mul_f32 v49, v49, v242\n\t"
    "v_mul_f32 v50, v50, v242\n\t"
    "v_mul_f32 v51, v51, v242\n\t"
    "v_mul_f32 v52, v52, v243\n\t"
    "v_mul_f32 v53, v53, v243\n\t"
    "v_mul_f32 v54, v54, v243\n\t"
    "v_mul_f32 v55, v55, v243\n\t"
    "v_mul_f32 v56, v56, v244\n\t"
    "v_mul_f32 v57, v57, v244\n\t"
    "v_mul_f32 v58, v58, v244\n\t"
    "v_mul_f32 v59, v59, v244\n\t"
    "v_mul_f32 v60, v60, v242\n\t"
    "v_mul_f32 v61, v61, v242\n\t"
    "v_mul_f32 v62, v62, v242\n\t"
    "v_mul_f32 v63, v63, v242\n\t"
    "v_mul_f32 v64, v64, v243\n\t"
    "v_mul_f32 v65, v65, v243\n\t"
    "v_mul_f32 v66, v66, v243\n\t"
    "v_mul_f32 v67, v67, v243\n\t"
    "v_mul_f32 v68, v68, v244\n\t"
    "v_mul_f32 v69, v69, v244\n\t"
    "v_mul_f32 v70, v70, v244\n\t"
    "v_mul_f32 v71, v71, v244\n\t"
    "v_mul_f32 v72, v72, v242\n\t"
    "v_mul_f32 v73, v73, v242\n\t"
    "v_mul_f32 v74, v74, v242\n\t"
    "v_mul_f32 v75, v75, v242\n\t"
    "v_mul_f32 v76, v76, v243\n\t"
    "v_mul_f32 v77, v77, v243\n\t"
    "v_mul_f32 v78, v78, v243\n\t"
    "v_mul_f32 v79, v79, v243\n\t"
    "v_mul_f32 v80, v80, v244\n\t"
    "v_mul_f32 v81, v81, v244\n\t"
    "v_mul_f32 v82, v82, v244\n\t"
    "v_mul_f32 v83, v83, v244\n\t"
    "v_mul_f32 v84, v84, v242\n\t"
    "v_mul_f32 v85, v85, v242\n\t"
    "v_mul_f32 v86, v86, v242\n\t"
    "v_mul_f32 v87, v87, v242\n\t"
    "v_mul_f32 v88, v88, v243\n\t"
    "v_mul_f32 v89, v89, v243\n\t"
    "v_mul_f32 v90, v90, v243\n\t"
    "v_mul_f32 v91, v91, v243\n\t"
    "v_mul_f32 v92, v92, v244\n\t"
    "v_mul_f32 v93, v93, v244\n\t"
    "v_mul_f32 v94, v94, v244\n\t"
    "v_mul_f32 v95, v95, v244\n\t"
    "s_mov_b32 s78, 0\n\t"
    "s_nop 1\n\t"
    "s_branch L_rbrle_%=\n\t"
    "L_rap15_%=:\n\t"
    "s_nop 7\n\t"
    "v_max3_f32 v239, v168, v169, v170\n\t"
    "v_max3_f32 v239, v239, v171, v172\n\t"
    "v_max3_f32 v239, v239, v173, v174\n\t"
    "v_max_f32 v239, v239, v175\n\t"
    "v_mov_b32 v240, v239\n\t"
    "s_nop 1\n\t"
    "v_permlane16_swap_b32 v239, v240\n\t"
    "v_max_f32 v239, v239, v240\n\t"
    "v_mov_b32 v240, v239\n\t"
    "s_nop 1\n\t"
    "v_permlane32_swap_b32 v239, v240\n\t"
    "v_max_f32 v239, v239, v240\n\t"
    "v_max_f32 v239, 0, v239\n\t"
    "v_exp_f32 v242, -v239\n\t"
    "v_sub_f32 v168, v168, v239\n\t"
    "v_sub_f32 v169, v169, v239\n\t"
    "v_sub_f32 v170, v170, v239\n\t"
    "v_sub_f32 v171, v171, v239\n\t"
    "v_sub_f32 v172, v172, v239\n\t"
    "v_sub_f32 v173, v173, v239\n\t"
    "v_sub_f32 v174, v174, v239\n\t"
    "v_sub_f32 v175, v175, v239\n\t"
    "v_sub_f32 v192, v192, v239\n\t"
    "v_sub_f32 v193, v193, v239\n\t"
    "v_sub_f32 v194, v194, v239\n\t"
    "v_sub_f32 v195, v195, v239\n\t"
    "v_mul_f32 v236, v236, v242\n\t"
    "v_max3_f32 v239, v176, v177, v178\n\t"
    "v_max3_f32 v239, v239, v179, v180\n\t"
    "v_max3_f32 v239, v239, v181, v182\n\t"
    "v_max_f32 v239, v239, v183\n\t"
    "v_mov_b32 v240, v239\n\t"
    "s_nop 1\n\t"
    "v_permlane16_swap_b32 v239, v240\n\t"
    "v_max_f32 v239, v239, v240\n\t"
    "v_mov_b32 v240, v239\n\t"
    "s_nop 1\n\t"
    "v_permlane32_swap_b32 v239, v240\n\t"
    "v_max_f32 v239, v239, v240\n\t"
    "v_max_f32 v239, 0, v239\n\t"
    "v_exp_f32 v243, -v239\n\t"
    "v_sub_f32 v176, v176, v239\n\t"
    "v_sub_f32 v177, v177, v239\n\t"
    "v_sub_f32 v178, v178, v239\n\t"
    "v_sub_f32 v179, v179, v239\n\t"
    "v_sub_f32 v180, v180, v239\n\t"
    "v_sub_f32 v181, v181, v239\n\t"
    "v_sub_f32 v182, v182, v239\n\t"
    "v_sub_f32 v183, v183, v239\n\t"
    "v_sub_f32 v196, v196, v239\n\t"
    "v_sub_f32 v197, v197, v239\n\t"
    "v_sub_f32 v198, v198, v239\n\t"
    "v_sub_f32 v199, v199, v239\n\t"
    "v_mul_f32 v237, v237, v243\n\t"
    "v_max3_f32 v239, v184, v185, v186\n\t"
    "v_max3_f32 v239, v239, v187, v188\n\t"
    "v_max3_f32 v239, v239, v189, v190\n\t"
    "v_max_f32 v239, v239, v191\n\t"
    "v_mov_b32 v240, v239\n\t"
    "s_nop 1\n\t"
    "v_permlane16_swap_b32 v239, v240\n\t"
    "v_max_f32 v239, v239, v240\n\t"
    "v_mov_b32 v240, v239\n\t"
    "s_nop 1\n\t"
    "v_permlane32_swap_b32 v239, v240\n\t"
    "v_max_f32 v239, v239, v240\n\t"
    "v_max_f32 v239, 0, v239\n\t"
    "v_exp_f32 v244, -v239\n\t"
    "v_sub_f32 v184, v184, v239\n\t"
    "v_sub_f32 v185, v185, v239\n\t"
    "v_sub_f32 v186, v186, v239\n\t"
    "v_sub_f32 v187, v187, v239\n\t"
    "v_sub_f32 v188, v188, v239\n\t"
    "v_sub_f32 v189, v189, v239\n\t"
    "v_sub_f32 v190, v190, v239\n\t"
    "v_sub_f32 v191, v191, v239\n\t"
    "v_sub_f32 v200, v200, v239\n\t"
    "v_sub_f32 v201, v201, v239\n\t"
    "v_sub_f32 v202, v202, v239\n\t"
    "v_sub_f32 v203, v203, v239\n\t"
    "v_mul_f32 v238, v238, v244\n\t"
    "s_mov_b32 s78, 1\n\t"
    "s_nop 1\n\t"
    "s_branch L_rarp15_%=\n\t"
    "L_rbp15_%=:\n\t"
    "s_nop 8\n\t"
    "v_mul_f32 v0, v0, v242\n\t"
    "v_mul_f32 v1, v1, v242\n\t"
    "v_mul_f32 v2, v2, v242\n\t"
    "v_mul_f32 v3, v3, v242\n\t"
    "v_mul_f32 v4, v4, v243\n\t"
    "v_mul_f32 v5, v5, v243\n\t"
    "v_mul_f32 v6, v6, v243\n\t"
    "v_mul_f32 v7, v7, v243\n\t"
    "v_mul_f32 v8, v8, v244\n\t"
    "v_mul_f32 v9, v9, v244\n\t"
    "v_mul_f32 v10, v10, v244\n\t"
    "v_mul_f32 v11, v11, v244\n\t"
    "v_mul_f32 v12, v12, v242\n\t"
    "v_mul_f32 v13, v13, v242\n\t"
    "v_mul_f32 v14, v14, v242\n\t"
    "v_mul_f32 v15, v15, v242\n\t"
    "v_mul_f32 v16, v16, v243\n\t"
    "v_mul_f32 v17, v17, v243\n\t"
    "v_mul_f32 v18, v18, v243\n\t"
    "v_mul_f32 v19, v19, v243\n\t"
    "v_mul_f32 v20, v20, v244\n\t"
    "v_mul_f32 v21, v21, v244\n\t"
    "v_mul_f32 v22, v22, v244\n\t"
    "v_mul_f32 v23, v23, v244\n\t"
    "v_mul_f32 v24, v24, v242\n\t"
    "v_mul_f32 v25, v25, v242\n\t"
    "v_mul_f32 v26, v26, v242\n\t"
    "v_mul_f32 v27, v27, v242\n\t"
    "v_mul_f32 v28, v28, v243\n\t"
    "v_mul_f32 v29, v29, v243\n\t"
    "v_mul_f32 v30, v30, v243\n\t"
    "v_mul_f32 v31, v31, v243\n\t"
    "v_mul_f32 v32, v32, v244\n\t"
    "v_mul_f32 v33, v33, v244\n\t"
    "v_mul_f32 v34, v34, v244\n\t"
    "v_mul_f32 v35, v35, v244\n\t"
    "v_mul_f32 v36, v36, v242\n\t"
    "v_mul_f32 v37, v37, v242\n\t"
    "v_mul_f32 v38, v38, v242\n\t"
    "v_mul_f32 v39, v39, v242\n\t"
    "v_mul_f32 v40, v40, v243\n\t"
    "v_mul_f32 v41, v41, v243\n\t"
    "v_mul_f32 v42, v42, v243\n\t"
    "v_mul_f32 v43, v43, v243\n\t"
    "v_mul_f32 v44, v44, v244\n\t"
    "v_mul_f32 v45, v45, v244\n\t"
    "v_mul_f32 v46, v46, v244\n\t"
    "v_mul_f32 v47, v47, v244\n\t"
    "v_mul_f32 v48, v48, v242\n\t"
    "v_mul_f32 v49, v49, v242\n\t"
    "v_mul_f32 v50, v50, v242\n\t"
    "v_mul_f32 v51, v51, v242\n\t"
    "v_mul_f32 v52, v52, v243\n\t"
    "v_mul_f32 v53, v53, v243\n\t"
    "v_mul_f32 v54, v54, v243\n\t"
    "v_mul_f32 v55, v55, v243\n\t"
    "v_mul_f32 v56, v56, v244\n\t"
    "v_mul_f32 v57, v57, v244\n\t"
    "v_mul_f32 v58, v58, v244\n\t"
    "v_mul_f32 v59, v59, v244\n\t"
    "v_mul_f32 v60, v60, v242\n\t"
    "v_mul_f32 v61, v61, v242\n\t"
    "v_mul_f32 v62, v62, v242\n\t"
    "v_mul_f32 v63, v63, v242\n\t"
    "v_mul_f32 v64, v64, v243\n\t"
    "v_mul_f32 v65, v65, v243\n\t"
    "v_mul_f32 v66, v66, v243\n\t"
    "v_mul_f32 v67, v67, v243\n\t"
    "v_mul_f32 v68, v68, v244\n\t"
    "v_mul_f32 v69, v69, v244\n\t"
    "v_mul_f32 v70, v70, v244\n\t"
    "v_mul_f32 v71, v71, v244\n\t"
    "v_mul_f32 v72, v72, v242\n\t"
    "v_mul_f32 v73, v73, v242\n\t"
    "v_mul_f32 v74, v74, v242\n\t"
    "v_mul_f32 v75, v75, v242\n\t"
    "v_mul_f32 v76, v76, v243\n\t"
    "v_mul_f32 v77, v77, v243\n\t"
    "v_mul_f32 v78, v78, v243\n\t"
    "v_mul_f32 v79, v79, v243\n\t"
    "v_mul_f32 v80, v80, v244\n\t"
    "v_mul_f32 v81, v81, v244\n\t"
    "v_mul_f32 v82, v82, v244\n\t"
    "v_mul_f32 v83, v83, v244\n\t"
    "v_mul_f32 v84, v84, v242\n\t"
    "v_mul_f32 v85, v85, v242\n\t"
    "v_mul_f32 v86, v86, v242\n\t"
    "v_mul_f32 v87, v87, v242\n\t"
    "v_mul_f32 v88, v88, v243\n\t"
    "v_mul_f32 v89, v89, v243\n\t"
    "v_mul_f32 v90, v90, v243\n\t"
    "v_mul_f32 v91, v91, v243\n\t"
    "v_mul_f32 v92, v92, v244\n\t"
    "v_mul_f32 v93, v93, v244\n\t"
    "v_mul_f32 v94, v94, v244\n\t"
    "v_mul_f32 v95, v95, v244\n\t"
    "s_mov_b32 s78, 0\n\t"
    "s_nop 1\n\t"
    "s_branch L_rbrp15_%=\n\t"
    "L_end_%=:\n\t"
    :: "s"(ka), "s"(bid), "s"(w), "s"(lb)
    : "v0", "v1", "v2", "v3", "v4", "v5", "v6", "v7", "v8", "v9", "v10", "v11", "v12", "v13", "v14", "v15", "v16", "v17", "v18", "v19", "v20", "v21", "v22", "v23", "v24", "v25", "v26", "v27", "v28", "v29", "v30", "v31", "v32", "v33", "v34", "v35", "v36", "v37", "v38", "v39", "v40", "v41", "v42", "v43", "v44", "v45", "v46", "v47", "v48", "v49", "v50", "v51", "v52", "v53", "v54", "v55", "v56", "v57", "v58", "v59", "v60", "v61", "v62", "v63", "v64", "v65", "v66", "v67", "v68", "v69", "v70", "v71", "v72", "v73", "v74", "v75", "v76", "v77", "v78", "v79", "v80", "v81", "v82", "v83", "v84", "v85", "v86", "v87", "v88", "v89", "v90", "v91", "v92", "v93", "v94", "v95", "v96", "v97", "v98", "v99", "v100", "v101", "v102", "v103", "v104", "v105", "v106", "v107", "v108", "v109", "v110", "v111", "v112", "v113", "v114", "v115", "v116", "v117", "v118", "v119", "v120", "v121", "v122", "v123", "v124", "v125", "v126", "v127", "v128", "v129", "v130", "v131", "v132", "v133", "v134", "v135", "v136", "v137", "v138", "v139", "v140", "v141", "v142", "v143", "v144", "v145", "v146", "v147", "v148", "v149", "v150", "v151", "v152", "v153", "v154", "v155", "v156", "v157", "v158", "v159", "v160", "v161", "v162", "v163", "v164", "v165", "v166", "v167", "v168", "v169", "v170", "v171", "v172", "v173", "v174", "v175", "v176", "v177", "v178", "v179", "v180", "v181", "v182", "v183", "v184", "v185", "v186", "v187", "v188", "v189", "v190", "v191", "v192", "v193", "v194", "v195", "v196", "v197", "v198", "v199", "v200", "v201", "v202", "v203", "v204", "v205", "v206", "v207", "v208", "v209", "v210", "v211", "v212", "v213", "v214", "v215", "v216", "v217", "v218", "v219", "v220", "v221", "v222", "v223", "v224", "v225", "v226", "v227", "v228", "v229", "v230", "v231", "v232", "v233", "v234", "v235", "v236", "v237", "v238", "v239", "v240", "v241", "v242", "v243", "v244", "v245", "v246", "v247", "v248", "v249", "v250", "v251", "v252", "v253", "v254", "v255", "s12", "s13", "s14", "s15", "s16", "s17", "s18", "s19", "s20", "s21", "s22", "s23", "s24", "s25", "s26", "s27", "s28", "s29", "s30", "s31", "s34", "s35", "s36", "s37", "s38", "s39", "s40", "s41", "s42", "s43", "s44", "s45", "s46", "s47", "s48", "s49", "s50", "s51", "s52", "s53", "s54", "s55", "s56", "s57", "s58", "s59", "s60", "s61", "s62", "s63", "s64", "s65", "s66", "s67", "s68", "s69", "s70", "s71", "s72", "s73", "s74", "s75", "s76", "s77", "s78", "s79", "s80", "s81", "s82", "s83", "s84", "s85", "s86", "s87", "s88", "s89", "s90", "s91", "s92", "s93", "s94", "s95", "s96", "s97", "s98", "s99", "vcc", "scc", "memory");
}

extern "C" void kernel_launch(void* const* d_in, const int* in_sizes, int n_in, void* d_out, int out_size, void* d_ws, size_t ws_size, hipStream_t stream) {
  Ptrs P;
  for (int br = 0; br < 2; ++br) { P.x[br] = d_in[5 * br + 0]; P.k[br] = d_in[5 * br + 1]; P.v[br] = d_in[5 * br + 2]; P.wq[br] = d_in[5 * br + 3]; P.wo[br] = d_in[5 * br + 4]; }
  P.ws = d_ws; P.out = d_out;
  (void)hipFuncSetAttribute((const void*)kf_attn, hipFuncAttributeMaxDynamicSharedMemorySize, LDS1);
  hipLaunchKernelGGL(kf_attn, dim3(256), dim3(512), LDS1, stream, P);
}
```
